```python
import torch

def _softmax(x, axis=-1):
    return torch.softmax(x, dim=axis)
def _gelu(x):
    return torch.nn.functional.gelu(x, approximate="tanh")
def _silu(x):
    return torch.nn.functional.silu(x)
def _sigmoid(x):
    return torch.sigmoid(x)
def _layernorm(x, axis=-1, eps=1e-5):
    m = x.mean(dim=axis, keepdim=True)
    v = x.var(dim=axis, keepdim=True, unbiased=False)
    return (x - m) / torch.sqrt(v + eps)
def _scatter_add(tgt, idx, src, axis=0):
    return tgt.clone().index_add_(axis, idx, src)
def _segment_sum(src, seg, n):
    out = torch.zeros((n,) + src.shape[1:], dtype=src.dtype)
    return out.index_add_(0, seg, src)
def _pad(x, pw):
    flat = tuple(v for pair in reversed(pw) for v in pair)
    return torch.nn.functional.pad(x, flat)

kernel_name = 'interleave_layers__mha_block__dense_activation_gelu__prompt'

def setup_inputs():

    _w = (lambda a: (lambda t: t.to(torch.bfloat16) if t.is_floating_point() else t)(torch.as_tensor(a)))
    def _A_setup():
        B, S, D, H = 1, 2048, 448, 16
        return {
            "x": _w((lambda *s: torch.randn(*s, dtype=torch.bfloat16) * 0.05)(B, S, D)),
            "wqkv": _w((lambda *s: torch.randn(*s, dtype=torch.bfloat16) * 0.05)(D, 3 * D)),
            "wo": _w((lambda *s: torch.randn(*s, dtype=torch.bfloat16) * 0.05)(D, D)),
        }
    _A_in = _A_setup()

    def _B_setup():
        B, S, D, F = 1, 2048, 448, 1792
        return {
            "x": _w((lambda *s: torch.randn(*s, dtype=torch.bfloat16) * 0.05)(B, S, D)),
            "w1": _w((lambda *s: torch.randn(*s, dtype=torch.bfloat16) * 0.8)(D, F) * (D ** -0.5)),
            "b1": _w((lambda *s: torch.randn(*s, dtype=torch.bfloat16) * 12.8)(F) * 0.01),
            "w2": _w((lambda *s: torch.randn(*s, dtype=torch.bfloat16) * 0.2)(F, D) * (F ** -0.5)),
            "b2": _w((lambda *s: torch.randn(*s, dtype=torch.bfloat16) * 1.6)(D) * 0.01),
        }
    _B_in = _B_setup()

    return {
        "A_x": _A_in["x"],
        "A_wqkv": _A_in["wqkv"],
        "A_wo": _A_in["wo"],
        "B_x": _B_in["x"],
        "B_w1": _B_in["w1"],
        "B_b1": _B_in["b1"],
        "B_w2": _B_in["w2"],
        "B_b2": _B_in["b2"],
    }

def reference(A_x, A_wqkv, A_wo, B_x, B_w1, B_b1, B_w2, B_b2):
    def _A_ref(x, wqkv, wo):
        B, S, D = x.shape
        H = 16
        Dh = D // H
        qkv = (lambda x, *d: x.permute(*d))((x @ wqkv).reshape(B, S, 3, H, Dh), 2, 0, 3, 1, 4)
        q, k, v = qkv[0], qkv[1], qkv[2]
        scores = torch.einsum("bhqd,bhkd->bhqk", q, k) * (Dh ** -0.5)
        p = _softmax(scores, axis=-1)
        o = torch.einsum("bhqk,bhkd->bhqd", p, v)
        return (lambda x, *d: x.permute(*d))(o, 0, 2, 1, 3).reshape(B, S, D) @ wo

    def _B_ref(x, w1, b1, w2, b2):
        h = torch.einsum("bsd,df->bsf", x, w1) + b1
        h = _gelu(h)
        return torch.einsum("bsf,fd->bsd", h, w2) + b2

    x = A_x
    x = x + _A_ref(_layernorm(x, axis=-1), A_wqkv, A_wo)
    x = x + _B_ref(_layernorm(x, axis=-1), B_w1, B_b1, B_w2, B_b2)
    x = x + _A_ref(_layernorm(x, axis=-1), A_wqkv, A_wo)
    x = x + _B_ref(_layernorm(x, axis=-1), B_w1, B_b1, B_w2, B_b2)
    return x
```

```cpp
#include <hip/hip_runtime.h>
#include <hip/hip_bf16.h>
#include <stdint.h>

typedef __bf16 bf16x2 __attribute__((ext_vector_type(2)));
typedef float f32x16 __attribute__((ext_vector_type(16)));
typedef float f32x4 __attribute__((ext_vector_type(4)));
typedef float f32x2 __attribute__((ext_vector_type(2)));
typedef unsigned u32x2 __attribute__((ext_vector_type(2)));
typedef unsigned u32x4 __attribute__((ext_vector_type(4)));
typedef unsigned u32x8 __attribute__((ext_vector_type(8)));
typedef unsigned u32x16 __attribute__((ext_vector_type(16)));
typedef unsigned u32x32 __attribute__((ext_vector_type(32)));
typedef short s16x4 __attribute__((ext_vector_type(4)));
typedef unsigned long long u64;
#define LAS __attribute__((address_space(3)))
typedef __amdgpu_buffer_rsrc_t rsrc_t;
#define DI __device__ __forceinline__
#define ACC_FILE "a0", "a1", "a2", "a3", "a4", "a5", "a6", "a7", "a8", "a9", "a10", "a11", "a12", "a13", "a14", "a15", "a16", "a17", "a18", "a19", "a20", "a21", "a22", "a23", "a24", "a25", "a26", "a27", "a28", "a29", "a30", "a31", "a32", "a33", "a34", "a35", "a36", "a37", "a38", "a39", "a40", "a41", "a42", "a43", "a44", "a45", "a46", "a47", "a48", "a49", "a50", "a51", "a52", "a53", "a54", "a55", "a56", "a57", "a58", "a59", "a60", "a61", "a62", "a63", "a64", "a65", "a66", "a67", "a68", "a69", "a70", "a71", "a72", "a73", "a74", "a75", "a76", "a77", "a78", "a79", "a80", "a81", "a82", "a83", "a84", "a85", "a86", "a87", "a88", "a89", "a90", "a91", "a92", "a93", "a94", "a95", "a96", "a97", "a98", "a99", "a100", "a101", "a102", "a103", "a104", "a105", "a106", "a107", "a108", "a109", "a110", "a111", "a112", "a113", "a114", "a115", "a116", "a117", "a118", "a119", "a120", "a121", "a122", "a123", "a124", "a125", "a126", "a127", "a128", "a129", "a130", "a131", "a132", "a133", "a134", "a135", "a136", "a137", "a138", "a139", "a140", "a141", "a142", "a143", "a144", "a145", "a146", "a147", "a148", "a149", "a150", "a151", "a152", "a153", "a154", "a155", "a156", "a157", "a158", "a159", "a160", "a161", "a162", "a163", "a164", "a165", "a166", "a167", "a168", "a169", "a170", "a171", "a172", "a173", "a174", "a175", "a176", "a177", "a178", "a179", "a180", "a181", "a182", "a183", "a184", "a185", "a186", "a187", "a188", "a189", "a190", "a191", "a192", "a193", "a194", "a195", "a196", "a197", "a198", "a199", "a200", "a201", "a202", "a203", "a204", "a205", "a206", "a207", "a208", "a209", "a210", "a211", "a212", "a213", "a214", "a215", "a216", "a217", "a218", "a219", "a220", "a221", "a222", "a223", "a224", "a225", "a226", "a227", "a228", "a229", "a230", "a231", "a232", "a233", "a234", "a235", "a236", "a237", "a238", "a239", "a240", "a241", "a242", "a243", "a244", "a245", "a246", "a247", "a248", "a249", "a250", "a251", "a252", "a253", "a254", "a255"

constexpr int SEQ = 2048, DM = 448, DFF = 1792;
constexpr int SC1 = 16;
constexpr int FS = 128;
constexpr int PDELAY = 8;
constexpr unsigned CODE_BYTES = 43008;
constexpr unsigned OOB = 0x40000000u;
constexpr unsigned WS_FLAGS = 4096;
constexpr unsigned WS_W2F = 524288;
constexpr unsigned WS_W1F = WS_W2F + 1568 * 1024;
constexpr unsigned WS_WQF = WS_W1F + 1568 * 1024;
constexpr unsigned WS_WOF = WS_WQF + 1344 * 1024;
constexpr unsigned WS_WEQ = WS_WOF + 448 * 1024;
constexpr unsigned WS_L0 = 6u << 20, LSTRIDE = 20u << 20;
constexpr unsigned O_QF = 0;
constexpr unsigned O_KVF = 2u << 20;
constexpr unsigned O_OF = 6u << 20;
constexpr unsigned O_X2F = 8u << 20;
constexpr unsigned O_HF = 10u << 20;
constexpr unsigned O_XF = 17u << 20;
constexpr unsigned O_ST2 = 19u << 20;
constexpr unsigned O_ST = O_ST2 + 262144;
constexpr unsigned WS_END = WS_L0 + 2 * LSTRIDE;
constexpr unsigned L_W1 = 0;
constexpr unsigned L_WQKV = 0;
constexpr unsigned WREG = 40960;
constexpr unsigned L_STG = 21504, L_XIM = 28672;
constexpr unsigned L_CS = 36864;
constexpr unsigned L_MISC0 = 38912;
constexpr unsigned L_MISC = 88064;
constexpr unsigned L_SCR = 114688;
constexpr unsigned L_WO = 114688;
constexpr unsigned L_EX1 = 147456;
constexpr unsigned L_TOTAL = 163840;
constexpr unsigned TILEB = 448 * 64;

struct Params {
  const void* x; const void* wqkv; const void* wo; const void* w1; const void* b1; const void* w2; const void* b2;
  void* out; unsigned char* ws;
};

DI unsigned U(unsigned x) { return __builtin_amdgcn_readfirstlane(x); }
DI unsigned pk2(float lo, float hi) { f32x2 v = {lo, hi}; return __builtin_bit_cast(unsigned, __builtin_convertvector(v, bf16x2)); }
DI float blo(unsigned u) { return __uint_as_float(u << 16); }
DI float xhalf_max(float x) { auto r = __builtin_amdgcn_permlane32_swap(__float_as_uint(x), __float_as_uint(x), false, false); return fmaxf(__uint_as_float(r[0]), __uint_as_float(r[1])); }
DI u32x4 ldx(rsrc_t rs, unsigned voff, unsigned soff) { return __builtin_amdgcn_raw_buffer_load_b128(rs, voff, soff, SC1); }
DI void stx(u32x4 v, rsrc_t rs, unsigned voff, unsigned soff) { __builtin_amdgcn_raw_buffer_store_b128(v, rs, voff, soff, SC1); }
DI u32x4 trfrag(unsigned a) {
  s16x4 lo = __builtin_amdgcn_ds_read_tr16_b64_v4i16((LAS s16x4*)a);
  s16x4 hi = __builtin_amdgcn_ds_read_tr16_b64_v4i16((LAS s16x4*)(a + 512));
  u32x2 l = __builtin_bit_cast(u32x2, lo), h = __builtin_bit_cast(u32x2, hi);
  return u32x4{l[0], l[1], h[0], h[1]};
}
DI void dma4(unsigned ldsaddr, unsigned voff, rsrc_t rs, unsigned soff) {
  asm volatile("s_mov_b32 m0, %0\n\ts_nop 0\n\tbuffer_load_dwordx4 %1, %2, %3 offen sc1 lds\n\tbuffer_load_dwordx4 %1, %2, %3 offen offset:1024 sc1 lds\n\t"
               "buffer_load_dwordx4 %1, %2, %3 offen offset:2048 sc1 lds\n\tbuffer_load_dwordx4 %1, %2, %3 offen offset:3072 sc1 lds"
               :: "s"(U(ldsaddr)), "v"(voff), "s"(rs), "s"(U(soff)) : "m0", "memory");
}
DI void bar() { asm volatile("s_waitcnt lgkmcnt(0)\n\ts_barrier" ::: "memory"); }
DI void drain() { asm volatile("s_waitcnt vmcnt(0)" ::: "memory"); }
struct Ctx {
  rsrc_t ws, rx;
  unsigned lane, lane16, w, half, l31, lds0;
  unsigned r, c;
  u64 epoch;
};
DI void refresh(Ctx& c) {
  c.w = U(c.w); c.r = U(c.r); c.c = U(c.c); c.lds0 = U(c.lds0); c.epoch = ((u64)U((unsigned)(c.epoch >> 32)) << 32) | U((unsigned)c.epoch);
  asm volatile("" : "+s"(c.w), "+s"(c.r), "+s"(c.c), "+s"(c.lds0), "+s"(c.epoch));
  asm volatile("" : "+v"(c.lane));
  c.lane16 = c.lane * 16; c.half = c.lane >> 5; c.l31 = c.lane & 31;
}
DI unsigned panel0(unsigned g) { return (g & 6) * 2 + ((g >> 3) & 3); }
DI unsigned head0(unsigned g) { return (g & 1) * 8 + (((g & 1 ? 0x76420531u : 0x75310642u) >> (4 * (g >> 5))) & 7); }
DI u64 tag_of(const Ctx& c, int phase) { return (c.epoch + 1 + phase) << 16; }
DI unsigned line_of(int phase, unsigned group) { return WS_FLAGS + (phase * 16 + group) * 16 * FS; }
DI void st_flag(const Ctx& c, unsigned wave, unsigned off, u64 v) {
  __builtin_amdgcn_raw_buffer_store_b64(u32x2{(unsigned)v, (unsigned)(v >> 32)}, c.ws, c.lane ? OOB : 0u, U(c.w == wave ? off : OOB), SC1);
}
template <int DELAY = PDELAY> DI void poll(const Ctx& c, u32x32& warm, unsigned voff, bool active, u64 tag) {
  const unsigned tl = (unsigned)tag, th = (unsigned)(tag >> 32), am = active ? ~0u : 0u;
  unsigned n, ta, tb;
  if (DELAY) __builtin_amdgcn_s_sleep(DELAY);
    asm volatile(
      "s_mov_b32 %[n], 0x100000\n\t"
      "buffer_load_dwordx2 v[248:249], %[fv], %[ws], 0 offen sc1\n\t"
      "1:\n\t"
      "s_waitcnt vmcnt(0)\n\t"
      "v_xor_b32 %[a], %[tl], v248\n\t"
      "v_xor_b32 %[b], %[th], v249\n\t"
      "v_lshrrev_b32 %[a], 16, %[a]\n\t"
      "v_or_b32 %[a], %[a], %[b]\n\t"
      "v_and_b32 %[a], %[am], %[a]\n\t"
      "v_cmp_ne_u32 vcc, 0, %[a]\n\t"
      "s_cbranch_vccz 2f\n\t"
      "buffer_load_dwordx2 v[248:249], %[fv], %[ws], 0 offen sc1\n\t"
      "s_sub_u32 %[n], %[n], 1\n\t"
      "s_cmp_lg_u32 %[n], 0\n\t"
      "s_cbranch_scc1 1b\n\t"
      "2:\n\t"
      : [n] "=&s"(n), [a] "=&v"(ta), [b] "=&v"(tb), "+{v[224:255]}"(warm)
      : [fv] "v"(voff), [ws] "s"(c.ws), [tl] "s"(tl), [th] "s"(th), [am] "v"(am)
      : "vcc", "scc", "memory", ACC_FILE);
}
DI unsigned code8(float x) { const int u = (int)((__float_as_uint(__builtin_amdgcn_sqrtf(x) * 1.015625f) + 0xfffffu) >> 20) - 920; return u < 0 ? 0 : u > 255 ? 255 : u; }
DI void publish_kv(const Ctx& c, int phase, unsigned head, unsigned panel, unsigned misc) {
  bar();
  const f32x4 a = *(LAS f32x4*)(c.lds0 + misc + 512), b = *(LAS f32x4*)(c.lds0 + misc + 528);
  const u64 t = tag_of(c, phase);
  u32x4 v = {(unsigned)t | code8(fmaxf(fmaxf(a[0], a[2]), fmaxf(b[0], b[2]))) << 8 | code8(fmaxf(fmaxf(a[1], a[3]), fmaxf(b[1], b[3]))), (unsigned)(t >> 32), (unsigned)t, (unsigned)(t >> 32)};
  unsigned vo = c.lane < 8 ? c.lane16 : OOB, so = U(c.w == 0 ? line_of(phase, head) + panel * FS : OOB);
  asm volatile("" : "+v"(v), "+v"(vo));
  asm volatile("" : "+s"(so));
  drain();
  bar();
  __builtin_amdgcn_raw_buffer_store_b128(v, c.ws, vo, so, SC1);
}
DI void write_wext(const Ctx& c, unsigned ldsoff, float cs, unsigned bias_bits) {
  const unsigned ch = pk2(cs, 0.f) & 0xffffu, cl = pk2(cs - blo(ch), 0.f) & 0xffffu, d = c.half ? 0u : ch | (bias_bits << 16);
  unsigned z; asm volatile("v_mov_b32 %0, 0" : "=v"(z));
  *(LAS u32x4*)(c.lds0 + ldsoff + c.lane16) = u32x4{d, d, c.half ? 0u : cl, z};
}
#define QKV_ARGS(lb, head, panel, misc) \
    const unsigned rtw = c.lds0 + (misc) + c.w * 128 + c.l31 * 4, rtr = c.lds0 + (misc) + c.w * 128 + c.half * 16, kmw = c.lds0 + (misc) + 512 + c.w * 8; \
    const unsigned kl = ((c.l31 & 0x13u) | ((c.l31 & 4u) << 1) | ((c.l31 & 8u) >> 1) | (c.half << 5)) * 16;     \
    const unsigned qo = U((lb) + O_QF + (((head) * 16 + (panel)) * 4 + c.w) * 2048), ko = U((lb) + O_KVF + ((head) * 64 + (panel) * 4 + c.w) * 4096);
#define RED_ARGS \
    const unsigned sb = c.lds0 + L_SCR + c.lane16; \
    const unsigned wr0 = sb + ((c.w ^ 1) * 3 + 0) * 4096, wr1 = sb + ((c.w ^ 2) * 3 + 1) * 4096, wr2 = sb + ((c.w ^ 3) * 3 + 2) * 4096, rd = sb + c.w * 3 * 4096;

__global__ void __launch_bounds__(256, 1) fused_kernel(Params p) {
  extern __shared__ __attribute__((aligned(16))) char smem[];
  unsigned pfc;
  {
    const unsigned char* pcb; asm volatile("s_getpc_b64 %0" : "=s"(pcb));
    asm volatile("buffer_load_dword %0, %1, %2, %3 offen" : "=v"(pfc) : "v"((threadIdx.x & 63) < 4 ? (threadIdx.x >> 6) * 512 + (threadIdx.x & 3) * 128 : OOB),
                 "s"(__builtin_amdgcn_make_buffer_rsrc((void*)pcb, 0, CODE_BYTES, 0x00020000)), "s"((blockIdx.x >> 3) * 2048) : "memory");
  }
  Ctx c;
  c.lane = threadIdx.x & 63; c.lane16 = c.lane * 16; c.w = __builtin_amdgcn_readfirstlane(threadIdx.x >> 6);
  c.half = c.lane >> 5; c.l31 = c.lane & 31;
  c.lds0 = (unsigned)(size_t)(LAS char*)smem;
  {
    const unsigned g = blockIdx.x, xcd = g & 7, ii = g >> 3;
    c.r = 2 * xcd + (ii & 1); c.c = ii >> 1;
  }
  c.ws = __builtin_amdgcn_make_buffer_rsrc(p.ws, 0, WS_END, 0x00020000);
  c.rx = __builtin_amdgcn_make_buffer_rsrc((void*)p.x, 0, SEQ * DM * 2, 0x00020000);
  const bool act = c.c < 14;
  u32x32 zo0, zo1;
  u32x32 warm;
  {
    const unsigned p0 = panel0(blockIdx.x), h0 = head0(blockIdx.x);
    c.epoch = *(const __attribute__((address_space(4))) u64*)p.ws;
    const unsigned reg = c.lds0 + c.w * WREG;
    const unsigned rq = c.lane >> 2, pc = (c.lane & 3) * 16;
    const rsrc_t rqkv = __builtin_amdgcn_make_buffer_rsrc((void*)p.wqkv, 0, DM * 3 * DM * 2, 0x00020000);
    const unsigned wsrc = h0 * 56 + c.w * 112 * (3 * DM * 2);
    const unsigned wv = rq * (3 * DM * 2) + pc;
    u32x32 xr0, xr1, xr2; u32x16 xr3;
    {
      const unsigned xv = (c.lane >> 3) * (DM * 2) + (c.lane & 7) * 16, xs = U((p0 * 128 + c.w * 32) * (DM * 2)), km = U(reg);
      unsigned ks = U(wsrc + DM * 2), n, s1, s2, s3;
    asm volatile(
      "s_mov_b32 m0, %[km]\n\t"
      "s_mov_b32 %[n], 7\n\t"
      "2:\n\t"
      "buffer_load_dwordx4 %[wv], %[rw], %[ks] offen lds\n\t"
      "s_add_u32 m0, m0, 0x400\n\t"
      "s_add_u32 %[ks], %[ks], 0xa800\n\t"
      "s_sub_u32 %[n], %[n], 1\n\t"
      "s_cmp_lg_u32 %[n], 0\n\t"
      "s_cbranch_scc1 2b\n\t"
      "s_add_u32 %[s1], %[s0], 7168\n\t"
      "s_add_u32 %[s2], %[s0], 14336\n\t"
      "s_add_u32 %[s3], %[s0], 21504\n\t"
      "buffer_load_dwordx4 v[144:147], %[xv], %[rx], %[s0] offen offset:0\n\t"
      "buffer_load_dwordx4 v[148:151], %[xv], %[rx], %[s1] offen offset:0\n\t"
      "buffer_load_dwordx4 v[152:155], %[xv], %[rx], %[s2] offen offset:0\n\t"
      "buffer_load_dwordx4 v[156:159], %[xv], %[rx], %[s3] offen offset:0\n\t"
      "buffer_load_dwordx4 v[160:163], %[xv], %[rx], %[s0] offen offset:128\n\t"
      "buffer_load_dwordx4 v[164:167], %[xv], %[rx], %[s1] offen offset:128\n\t"
      "buffer_load_dwordx4 v[168:171], %[xv], %[rx], %[s2] offen offset:128\n\t"
      "buffer_load_dwordx4 v[172:175], %[xv], %[rx], %[s3] offen offset:128\n\t"
      "buffer_load_dwordx4 v[176:179], %[xv], %[rx], %[s0] offen offset:256\n\t"
      "buffer_load_dwordx4 v[180:183], %[xv], %[rx], %[s1] offen offset:256\n\t"
      "buffer_load_dwordx4 v[184:187], %[xv], %[rx], %[s2] offen offset:256\n\t"
      "buffer_load_dwordx4 v[188:191], %[xv], %[rx], %[s3] offen offset:256\n\t"
      "buffer_load_dwordx4 v[192:195], %[xv], %[rx], %[s0] offen offset:384\n\t"
      "buffer_load_dwordx4 v[196:199], %[xv], %[rx], %[s1] offen offset:384\n\t"
      "buffer_load_dwordx4 v[200:203], %[xv], %[rx], %[s2] offen offset:384\n\t"
      "buffer_load_dwordx4 v[204:207], %[xv], %[rx], %[s3] offen offset:384\n\t"
      "buffer_load_dwordx4 v[208:211], %[xv], %[rx], %[s0] offen offset:512\n\t"
      "buffer_load_dwordx4 v[212:215], %[xv], %[rx], %[s1] offen offset:512\n\t"
      "buffer_load_dwordx4 v[216:219], %[xv], %[rx], %[s2] offen offset:512\n\t"
      "buffer_load_dwordx4 v[220:223], %[xv], %[rx], %[s3] offen offset:512\n\t"
      "buffer_load_dwordx4 v[224:227], %[xv], %[rx], %[s0] offen offset:640\n\t"
      "buffer_load_dwordx4 v[228:231], %[xv], %[rx], %[s1] offen offset:640\n\t"
      "buffer_load_dwordx4 v[232:235], %[xv], %[rx], %[s2] offen offset:640\n\t"
      "buffer_load_dwordx4 v[236:239], %[xv], %[rx], %[s3] offen offset:640\n\t"
      "buffer_load_dwordx4 v[240:243], %[xv], %[rx], %[s0] offen offset:768\n\t"
      "buffer_load_dwordx4 v[244:247], %[xv], %[rx], %[s1] offen offset:768\n\t"
      "buffer_load_dwordx4 v[248:251], %[xv], %[rx], %[s2] offen offset:768\n\t"
      "buffer_load_dwordx4 v[252:255], %[xv], %[rx], %[s3] offen offset:768\n\t"
      : [ks] "+s"(ks), [n] "=&s"(n), [s1] "=&s"(s1), [s2] "=&s"(s2), [s3] "=&s"(s3), "=&{v[144:175]}"(xr0), "=&{v[176:207]}"(xr1), "=&{v[208:239]}"(xr2), "=&{v[240:255]}"(xr3)
      : [km] "s"(km), [s0] "s"(xs), [xv] "v"(xv), [wv] "v"(wv), [rx] "s"(c.rx), [rw] "s"(rqkv)
      : "m0", "scc", "memory", ACC_FILE);
    }
    const unsigned trc = ((c.lane & 15) >> 2) * 64 + ((c.lane >> 4) & 1) * 32 + (c.lane & 3) * 8;
    {
      const unsigned xb = reg + L_XIM + (c.l31 >> 3) * 1024 + (c.l31 & 7) * 128;
      const unsigned xw = reg + L_XIM + (c.lane >> 3) * 128 + (((c.lane & 7) ^ (c.lane >> 3)) * 16);
      const unsigned xa0 = xb + ((c.half ^ (c.l31 & 7)) * 16);
      const unsigned ra = xb + ((((h0 & 1) * 4) ^ (c.l31 & 7)) * 16) + c.half * 8, rj = U(h0 >> 1);
      const unsigned xo = U(h0 < 14 ? WS_L0 + LSTRIDE + O_XF + ((p0 * 4 + c.w) * 28 + 2 * h0) * 1024 : OOB);
      const unsigned wm = U(reg + 7168);
      unsigned wo = U(wsrc + 2 * DM * 2);
      const unsigned b0 = c.lds0 + 8 * 64 * c.half + trc, c0 = b0 + c.w * WREG;
      const unsigned zp0 = reg + 56 + c.lane * 64, zp1 = reg + 56 + (64 + min(c.lane, 47u)) * 64;
      const rsrc_t rs = __builtin_amdgcn_make_buffer_rsrc((void*)(c.w == 0 ? p.w1 : c.w == 1 ? p.w2 : p.wo), 0,
                                                          h0 >= 14 || c.w == 3 ? 0 : c.w == 2 ? (p0 + 1) * 28 * (DM * 2) : DM * DFF * 2, 0x00020000);
      const unsigned rstride = c.w == 0 ? DFF * 2 : DM * 2, sv = rq * rstride + pc, sst = U(16 * rstride);
      unsigned ss = U(c.w == 0 ? h0 * 256 + (p0 >> 2) * 64 + (p0 & 3) * 112 * (DFF * 2) : c.w == 1 ? p0 * 112 * (DM * 2) + h0 * 64 : p0 * 28 * (DM * 2) + h0 * 64);
      const unsigned csr = c.lds0 + L_CS + c.l31 * 16, csw = csr + c.w * 4;
      const float lo1 = c.half ? 0.f : 1.f, pb1 = c.lane >= 28 && c.lane < 31 ? 1.f : 0.f, pb2 = c.lane == 28 ? 1.f : 0.f;
      const unsigned weo = U(p0 == 0 && c.w == 0 ? WS_WEQ + h0 * 3072 : OOB);
      QKV_ARGS(WS_L0, h0, p0, L_MISC0)
      unsigned tmp;
      const u32x8 aux0 = {b0 + 2 * WREG, zp1, csw, rtr, ra ^ 16, ra ^ 32, ra ^ 48, xa0 ^ 32};
      const u32x8 aux1 = {__float_as_uint(lo1), __float_as_uint(pb1), __float_as_uint(pb2), kmw, rtw, kl, zp0, csr};
    asm volatile(
      "v_xor_b32 v34, 64, %[x0]\n\t"
      "v_xor_b32 v35, 96, %[x0]\n\t"
      "v_mov_b32 v40, 0x3f803f80\n\t"
      "v_mov_b32 v41, 0x3f803f80\n\t"
      "v_mov_b32 v42, 0x3f803f80\n\t"
      "v_mov_b32 v43, 0x3f803f80\n\t"
      "v_mov_b32 v54, 0\n\t"
      "v_mov_b32 v55, 1.0\n\t"
      "s_mov_b32 m0, %[wm]\n\t"
      "v_mov_b32 v36, 0\n\t"
      "v_mov_b32 v37, 0\n\t"
      "v_mov_b32 v38, 0\n\t"
      "v_mov_b32 v39, 0\n\t"
      "s_waitcnt vmcnt(28)\n\t"
      "ds_write_b64 v30, v[54:55] offset:0\n\t"
      "ds_write_b64 v17, v[54:55] offset:0\n\t"
      "s_waitcnt lgkmcnt(0)\n\t"
      "s_barrier\n\t"
      "v_mov_b32 v55, 0\n\t"
      "v_lshrrev_b32 v52, 4, %[l16]\n\t"
      "v_mul_lo_u32 v52, v52, %[sst]\n\t"
      "v_lshrrev_b32 v52, 4, v52\n\t"
      "s_lshl_b32 %[tmp], %[sst], 2\n\t"
      "s_add_u32 %[tmp], %[tmp], %[ss]\n\t"
      "buffer_load_dword v53, v52, %[rs], %[ss] offen\n\t"
      "buffer_load_dword v53, v52, %[rs], %[tmp] offen\n\t"
      "v_min_u32 v52, 0x370, %[l16]\n\t"
      "global_load_dword v53, v52, %[pb2a]\n\t"
      "v_lshlrev_b32 v52, 2, v52\n\t"
      "global_load_dword v53, v52, %[pb1a]\n\t"
      "ds_read_b64_tr_b16 v[120:121], %[c0] offset:0\n\t"
      "ds_read_b64_tr_b16 v[122:123], %[c0] offset:256\n\t"
      "ds_read_b64_tr_b16 v[124:125], %[c0] offset:1024\n\t"
      "ds_read_b64_tr_b16 v[126:127], %[c0] offset:1280\n\t"
      "ds_read_b64_tr_b16 v[128:129], %[c0] offset:2048\n\t"
      "ds_read_b64_tr_b16 v[130:131], %[c0] offset:2304\n\t"
      "ds_read_b64_tr_b16 v[132:133], %[c0] offset:3072\n\t"
      "ds_read_b64_tr_b16 v[134:135], %[c0] offset:3328\n\t"
      "ds_read_b64_tr_b16 v[136:137], %[c0] offset:4096\n\t"
      "ds_read_b64_tr_b16 v[138:139], %[c0] offset:4352\n\t"
      "ds_read_b64_tr_b16 v[140:141], %[c0] offset:5120\n\t"
      "ds_read_b64_tr_b16 v[142:143], %[c0] offset:5376\n\t"
      "buffer_load_dwordx4 %[wv], %[rw], %[wo] offen lds\n\t"
      "s_add_u32 m0, m0, 0x400\n\t"
      "s_add_u32 %[wo], %[wo], 0xa800\n\t"
      "buffer_load_dwordx4 %[wv], %[rw], %[wo] offen lds\n\t"
      "s_add_u32 m0, m0, 0x400\n\t"
      "s_add_u32 %[wo], %[wo], 0xa800\n\t"
      "s_waitcnt lgkmcnt(10)\n\t"
      "v_mfma_f32_32x32x16_bf16 v[56:71], v[40:43], v[120:123], 0\n\t"
      "ds_read_b64_tr_b16 v[120:121], %[c0] offset:6144\n\t"
      "ds_read_b64_tr_b16 v[122:123], %[c0] offset:6400\n\t"
      "s_waitcnt lgkmcnt(10)\n\t"
      "v_mfma_f32_32x32x16_bf16 v[56:71], v[40:43], v[124:127], v[56:71]\n\t"
      "s_waitcnt lgkmcnt(8)\n\t"
      "v_mfma_f32_32x32x16_bf16 v[56:71], v[40:43], v[128:131], v[56:71]\n\t"
      "s_waitcnt lgkmcnt(6)\n\t"
      "v_mfma_f32_32x32x16_bf16 v[56:71], v[40:43], v[132:135], v[56:71]\n\t"
      "s_waitcnt lgkmcnt(4)\n\t"
      "v_mfma_f32_32x32x16_bf16 v[56:71], v[40:43], v[136:139], v[56:71]\n\t"
      "s_waitcnt lgkmcnt(2)\n\t"
      "v_mfma_f32_32x32x16_bf16 v[56:71], v[40:43], v[140:143], v[56:71]\n\t"
      "s_waitcnt lgkmcnt(0)\n\t"
      "v_mfma_f32_32x32x16_bf16 v[56:71], v[40:43], v[120:123], v[56:71]\n\t"
      "s_waitcnt vmcnt(30)\n\t"
      "ds_write_b128 %[xw], v[144:147] offset:0\n\t"
      "ds_write_b128 %[xw], v[148:151] offset:1024\n\t"
      "ds_write_b128 %[xw], v[152:155] offset:2048\n\t"
      "ds_write_b128 %[xw], v[156:159] offset:3072\n\t"
      "ds_read_b128 v[144:147], %[x0] offset:0\n\t"
      "ds_read_b128 v[148:151], v23 offset:0\n\t"
      "ds_read_b128 v[152:155], v34 offset:0\n\t"
      "ds_read_b128 v[156:159], v35 offset:0\n\t"
      "s_cmp_lg_u32 %[rj], 0\n\t"
      "s_cbranch_scc1 10f\n\t"
      "ds_read_b64 v[44:45], %[r0] offset:0\n\t"
      "ds_read_b64 v[46:47], v20 offset:0\n\t"
      "ds_read_b64 v[48:49], v21 offset:0\n\t"
      "ds_read_b64 v[50:51], v22 offset:0\n\t"
      "10:\n\t"
      "s_waitcnt lgkmcnt(7)\n\t"
      "ds_read_b64_tr_b16 v[104:105], %[b0] offset:0\n\t"
      "ds_read_b64_tr_b16 v[106:107], %[b0] offset:256\n\t"
      "ds_read_b64_tr_b16 v[108:109], %[b0] offset:1024\n\t"
      "ds_read_b64_tr_b16 v[110:111], %[b0] offset:1280\n\t"
      "ds_read_b64_tr_b16 v[112:113], %[b0] offset:2048\n\t"
      "ds_read_b64_tr_b16 v[114:115], %[b0] offset:2304\n\t"
      "ds_read_b64_tr_b16 v[116:117], %[b0] offset:3072\n\t"
      "ds_read_b64_tr_b16 v[118:119], %[b0] offset:3328\n\t"
      "s_waitcnt lgkmcnt(0)\n\t"
      "ds_write_b32 v18, v56 offset:512\n\t"
      "v_mfma_f32_32x32x16_bf16 v[72:87], v[104:107], v[144:147], 0\n\t"
      "s_waitcnt vmcnt(26)\n\t"
      "ds_write_b128 %[xw], v[160:163] offset:4096\n\t"
      "ds_write_b128 %[xw], v[164:167] offset:5120\n\t"
      "ds_write_b128 %[xw], v[168:171] offset:6144\n\t"
      "ds_write_b128 %[xw], v[172:175] offset:7168\n\t"
      "ds_read_b128 v[160:163], %[x0] offset:4096\n\t"
      "ds_read_b128 v[164:167], v23 offset:4096\n\t"
      "ds_read_b128 v[168:171], v34 offset:4096\n\t"
      "ds_read_b128 v[172:175], v35 offset:4096\n\t"
      "s_cmp_lg_u32 %[rj], 1\n\t"
      "s_cbranch_scc1 11f\n\t"
      "ds_read_b64 v[44:45], %[r0] offset:4096\n\t"
      "ds_read_b64 v[46:47], v20 offset:4096\n\t"
      "ds_read_b64 v[48:49], v21 offset:4096\n\t"
      "ds_read_b64 v[50:51], v22 offset:4096\n\t"
      "11:\n\t"
      "v_mfma_f32_32x32x16_bf16 v[72:87], v[108:111], v[148:151], v[72:87]\n\t"
      "buffer_load_dwordx4 %[wv], %[rw], %[wo] offen lds\n\t"
      "s_add_u32 m0, m0, 0x400\n\t"
      "s_add_u32 %[wo], %[wo], 0xa800\n\t"
      "v_mfma_f32_32x32x16_bf16 v[72:87], v[112:115], v[152:155], v[72:87]\n\t"
      "s_waitcnt lgkmcnt(7)\n\t"
      "ds_read_b64_tr_b16 v[120:121], %[b0] offset:4096\n\t"
      "ds_read_b64_tr_b16 v[122:123], %[b0] offset:4352\n\t"
      "ds_read_b64_tr_b16 v[124:125], %[b0] offset:5120\n\t"
      "ds_read_b64_tr_b16 v[126:127], %[b0] offset:5376\n\t"
      "ds_read_b64_tr_b16 v[128:129], %[b0] offset:6144\n\t"
      "ds_read_b64_tr_b16 v[130:131], %[b0] offset:6400\n\t"
      "ds_read_b64_tr_b16 v[132:133], %[b0] offset:40960\n\t"
      "ds_read_b64_tr_b16 v[134:135], %[b0] offset:41216\n\t"
      "v_mfma_f32_32x32x16_bf16 v[72:87], v[116:119], v[156:159], v[72:87]\n\t"
      "buffer_load_dwordx4 %[wv], %[rw], %[wo] offen lds\n\t"
      "s_add_u32 m0, m0, 0x400\n\t"
      "s_add_u32 %[wo], %[wo], 0xa800\n\t"
      "v_dot2c_f32_bf16 v36, v144, v144\n\t"
      "v_dot2c_f32_bf16 v37, v145, v145\n\t"
      "v_dot2c_f32_bf16 v38, v146, v146\n\t"
      "v_dot2c_f32_bf16 v39, v147, v147\n\t"
      "v_dot2c_f32_bf16 v36, v148, v148\n\t"
      "v_dot2c_f32_bf16 v37, v149, v149\n\t"
      "v_dot2c_f32_bf16 v38, v150, v150\n\t"
      "v_dot2c_f32_bf16 v39, v151, v151\n\t"
      "v_dot2c_f32_bf16 v36, v152, v152\n\t"
      "v_dot2c_f32_bf16 v37, v153, v153\n\t"
      "v_dot2c_f32_bf16 v38, v154, v154\n\t"
      "v_dot2c_f32_bf16 v39, v155, v155\n\t"
      "v_dot2c_f32_bf16 v36, v156, v156\n\t"
      "v_dot2c_f32_bf16 v37, v157, v157\n\t"
      "v_dot2c_f32_bf16 v38, v158, v158\n\t"
      "v_dot2c_f32_bf16 v39, v159, v159\n\t"
      "s_waitcnt lgkmcnt(0)\n\t"
      "v_mfma_f32_32x32x16_bf16 v[72:87], v[120:123], v[160:163], v[72:87]\n\t"
      "s_waitcnt vmcnt(24)\n\t"
      "ds_write_b128 %[xw], v[176:179] offset:0\n\t"
      "ds_write_b128 %[xw], v[180:183] offset:1024\n\t"
      "ds_write_b128 %[xw], v[184:187] offset:2048\n\t"
      "ds_write_b128 %[xw], v[188:191] offset:3072\n\t"
      "ds_read_b128 v[176:179], %[x0] offset:0\n\t"
      "ds_read_b128 v[180:183], v23 offset:0\n\t"
      "ds_read_b128 v[184:187], v34 offset:0\n\t"
      "ds_read_b128 v[188:191], v35 offset:0\n\t"
      "s_cmp_lg_u32 %[rj], 2\n\t"
      "s_cbranch_scc1 12f\n\t"
      "ds_read_b64 v[44:45], %[r0] offset:0\n\t"
      "ds_read_b64 v[46:47], v20 offset:0\n\t"
      "ds_read_b64 v[48:49], v21 offset:0\n\t"
      "ds_read_b64 v[50:51], v22 offset:0\n\t"
      "12:\n\t"
      "v_mfma_f32_32x32x16_bf16 v[72:87], v[124:127], v[164:167], v[72:87]\n\t"
      "buffer_load_dwordx4 %[wv], %[rw], %[wo] offen lds\n\t"
      "s_add_u32 m0, m0, 0x400\n\t"
      "s_add_u32 %[wo], %[wo], 0xa800\n\t"
      "v_mfma_f32_32x32x16_bf16 v[72:87], v[128:131], v[168:171], v[72:87]\n\t"
      "s_waitcnt lgkmcnt(7)\n\t"
      "ds_read_b64_tr_b16 v[104:105], %[b0] offset:41984\n\t"
      "ds_read_b64_tr_b16 v[106:107], %[b0] offset:42240\n\t"
      "ds_read_b64_tr_b16 v[108:109], %[b0] offset:43008\n\t"
      "ds_read_b64_tr_b16 v[110:111], %[b0] offset:43264\n\t"
      "ds_read_b64_tr_b16 v[112:113], %[b0] offset:44032\n\t"
      "ds_read_b64_tr_b16 v[114:115], %[b0] offset:44288\n\t"
      "ds_read_b64_tr_b16 v[116:117], %[b0] offset:45056\n\t"
      "ds_read_b64_tr_b16 v[118:119], %[b0] offset:45312\n\t"
      "v_mfma_f32_32x32x16_bf16 v[72:87], v[132:135], v[172:175], v[72:87]\n\t"
      "buffer_load_dwordx4 %[wv], %[rw], %[wo] offen lds\n\t"
      "s_add_u32 m0, m0, 0x400\n\t"
      "s_add_u32 %[wo], %[wo], 0xa800\n\t"
      "v_dot2c_f32_bf16 v36, v160, v160\n\t"
      "v_dot2c_f32_bf16 v37, v161, v161\n\t"
      "v_dot2c_f32_bf16 v38, v162, v162\n\t"
      "v_dot2c_f32_bf16 v39, v163, v163\n\t"
      "v_dot2c_f32_bf16 v36, v164, v164\n\t"
      "v_dot2c_f32_bf16 v37, v165, v165\n\t"
      "v_dot2c_f32_bf16 v38, v166, v166\n\t"
      "v_dot2c_f32_bf16 v39, v167, v167\n\t"
      "v_dot2c_f32_bf16 v36, v168, v168\n\t"
      "v_dot2c_f32_bf16 v37, v169, v169\n\t"
      "v_dot2c_f32_bf16 v38, v170, v170\n\t"
      "v_dot2c_f32_bf16 v39, v171, v171\n\t"
      "v_dot2c_f32_bf16 v36, v172, v172\n\t"
      "v_dot2c_f32_bf16 v37, v173, v173\n\t"
      "v_dot2c_f32_bf16 v38, v174, v174\n\t"
      "v_dot2c_f32_bf16 v39, v175, v175\n\t"
      "s_waitcnt lgkmcnt(0)\n\t"
      "v_mfma_f32_32x32x16_bf16 v[72:87], v[104:107], v[176:179], v[72:87]\n\t"
      "s_waitcnt vmcnt(22)\n\t"
      "ds_write_b128 %[xw], v[192:195] offset:4096\n\t"
      "ds_write_b128 %[xw], v[196:199] offset:5120\n\t"
      "ds_write_b128 %[xw], v[200:203] offset:6144\n\t"
      "ds_write_b128 %[xw], v[204:207] offset:7168\n\t"
      "ds_read_b128 v[192:195], %[x0] offset:4096\n\t"
      "ds_read_b128 v[196:199], v23 offset:4096\n\t"
      "ds_read_b128 v[200:203], v34 offset:4096\n\t"
      "ds_read_b128 v[204:207], v35 offset:4096\n\t"
      "s_cmp_lg_u32 %[rj], 3\n\t"
      "s_cbranch_scc1 13f\n\t"
      "ds_read_b64 v[44:45], %[r0] offset:4096\n\t"
      "ds_read_b64 v[46:47], v20 offset:4096\n\t"
      "ds_read_b64 v[48:49], v21 offset:4096\n\t"
      "ds_read_b64 v[50:51], v22 offset:4096\n\t"
      "13:\n\t"
      "v_mfma_f32_32x32x16_bf16 v[72:87], v[108:111], v[180:183], v[72:87]\n\t"
      "buffer_load_dwordx4 %[wv], %[rw], %[wo] offen lds\n\t"
      "s_add_u32 m0, m0, 0x400\n\t"
      "s_sub_u32 %[wo], %[wo], 0x3f700\n\t"
      "v_mfma_f32_32x32x16_bf16 v[72:87], v[112:115], v[184:187], v[72:87]\n\t"
      "s_waitcnt lgkmcnt(7)\n\t"
      "ds_read_b64_tr_b16 v[120:121], %[b0] offset:46080\n\t"
      "ds_read_b64_tr_b16 v[122:123], %[b0] offset:46336\n\t"
      "ds_read_b64_tr_b16 v[124:125], %[b0] offset:47104\n\t"
      "ds_read_b64_tr_b16 v[126:127], %[b0] offset:47360\n\t"
      "ds_read_b64_tr_b16 v[128:129], v16 offset:0\n\t"
      "ds_read_b64_tr_b16 v[130:131], v16 offset:256\n\t"
      "ds_read_b64_tr_b16 v[132:133], v16 offset:1024\n\t"
      "ds_read_b64_tr_b16 v[134:135], v16 offset:1280\n\t"
      "v_mfma_f32_32x32x16_bf16 v[72:87], v[116:119], v[188:191], v[72:87]\n\t"
      "buffer_load_dwordx4 %[wv], %[rw], %[wo] offen lds\n\t"
      "s_add_u32 m0, m0, 0x400\n\t"
      "s_add_u32 %[wo], %[wo], 0xa800\n\t"
      "v_dot2c_f32_bf16 v36, v176, v176\n\t"
      "v_dot2c_f32_bf16 v37, v177, v177\n\t"
      "v_dot2c_f32_bf16 v38, v178, v178\n\t"
      "v_dot2c_f32_bf16 v39, v179, v179\n\t"
      "v_dot2c_f32_bf16 v36, v180, v180\n\t"
      "v_dot2c_f32_bf16 v37, v181, v181\n\t"
      "v_dot2c_f32_bf16 v38, v182, v182\n\t"
      "v_dot2c_f32_bf16 v39, v183, v183\n\t"
      "v_dot2c_f32_bf16 v36, v184, v184\n\t"
      "v_dot2c_f32_bf16 v37, v185, v185\n\t"
      "v_dot2c_f32_bf16 v38, v186, v186\n\t"
      "v_dot2c_f32_bf16 v39, v187, v187\n\t"
      "v_dot2c_f32_bf16 v36, v188, v188\n\t"
      "v_dot2c_f32_bf16 v37, v189, v189\n\t"
      "v_dot2c_f32_bf16 v38, v190, v190\n\t"
      "v_dot2c_f32_bf16 v39, v191, v191\n\t"
      "s_waitcnt lgkmcnt(0)\n\t"
      "v_mfma_f32_32x32x16_bf16 v[72:87], v[120:123], v[192:195], v[72:87]\n\t"
      "s_waitcnt vmcnt(20)\n\t"
      "ds_write_b128 %[xw], v[208:211] offset:0\n\t"
      "ds_write_b128 %[xw], v[212:215] offset:1024\n\t"
      "ds_write_b128 %[xw], v[216:219] offset:2048\n\t"
      "ds_write_b128 %[xw], v[220:223] offset:3072\n\t"
      "ds_read_b128 v[208:211], %[x0] offset:0\n\t"
      "ds_read_b128 v[212:215], v23 offset:0\n\t"
      "ds_read_b128 v[216:219], v34 offset:0\n\t"
      "ds_read_b128 v[220:223], v35 offset:0\n\t"
      "s_cmp_lg_u32 %[rj], 4\n\t"
      "s_cbranch_scc1 14f\n\t"
      "ds_read_b64 v[44:45], %[r0] offset:0\n\t"
      "ds_read_b64 v[46:47], v20 offset:0\n\t"
      "ds_read_b64 v[48:49], v21 offset:0\n\t"
      "ds_read_b64 v[50:51], v22 offset:0\n\t"
      "14:\n\t"
      "v_mfma_f32_32x32x16_bf16 v[72:87], v[124:127], v[196:199], v[72:87]\n\t"
      "buffer_load_dwordx4 %[wv], %[rw], %[wo] offen lds\n\t"
      "s_add_u32 m0, m0, 0x400\n\t"
      "s_add_u32 %[wo], %[wo], 0xa800\n\t"
      "v_mfma_f32_32x32x16_bf16 v[72:87], v[128:131], v[200:203], v[72:87]\n\t"
      "s_waitcnt lgkmcnt(7)\n\t"
      "ds_read_b64_tr_b16 v[104:105], v16 offset:2048\n\t"
      "ds_read_b64_tr_b16 v[106:107], v16 offset:2304\n\t"
      "ds_read_b64_tr_b16 v[108:109], v16 offset:3072\n\t"
      "ds_read_b64_tr_b16 v[110:111], v16 offset:3328\n\t"
      "ds_read_b64_tr_b16 v[112:113], v16 offset:4096\n\t"
      "ds_read_b64_tr_b16 v[114:115], v16 offset:4352\n\t"
      "ds_read_b64_tr_b16 v[116:117], v16 offset:5120\n\t"
      "ds_read_b64_tr_b16 v[118:119], v16 offset:5376\n\t"
      "v_mfma_f32_32x32x16_bf16 v[72:87], v[132:135], v[204:207], v[72:87]\n\t"
      "buffer_load_dwordx4 %[wv], %[rw], %[wo] offen lds\n\t"
      "s_add_u32 m0, m0, 0x400\n\t"
      "s_add_u32 %[wo], %[wo], 0xa800\n\t"
      "v_dot2c_f32_bf16 v36, v192, v192\n\t"
      "v_dot2c_f32_bf16 v37, v193, v193\n\t"
      "v_dot2c_f32_bf16 v38, v194, v194\n\t"
      "v_dot2c_f32_bf16 v39, v195, v195\n\t"
      "v_dot2c_f32_bf16 v36, v196, v196\n\t"
      "v_dot2c_f32_bf16 v37, v197, v197\n\t"
      "v_dot2c_f32_bf16 v38, v198, v198\n\t"
      "v_dot2c_f32_bf16 v39, v199, v199\n\t"
      "v_dot2c_f32_bf16 v36, v200, v200\n\t"
      "v_dot2c_f32_bf16 v37, v201, v201\n\t"
      "v_dot2c_f32_bf16 v38, v202, v202\n\t"
      "v_dot2c_f32_bf16 v39, v203, v203\n\t"
      "v_dot2c_f32_bf16 v36, v204, v204\n\t"
      "v_dot2c_f32_bf16 v37, v205, v205\n\t"
      "v_dot2c_f32_bf16 v38, v206, v206\n\t"
      "v_dot2c_f32_bf16 v39, v207, v207\n\t"
      "s_waitcnt lgkmcnt(0)\n\t"
      "v_mfma_f32_32x32x16_bf16 v[72:87], v[104:107], v[208:211], v[72:87]\n\t"
      "s_waitcnt vmcnt(18)\n\t"
      "ds_write_b128 %[xw], v[224:227] offset:4096\n\t"
      "ds_write_b128 %[xw], v[228:231] offset:5120\n\t"
      "ds_write_b128 %[xw], v[232:235] offset:6144\n\t"
      "ds_write_b128 %[xw], v[236:239] offset:7168\n\t"
      "ds_read_b128 v[224:227], %[x0] offset:4096\n\t"
      "ds_read_b128 v[228:231], v23 offset:4096\n\t"
      "ds_read_b128 v[232:235], v34 offset:4096\n\t"
      "ds_read_b128 v[236:239], v35 offset:4096\n\t"
      "s_cmp_lg_u32 %[rj], 5\n\t"
      "s_cbranch_scc1 15f\n\t"
      "ds_read_b64 v[44:45], %[r0] offset:4096\n\t"
      "ds_read_b64 v[46:47], v20 offset:4096\n\t"
      "ds_read_b64 v[48:49], v21 offset:4096\n\t"
      "ds_read_b64 v[50:51], v22 offset:4096\n\t"
      "15:\n\t"
      "v_mfma_f32_32x32x16_bf16 v[72:87], v[108:111], v[212:215], v[72:87]\n\t"
      "buffer_load_dwordx4 %[wv], %[rw], %[wo] offen lds\n\t"
      "s_add_u32 m0, m0, 0x400\n\t"
      "s_add_u32 %[wo], %[wo], 0xa800\n\t"
      "v_mfma_f32_32x32x16_bf16 v[72:87], v[112:115], v[216:219], v[72:87]\n\t"
      "s_waitcnt lgkmcnt(7)\n\t"
      "ds_read_b64_tr_b16 v[120:121], v16 offset:6144\n\t"
      "ds_read_b64_tr_b16 v[122:123], v16 offset:6400\n\t"
      "ds_read_b64_tr_b16 v[124:125], v16 offset:40960\n\t"
      "ds_read_b64_tr_b16 v[126:127], v16 offset:41216\n\t"
      "ds_read_b64_tr_b16 v[128:129], v16 offset:41984\n\t"
      "ds_read_b64_tr_b16 v[130:131], v16 offset:42240\n\t"
      "ds_read_b64_tr_b16 v[132:133], v16 offset:43008\n\t"
      "ds_read_b64_tr_b16 v[134:135], v16 offset:43264\n\t"
      "v_mfma_f32_32x32x16_bf16 v[72:87], v[116:119], v[220:223], v[72:87]\n\t"
      "buffer_load_dwordx4 %[wv], %[rw], %[wo] offen lds\n\t"
      "s_add_u32 m0, m0, 0x400\n\t"
      "s_add_u32 %[wo], %[wo], 0xa800\n\t"
      "v_dot2c_f32_bf16 v36, v208, v208\n\t"
      "v_dot2c_f32_bf16 v37, v209, v209\n\t"
      "v_dot2c_f32_bf16 v38, v210, v210\n\t"
      "v_dot2c_f32_bf16 v39, v211, v211\n\t"
      "v_dot2c_f32_bf16 v36, v212, v212\n\t"
      "v_dot2c_f32_bf16 v37, v213, v213\n\t"
      "v_dot2c_f32_bf16 v38, v214, v214\n\t"
      "v_dot2c_f32_bf16 v39, v215, v215\n\t"
      "v_dot2c_f32_bf16 v36, v216, v216\n\t"
      "v_dot2c_f32_bf16 v37, v217, v217\n\t"
      "v_dot2c_f32_bf16 v38, v218, v218\n\t"
      "v_dot2c_f32_bf16 v39, v219, v219\n\t"
      "v_dot2c_f32_bf16 v36, v220, v220\n\t"
      "v_dot2c_f32_bf16 v37, v221, v221\n\t"
      "v_dot2c_f32_bf16 v38, v222, v222\n\t"
      "v_dot2c_f32_bf16 v39, v223, v223\n\t"
      "s_waitcnt lgkmcnt(0)\n\t"
      "v_mfma_f32_32x32x16_bf16 v[72:87], v[120:123], v[224:227], v[72:87]\n\t"
      "s_waitcnt vmcnt(16)\n\t"
      "ds_write_b128 %[xw], v[240:243] offset:0\n\t"
      "ds_write_b128 %[xw], v[244:247] offset:1024\n\t"
      "ds_write_b128 %[xw], v[248:251] offset:2048\n\t"
      "ds_write_b128 %[xw], v[252:255] offset:3072\n\t"
      "ds_read_b128 v[240:243], %[x0] offset:0\n\t"
      "ds_read_b128 v[244:247], v23 offset:0\n\t"
      "ds_read_b128 v[248:251], v34 offset:0\n\t"
      "ds_read_b128 v[252:255], v35 offset:0\n\t"
      "s_cmp_lg_u32 %[rj], 6\n\t"
      "s_cbranch_scc1 16f\n\t"
      "ds_read_b64 v[44:45], %[r0] offset:0\n\t"
      "ds_read_b64 v[46:47], v20 offset:0\n\t"
      "ds_read_b64 v[48:49], v21 offset:0\n\t"
      "ds_read_b64 v[50:51], v22 offset:0\n\t"
      "16:\n\t"
      "v_mfma_f32_32x32x16_bf16 v[72:87], v[124:127], v[228:231], v[72:87]\n\t"
      "buffer_load_dwordx4 %[wv], %[rw], %[wo] offen lds\n\t"
      "s_add_u32 m0, m0, 0x400\n\t"
      "s_add_u32 %[wo], %[wo], 0xa800\n\t"
      "v_mfma_f32_32x32x16_bf16 v[72:87], v[128:131], v[232:235], v[72:87]\n\t"
      "s_waitcnt lgkmcnt(7)\n\t"
      "ds_read_b64_tr_b16 v[104:105], v16 offset:44032\n\t"
      "ds_read_b64_tr_b16 v[106:107], v16 offset:44288\n\t"
      "ds_read_b64_tr_b16 v[108:109], v16 offset:45056\n\t"
      "ds_read_b64_tr_b16 v[110:111], v16 offset:45312\n\t"
      "ds_read_b64_tr_b16 v[112:113], v16 offset:46080\n\t"
      "ds_read_b64_tr_b16 v[114:115], v16 offset:46336\n\t"
      "ds_read_b64_tr_b16 v[116:117], v16 offset:47104\n\t"
      "ds_read_b64_tr_b16 v[118:119], v16 offset:47360\n\t"
      "v_mfma_f32_32x32x16_bf16 v[72:87], v[132:135], v[236:239], v[72:87]\n\t"
      "buffer_load_dwordx4 %[wv], %[rw], %[wo] offen lds\n\t"
      "s_add_u32 m0, m0, 0x400\n\t"
      "s_add_u32 %[wo], %[wo], 0xa800\n\t"
      "v_dot2c_f32_bf16 v36, v224, v224\n\t"
      "v_dot2c_f32_bf16 v37, v225, v225\n\t"
      "v_dot2c_f32_bf16 v38, v226, v226\n\t"
      "v_dot2c_f32_bf16 v39, v227, v227\n\t"
      "v_dot2c_f32_bf16 v36, v228, v228\n\t"
      "v_dot2c_f32_bf16 v37, v229, v229\n\t"
      "v_dot2c_f32_bf16 v38, v230, v230\n\t"
      "v_dot2c_f32_bf16 v39, v231, v231\n\t"
      "v_dot2c_f32_bf16 v36, v232, v232\n\t"
      "v_dot2c_f32_bf16 v37, v233, v233\n\t"
      "v_dot2c_f32_bf16 v38, v234, v234\n\t"
      "v_dot2c_f32_bf16 v39, v235, v235\n\t"
      "v_dot2c_f32_bf16 v36, v236, v236\n\t"
      "v_dot2c_f32_bf16 v37, v237, v237\n\t"
      "v_dot2c_f32_bf16 v38, v238, v238\n\t"
      "v_dot2c_f32_bf16 v39, v239, v239\n\t"
      "s_waitcnt lgkmcnt(0)\n\t"
      "v_mfma_f32_32x32x16_bf16 v[72:87], v[104:107], v[240:243], v[72:87]\n\t"
      "v_mfma_f32_32x32x16_bf16 v[72:87], v[108:111], v[244:247], v[72:87]\n\t"
      "v_mfma_f32_32x32x16_bf16 v[72:87], v[112:115], v[248:251], v[72:87]\n\t"
      "v_mfma_f32_32x32x16_bf16 v[72:87], v[116:119], v[252:255], v[72:87]\n\t"
      "v_dot2c_f32_bf16 v36, v240, v240\n\t"
      "v_dot2c_f32_bf16 v37, v241, v241\n\t"
      "v_dot2c_f32_bf16 v38, v242, v242\n\t"
      "v_dot2c_f32_bf16 v39, v243, v243\n\t"
      "v_dot2c_f32_bf16 v36, v244, v244\n\t"
      "v_dot2c_f32_bf16 v37, v245, v245\n\t"
      "v_dot2c_f32_bf16 v38, v246, v246\n\t"
      "v_dot2c_f32_bf16 v39, v247, v247\n\t"
      "v_dot2c_f32_bf16 v36, v248, v248\n\t"
      "v_dot2c_f32_bf16 v37, v249, v249\n\t"
      "v_dot2c_f32_bf16 v38, v250, v250\n\t"
      "v_dot2c_f32_bf16 v39, v251, v251\n\t"
      "v_dot2c_f32_bf16 v36, v252, v252\n\t"
      "v_dot2c_f32_bf16 v37, v253, v253\n\t"
      "v_dot2c_f32_bf16 v38, v254, v254\n\t"
      "v_dot2c_f32_bf16 v39, v255, v255\n\t"
      "s_waitcnt lgkmcnt(0)\n\t"
      "buffer_store_dwordx4 v[44:47], %[l16], %[ws], %[xo] offen sc1\n\t"
      "buffer_store_dwordx4 v[48:51], %[l16], %[ws], %[xo] offen offset:1024 sc1\n\t"
      "s_waitcnt lgkmcnt(0)\n\t"
      "s_barrier\n\t"
      "v_pk_add_f32 v[36:37], v[36:37], v[38:39]\n\t"
      "v_fma_f32 v32, -v24, v87, v87\n\t"
      "v_add_f32 v33, v36, v37\n\t"
      "v_mov_b32 v34, v32\n\t"
      "v_mov_b32 v35, v33\n\t"
      "s_nop 0\n\t"
      "v_permlane32_swap_b32 v32, v34\n\t"
      "v_permlane32_swap_b32 v33, v35\n\t"
      "v_add_f32 v32, v32, v34\n\t"
      "v_add_f32 v33, v33, v35\n\t"
      "v_mul_f32 v32, 0xbb124925, v32\n\t"
      "v_mul_f32 v34, v32, -v32\n\t"
      "v_fmamk_f32 v33, v33, 0x3b124925, v34\n\t"
      "v_max_f32 v33, 0, v33\n\t"
      "v_add_f32 v33, 0x3727c5ac, v33\n\t"
      "v_rsq_f32 v50, v33\n\t"
      "s_nop 0\n\t"
      "v_mul_f32 v51, v33, v50\n\t"
      "v_cvt_pk_bf16_f32 v46, v32, v51\n\t"
      "v_lshlrev_b32 v34, 16, v46\n\t"
      "v_and_b32 v35, 0xffff0000, v46\n\t"
      "v_sub_f32 v34, v32, v34\n\t"
      "v_sub_f32 v35, v51, v35\n\t"
      "v_cvt_pk_bf16_f32 v47, v34, v35\n\t"
      "v_mov_b32 v48, v46\n\t"
      "v_mov_b32 v49, v46\n\t"
      "ds_read_b128 v[32:35], v31 offset:512\n\t"
      "ds_write_b32 v28, v50\n\t"
      "v_mul_f32 v52, 0x3e8b97f5, v50\n\t"
      "ds_read_b128 v[104:107], v19 offset:0\n\t"
      "ds_read_b128 v[108:111], v19 offset:32\n\t"
      "ds_read_b128 v[112:115], v19 offset:64\n\t"
      "ds_read_b128 v[116:119], v19 offset:96\n\t"
      "s_waitcnt lgkmcnt(5)\n\t"
      "v_add_f32 v32, v32, v33\n\t"
      "v_add_f32 v34, v34, v35\n\t"
      "v_add_f32 v32, v32, v34\n\t"
      "v_mul_f32 v32, v24, v32\n\t"
      "v_cvt_pk_bf16_f32 v33, v32, v25\n\t"
      "v_lshlrev_b32 v34, 16, v33\n\t"
      "v_sub_f32 v34, v32, v34\n\t"
      "v_cvt_pk_bf16_f32 v34, v34, 0\n\t"
      "v_mov_b32 v32, v33\n\t"
      "v_mov_b32 v35, 0\n\t"
      "buffer_store_dwordx4 v[32:35], %[l16], %[ws], %[weo] offen offset:1024 sc1\n\t"
      "s_nop 0\n\t"
      "v_mfma_f32_32x32x16_bf16 v[72:87], v[32:35], v[46:49], v[72:87]\n\t"
      "buffer_load_dwordx4 %[sv], %[rs], %[ss] offen lds\n\t"
      "s_add_u32 m0, m0, 0x400\n\t"
      "s_add_u32 %[ss], %[ss], %[sst]\n\t"
      "buffer_load_dwordx4 %[sv], %[rs], %[ss] offen lds\n\t"
      "s_add_u32 m0, m0, 0x400\n\t"
      "s_add_u32 %[ss], %[ss], %[sst]\n\t"
      "buffer_load_dwordx4 %[sv], %[rs], %[ss] offen lds\n\t"
      "s_add_u32 m0, m0, 0x400\n\t"
      "s_add_u32 %[ss], %[ss], %[sst]\n\t"
      "buffer_load_dwordx4 %[sv], %[rs], %[ss] offen lds\n\t"
      "s_add_u32 m0, m0, 0x400\n\t"
      "s_add_u32 %[ss], %[ss], %[sst]\n\t"
      "v_pk_mul_f32 v[72:73], v[72:73], v[50:51] op_sel_hi:[1,0]\n\t"
      "v_pk_mul_f32 v[74:75], v[74:75], v[50:51] op_sel_hi:[1,0]\n\t"
      "v_pk_mul_f32 v[76:77], v[76:77], v[50:51] op_sel_hi:[1,0]\n\t"
      "v_pk_mul_f32 v[78:79], v[78:79], v[50:51] op_sel_hi:[1,0]\n\t"
      "v_pk_mul_f32 v[80:81], v[80:81], v[50:51] op_sel_hi:[1,0]\n\t"
      "v_pk_mul_f32 v[82:83], v[82:83], v[50:51] op_sel_hi:[1,0]\n\t"
      "v_pk_mul_f32 v[84:85], v[84:85], v[50:51] op_sel_hi:[1,0]\n\t"
      "v_pk_mul_f32 v[86:87], v[86:87], v[50:51] op_sel_hi:[1,0]\n\t"
      "v_pk_mul_f32 v[36:37], v[72:73], v[72:73]\n\t"
      "v_pk_mul_f32 v[38:39], v[74:75], v[74:75]\n\t"
      "v_pk_fma_f32 v[36:37], v[76:77], v[76:77], v[36:37]\n\t"
      "v_pk_fma_f32 v[38:39], v[78:79], v[78:79], v[38:39]\n\t"
      "v_pk_fma_f32 v[36:37], v[80:81], v[80:81], v[36:37]\n\t"
      "v_pk_fma_f32 v[38:39], v[82:83], v[82:83], v[38:39]\n\t"
      "v_pk_fma_f32 v[36:37], v[84:85], v[84:85], v[36:37]\n\t"
      "v_pk_fma_f32 v[38:39], v[86:87], v[86:87], v[38:39]\n\t"
      "v_pk_add_f32 v[36:37], v[36:37], v[38:39]\n\t"
      "v_add_f32 v36, v36, v37\n\t"
      "v_cvt_pk_bf16_f32 v72, v72, v73\n\t"
      "v_cvt_pk_bf16_f32 v73, v74, v75\n\t"
      "v_cvt_pk_bf16_f32 v74, v76, v77\n\t"
      "v_cvt_pk_bf16_f32 v75, v78, v79\n\t"
      "v_cvt_pk_bf16_f32 v76, v80, v81\n\t"
      "v_cvt_pk_bf16_f32 v77, v82, v83\n\t"
      "v_cvt_pk_bf16_f32 v78, v84, v85\n\t"
      "v_cvt_pk_bf16_f32 v79, v86, v87\n\t"
      "buffer_store_dwordx4 v[72:75], v29, %[ws], %[ko] offen sc1\n\t"
      "buffer_store_dwordx4 v[76:79], v29, %[ws], %[ko] offen offset:1024 sc1\n\t"
      "s_waitcnt vmcnt(16)\n\t"
      "ds_write_b64 v30, v[54:55] offset:7168\n\t"
      "ds_write_b64 v17, v[54:55] offset:7168\n\t"
      "s_waitcnt lgkmcnt(0)\n\t"
      "s_barrier\n\t"
      "ds_read_b64_tr_b16 v[120:121], %[c0] offset:7168\n\t"
      "ds_read_b64_tr_b16 v[122:123], %[c0] offset:7424\n\t"
      "ds_read_b64_tr_b16 v[124:125], %[c0] offset:8192\n\t"
      "ds_read_b64_tr_b16 v[126:127], %[c0] offset:8448\n\t"
      "ds_read_b64_tr_b16 v[128:129], %[c0] offset:9216\n\t"
      "ds_read_b64_tr_b16 v[130:131], %[c0] offset:9472\n\t"
      "ds_read_b64_tr_b16 v[132:133], %[c0] offset:10240\n\t"
      "ds_read_b64_tr_b16 v[134:135], %[c0] offset:10496\n\t"
      "ds_read_b64_tr_b16 v[136:137], %[c0] offset:11264\n\t"
      "ds_read_b64_tr_b16 v[138:139], %[c0] offset:11520\n\t"
      "ds_read_b64_tr_b16 v[140:141], %[c0] offset:12288\n\t"
      "ds_read_b64_tr_b16 v[142:143], %[c0] offset:12544\n\t"
      "s_waitcnt lgkmcnt(10)\n\t"
      "v_mfma_f32_32x32x16_bf16 v[56:71], v[40:43], v[120:123], 0\n\t"
      "ds_read_b64_tr_b16 v[120:121], %[c0] offset:13312\n\t"
      "ds_read_b64_tr_b16 v[122:123], %[c0] offset:13568\n\t"
      "s_waitcnt lgkmcnt(10)\n\t"
      "v_mfma_f32_32x32x16_bf16 v[56:71], v[40:43], v[124:127], v[56:71]\n\t"
      "ds_read_b64_tr_b16 v[124:125], %[b0] offset:7168\n\t"
      "ds_read_b64_tr_b16 v[126:127], %[b0] offset:7424\n\t"
      "buffer_load_dwordx4 %[sv], %[rs], %[ss] offen lds\n\t"
      "s_add_u32 m0, m0, 0x400\n\t"
      "s_add_u32 %[ss], %[ss], %[sst]\n\t"
      "s_waitcnt lgkmcnt(10)\n\t"
      "v_mfma_f32_32x32x16_bf16 v[56:71], v[40:43], v[128:131], v[56:71]\n\t"
      "ds_read_b64_tr_b16 v[128:129], %[b0] offset:8192\n\t"
      "ds_read_b64_tr_b16 v[130:131], %[b0] offset:8448\n\t"
      "s_waitcnt lgkmcnt(10)\n\t"
      "v_mfma_f32_32x32x16_bf16 v[56:71], v[40:43], v[132:135], v[56:71]\n\t"
      "ds_read_b64_tr_b16 v[132:133], %[b0] offset:9216\n\t"
      "ds_read_b64_tr_b16 v[134:135], %[b0] offset:9472\n\t"
      "s_waitcnt lgkmcnt(10)\n\t"
      "v_mfma_f32_32x32x16_bf16 v[56:71], v[40:43], v[136:139], v[56:71]\n\t"
      "ds_read_b64_tr_b16 v[136:137], %[b0] offset:10240\n\t"
      "ds_read_b64_tr_b16 v[138:139], %[b0] offset:10496\n\t"
      "s_waitcnt lgkmcnt(10)\n\t"
      "v_mfma_f32_32x32x16_bf16 v[56:71], v[40:43], v[140:143], v[56:71]\n\t"
      "ds_read_b64_tr_b16 v[140:141], %[b0] offset:11264\n\t"
      "ds_read_b64_tr_b16 v[142:143], %[b0] offset:11520\n\t"
      "buffer_load_dwordx4 %[sv], %[rs], %[ss] offen lds\n\t"
      "s_add_u32 m0, m0, 0x400\n\t"
      "s_add_u32 %[ss], %[ss], %[sst]\n\t"
      "s_waitcnt lgkmcnt(10)\n\t"
      "v_mfma_f32_32x32x16_bf16 v[56:71], v[40:43], v[120:123], v[56:71]\n\t"
      "ds_read_b64_tr_b16 v[120:121], %[b0] offset:12288\n\t"
      "ds_read_b64_tr_b16 v[122:123], %[b0] offset:12544\n\t"
      "s_waitcnt lgkmcnt(10)\n\t"
      "v_mfma_f32_32x32x16_bf16 v[88:103], v[144:147], v[124:127], 0\n\t"
      "ds_read_b64_tr_b16 v[124:125], %[b0] offset:13312\n\t"
      "ds_read_b64_tr_b16 v[126:127], %[b0] offset:13568\n\t"
      "s_waitcnt lgkmcnt(10)\n\t"
      "v_mfma_f32_32x32x16_bf16 v[88:103], v[148:151], v[128:131], v[88:103]\n\t"
      "ds_read_b64_tr_b16 v[128:129], %[b0] offset:48128\n\t"
      "ds_read_b64_tr_b16 v[130:131], %[b0] offset:48384\n\t"
      "s_waitcnt lgkmcnt(10)\n\t"
      "v_mfma_f32_32x32x16_bf16 v[88:103], v[152:155], v[132:135], v[88:103]\n\t"
      "ds_read_b64_tr_b16 v[132:133], %[b0] offset:49152\n\t"
      "ds_read_b64_tr_b16 v[134:135], %[b0] offset:49408\n\t"
      "buffer_load_dwordx4 %[sv], %[rs], %[ss] offen lds\n\t"
      "s_add_u32 m0, m0, 0x400\n\t"
      "s_add_u32 %[ss], %[ss], %[sst]\n\t"
      "s_waitcnt lgkmcnt(10)\n\t"
      "v_mfma_f32_32x32x16_bf16 v[88:103], v[156:159], v[136:139], v[88:103]\n\t"
      "ds_read_b64_tr_b16 v[136:137], %[b0] offset:50176\n\t"
      "ds_read_b64_tr_b16 v[138:139], %[b0] offset:50432\n\t"
      "s_waitcnt lgkmcnt(10)\n\t"
      "v_mfma_f32_32x32x16_bf16 v[88:103], v[160:163], v[140:143], v[88:103]\n\t"
      "ds_write_b32 v18, v56 offset:1024\n\t"
      "ds_read_b64_tr_b16 v[140:141], %[b0] offset:51200\n\t"
      "ds_read_b64_tr_b16 v[142:143], %[b0] offset:51456\n\t"
      "s_waitcnt lgkmcnt(11)\n\t"
      "v_mfma_f32_32x32x16_bf16 v[88:103], v[164:167], v[120:123], v[88:103]\n\t"
      "ds_read_b64_tr_b16 v[120:121], %[b0] offset:52224\n\t"
      "ds_read_b64_tr_b16 v[122:123], %[b0] offset:52480\n\t"
      "s_waitcnt lgkmcnt(11)\n\t"
      "v_mfma_f32_32x32x16_bf16 v[88:103], v[168:171], v[124:127], v[88:103]\n\t"
      "ds_read_b64_tr_b16 v[124:125], %[b0] offset:53248\n\t"
      "ds_read_b64_tr_b16 v[126:127], %[b0] offset:53504\n\t"
      "s_waitcnt lgkmcnt(11)\n\t"
      "v_mfma_f32_32x32x16_bf16 v[88:103], v[172:175], v[128:131], v[88:103]\n\t"
      "ds_read_b64_tr_b16 v[128:129], %[b0] offset:54272\n\t"
      "ds_read_b64_tr_b16 v[130:131], %[b0] offset:54528\n\t"
      "s_waitcnt lgkmcnt(11)\n\t"
      "v_mfma_f32_32x32x16_bf16 v[88:103], v[176:179], v[132:135], v[88:103]\n\t"
      "ds_read_b64_tr_b16 v[132:133], v16 offset:7168\n\t"
      "ds_read_b64_tr_b16 v[134:135], v16 offset:7424\n\t"
      "s_waitcnt lgkmcnt(11)\n\t"
      "v_mfma_f32_32x32x16_bf16 v[88:103], v[180:183], v[136:139], v[88:103]\n\t"
      "ds_read_b64_tr_b16 v[136:137], v16 offset:8192\n\t"
      "ds_read_b64_tr_b16 v[138:139], v16 offset:8448\n\t"
      "s_waitcnt lgkmcnt(10)\n\t"
      "v_mfma_f32_32x32x16_bf16 v[88:103], v[184:187], v[140:143], v[88:103]\n\t"
      "ds_read_b64_tr_b16 v[140:141], v16 offset:9216\n\t"
      "ds_read_b64_tr_b16 v[142:143], v16 offset:9472\n\t"
      "s_barrier\n\t"
      "ds_read_b128 v[32:35], v31 offset:1024\n\t"
      "s_waitcnt lgkmcnt(11)\n\t"
      "v_mfma_f32_32x32x16_bf16 v[88:103], v[188:191], v[120:123], v[88:103]\n\t"
      "ds_read_b64_tr_b16 v[120:121], v16 offset:10240\n\t"
      "ds_read_b64_tr_b16 v[122:123], v16 offset:10496\n\t"
      "s_waitcnt lgkmcnt(11)\n\t"
      "v_mfma_f32_32x32x16_bf16 v[88:103], v[192:195], v[124:127], v[88:103]\n\t"
      "ds_read_b64_tr_b16 v[124:125], v16 offset:11264\n\t"
      "ds_read_b64_tr_b16 v[126:127], v16 offset:11520\n\t"
      "s_waitcnt lgkmcnt(11)\n\t"
      "v_mfma_f32_32x32x16_bf16 v[88:103], v[196:199], v[128:131], v[88:103]\n\t"
      "ds_read_b64_tr_b16 v[128:129], v16 offset:12288\n\t"
      "ds_read_b64_tr_b16 v[130:131], v16 offset:12544\n\t"
      "s_waitcnt lgkmcnt(11)\n\t"
      "v_mfma_f32_32x32x16_bf16 v[88:103], v[200:203], v[132:135], v[88:103]\n\t"
      "ds_read_b64_tr_b16 v[132:133], v16 offset:13312\n\t"
      "ds_read_b64_tr_b16 v[134:135], v16 offset:13568\n\t"
      "s_waitcnt lgkmcnt(11)\n\t"
      "v_mfma_f32_32x32x16_bf16 v[88:103], v[204:207], v[136:139], v[88:103]\n\t"
      "ds_read_b64_tr_b16 v[136:137], v16 offset:48128\n\t"
      "ds_read_b64_tr_b16 v[138:139], v16 offset:48384\n\t"
      "s_waitcnt lgkmcnt(11)\n\t"
      "v_mfma_f32_32x32x16_bf16 v[88:103], v[208:211], v[140:143], v[88:103]\n\t"
      "ds_read_b64_tr_b16 v[140:141], v16 offset:49152\n\t"
      "ds_read_b64_tr_b16 v[142:143], v16 offset:49408\n\t"
      "s_waitcnt lgkmcnt(10)\n\t"
      "v_mfma_f32_32x32x16_bf16 v[88:103], v[212:215], v[120:123], v[88:103]\n\t"
      "ds_read_b64_tr_b16 v[120:121], v16 offset:50176\n\t"
      "ds_read_b64_tr_b16 v[122:123], v16 offset:50432\n\t"
      "v_add_f32 v32, v32, v33\n\t"
      "v_add_f32 v34, v34, v35\n\t"
      "s_waitcnt lgkmcnt(10)\n\t"
      "v_mfma_f32_32x32x16_bf16 v[88:103], v[216:219], v[124:127], v[88:103]\n\t"
      "ds_read_b64_tr_b16 v[124:125], v16 offset:51200\n\t"
      "ds_read_b64_tr_b16 v[126:127], v16 offset:51456\n\t"
      "v_add_f32 v32, v32, v34\n\t"
      "v_mul_f32 v32, v24, v32\n\t"
      "s_waitcnt lgkmcnt(10)\n\t"
      "v_mfma_f32_32x32x16_bf16 v[88:103], v[220:223], v[128:131], v[88:103]\n\t"
      "ds_read_b64_tr_b16 v[128:129], v16 offset:52224\n\t"
      "ds_read_b64_tr_b16 v[130:131], v16 offset:52480\n\t"
      "v_cvt_pk_bf16_f32 v33, v32, v26\n\t"
      "v_lshlrev_b32 v34, 16, v33\n\t"
      "s_waitcnt lgkmcnt(10)\n\t"
      "v_mfma_f32_32x32x16_bf16 v[88:103], v[224:227], v[132:135], v[88:103]\n\t"
      "ds_read_b64_tr_b16 v[132:133], v16 offset:53248\n\t"
      "ds_read_b64_tr_b16 v[134:135], v16 offset:53504\n\t"
      "v_sub_f32 v34, v32, v34\n\t"
      "v_cvt_pk_bf16_f32 v34, v34, 0\n\t"
      "s_waitcnt lgkmcnt(10)\n\t"
      "v_mfma_f32_32x32x16_bf16 v[88:103], v[228:231], v[136:139], v[88:103]\n\t"
      "ds_read_b64_tr_b16 v[136:137], v16 offset:54272\n\t"
      "ds_read_b64_tr_b16 v[138:139], v16 offset:54528\n\t"
      "v_mov_b32 v32, v33\n\t"
      "v_mov_b32 v35, 0\n\t"
      "s_waitcnt lgkmcnt(10)\n\t"
      "v_mfma_f32_32x32x16_bf16 v[88:103], v[232:235], v[140:143], v[88:103]\n\t"
      "buffer_store_dwordx4 v[32:35], %[l16], %[ws], %[weo] offen offset:2048 sc1\n\t"
      "s_waitcnt lgkmcnt(8)\n\t"
      "v_mfma_f32_32x32x16_bf16 v[88:103], v[236:239], v[120:123], v[88:103]\n\t"
      "s_waitcnt lgkmcnt(6)\n\t"
      "v_mfma_f32_32x32x16_bf16 v[88:103], v[240:243], v[124:127], v[88:103]\n\t"
      "s_waitcnt lgkmcnt(4)\n\t"
      "v_mfma_f32_32x32x16_bf16 v[88:103], v[244:247], v[128:131], v[88:103]\n\t"
      "s_waitcnt lgkmcnt(2)\n\t"
      "v_mfma_f32_32x32x16_bf16 v[88:103], v[248:251], v[132:135], v[88:103]\n\t"
      "s_waitcnt lgkmcnt(0)\n\t"
      "v_mfma_f32_32x32x16_bf16 v[88:103], v[252:255], v[136:139], v[88:103]\n\t"
      "v_mfma_f32_32x32x16_bf16 v[88:103], v[46:49], v[32:35], v[88:103]\n\t"
      "s_waitcnt vmcnt(13)\n\t"
      "ds_write_b64 v30, v[54:55] offset:14336\n\t"
      "ds_write_b64 v17, v[54:55] offset:14336\n\t"
      "s_waitcnt lgkmcnt(0)\n\t"
      "s_barrier\n\t"
      "ds_read_b64_tr_b16 v[120:121], %[c0] offset:14336\n\t"
      "ds_read_b64_tr_b16 v[122:123], %[c0] offset:14592\n\t"
      "ds_read_b64_tr_b16 v[124:125], %[c0] offset:15360\n\t"
      "ds_read_b64_tr_b16 v[126:127], %[c0] offset:15616\n\t"
      "ds_read_b64_tr_b16 v[128:129], %[c0] offset:16384\n\t"
      "ds_read_b64_tr_b16 v[130:131], %[c0] offset:16640\n\t"
      "ds_read_b64_tr_b16 v[132:133], %[c0] offset:17408\n\t"
      "ds_read_b64_tr_b16 v[134:135], %[c0] offset:17664\n\t"
      "ds_read_b64_tr_b16 v[136:137], %[c0] offset:18432\n\t"
      "ds_read_b64_tr_b16 v[138:139], %[c0] offset:18688\n\t"
      "ds_read_b64_tr_b16 v[140:141], %[c0] offset:19456\n\t"
      "ds_read_b64_tr_b16 v[142:143], %[c0] offset:19712\n\t"
      "s_waitcnt lgkmcnt(10)\n\t"
      "v_mfma_f32_32x32x16_bf16 v[72:87], v[40:43], v[120:123], 0\n\t"
      "ds_read_b64_tr_b16 v[120:121], %[c0] offset:20480\n\t"
      "ds_read_b64_tr_b16 v[122:123], %[c0] offset:20736\n\t"
      "s_waitcnt lgkmcnt(10)\n\t"
      "v_mfma_f32_32x32x16_bf16 v[72:87], v[40:43], v[124:127], v[72:87]\n\t"
      "ds_read_b64_tr_b16 v[124:125], %[b0] offset:14336\n\t"
      "ds_read_b64_tr_b16 v[126:127], %[b0] offset:14592\n\t"
      "s_waitcnt lgkmcnt(10)\n\t"
      "v_mfma_f32_32x32x16_bf16 v[72:87], v[40:43], v[128:131], v[72:87]\n\t"
      "ds_read_b64_tr_b16 v[128:129], %[b0] offset:15360\n\t"
      "ds_read_b64_tr_b16 v[130:131], %[b0] offset:15616\n\t"
      "s_waitcnt lgkmcnt(10)\n\t"
      "v_mfma_f32_32x32x16_bf16 v[72:87], v[40:43], v[132:135], v[72:87]\n\t"
      "ds_read_b64_tr_b16 v[132:133], %[b0] offset:16384\n\t"
      "ds_read_b64_tr_b16 v[134:135], %[b0] offset:16640\n\t"
      "v_mul_f32 v88, v88, v104\n\t"
      "v_mul_f32 v89, v89, v105\n\t"
      "v_mul_f32 v90, v90, v106\n\t"
      "s_waitcnt lgkmcnt(10)\n\t"
      "v_mfma_f32_32x32x16_bf16 v[72:87], v[40:43], v[136:139], v[72:87]\n\t"
      "ds_read_b64_tr_b16 v[136:137], %[b0] offset:17408\n\t"
      "ds_read_b64_tr_b16 v[138:139], %[b0] offset:17664\n\t"
      "v_mul_f32 v91, v91, v107\n\t"
      "v_mul_f32 v92, v92, v108\n\t"
      "v_mul_f32 v93, v93, v109\n\t"
      "s_waitcnt lgkmcnt(10)\n\t"
      "v_mfma_f32_32x32x16_bf16 v[72:87], v[40:43], v[140:143], v[72:87]\n\t"
      "ds_read_b64_tr_b16 v[140:141], %[b0] offset:18432\n\t"
      "ds_read_b64_tr_b16 v[142:143], %[b0] offset:18688\n\t"
      "v_mul_f32 v94, v94, v110\n\t"
      "v_mul_f32 v95, v95, v111\n\t"
      "v_mul_f32 v96, v96, v112\n\t"
      "s_waitcnt lgkmcnt(10)\n\t"
      "v_mfma_f32_32x32x16_bf16 v[72:87], v[40:43], v[120:123], v[72:87]\n\t"
      "ds_read_b64_tr_b16 v[120:121], %[b0] offset:19456\n\t"
      "ds_read_b64_tr_b16 v[122:123], %[b0] offset:19712\n\t"
      "v_mul_f32 v97, v97, v113\n\t"
      "v_mul_f32 v98, v98, v114\n\t"
      "v_mul_f32 v99, v99, v115\n\t"
      "s_waitcnt lgkmcnt(10)\n\t"
      "v_mfma_f32_32x32x16_bf16 v[56:71], v[124:127], v[144:147], 0\n\t"
      "ds_read_b64_tr_b16 v[124:125], %[b0] offset:20480\n\t"
      "ds_read_b64_tr_b16 v[126:127], %[b0] offset:20736\n\t"
      "v_mul_f32 v100, v100, v116\n\t"
      "v_mul_f32 v101, v101, v117\n\t"
      "v_mul_f32 v102, v102, v118\n\t"
      "s_waitcnt lgkmcnt(10)\n\t"
      "v_mfma_f32_32x32x16_bf16 v[56:71], v[128:131], v[148:151], v[56:71]\n\t"
      "ds_read_b64_tr_b16 v[128:129], %[b0] offset:55296\n\t"
      "ds_read_b64_tr_b16 v[130:131], %[b0] offset:55552\n\t"
      "v_mul_f32 v103, v103, v119\n\t"
      "v_cvt_pk_bf16_f32 v88, v88, v89\n\t"
      "v_cvt_pk_bf16_f32 v89, v90, v91\n\t"
      "s_waitcnt lgkmcnt(10)\n\t"
      "v_mfma_f32_32x32x16_bf16 v[56:71], v[132:135], v[152:155], v[56:71]\n\t"
      "ds_read_b64_tr_b16 v[132:133], %[b0] offset:56320\n\t"
      "ds_read_b64_tr_b16 v[134:135], %[b0] offset:56576\n\t"
      "v_cvt_pk_bf16_f32 v90, v92, v93\n\t"
      "v_cvt_pk_bf16_f32 v91, v94, v95\n\t"
      "v_cvt_pk_bf16_f32 v92, v96, v97\n\t"
      "s_waitcnt lgkmcnt(10)\n\t"
      "v_mfma_f32_32x32x16_bf16 v[56:71], v[136:139], v[156:159], v[56:71]\n\t"
      "ds_read_b64_tr_b16 v[136:137], %[b0] offset:57344\n\t"
      "ds_read_b64_tr_b16 v[138:139], %[b0] offset:57600\n\t"
      "v_cvt_pk_bf16_f32 v93, v98, v99\n\t"
      "v_cvt_pk_bf16_f32 v94, v100, v101\n\t"
      "v_cvt_pk_bf16_f32 v95, v102, v103\n\t"
      "s_waitcnt lgkmcnt(10)\n\t"
      "v_mfma_f32_32x32x16_bf16 v[56:71], v[140:143], v[160:163], v[56:71]\n\t"
      "ds_write_b32 v18, v72 offset:0\n\t"
      "ds_read_b64_tr_b16 v[140:141], %[b0] offset:58368\n\t"
      "ds_read_b64_tr_b16 v[142:143], %[b0] offset:58624\n\t"
      "buffer_store_dwordx4 v[88:91], %[l16], %[ws], %[ko] offen offset:2048 sc1\n\t"
      "buffer_store_dwordx4 v[92:95], %[l16], %[ws], %[ko] offen offset:3072 sc1\n\t"
      "s_waitcnt lgkmcnt(11)\n\t"
      "v_mfma_f32_32x32x16_bf16 v[56:71], v[120:123], v[164:167], v[56:71]\n\t"
      "ds_read_b64_tr_b16 v[120:121], %[b0] offset:59392\n\t"
      "ds_read_b64_tr_b16 v[122:123], %[b0] offset:59648\n\t"
      "s_waitcnt lgkmcnt(11)\n\t"
      "v_mfma_f32_32x32x16_bf16 v[56:71], v[124:127], v[168:171], v[56:71]\n\t"
      "ds_read_b64_tr_b16 v[124:125], %[b0] offset:60416\n\t"
      "ds_read_b64_tr_b16 v[126:127], %[b0] offset:60672\n\t"
      "s_waitcnt lgkmcnt(11)\n\t"
      "v_mfma_f32_32x32x16_bf16 v[56:71], v[128:131], v[172:175], v[56:71]\n\t"
      "ds_read_b64_tr_b16 v[128:129], %[b0] offset:61440\n\t"
      "ds_read_b64_tr_b16 v[130:131], %[b0] offset:61696\n\t"
      "s_waitcnt lgkmcnt(11)\n\t"
      "v_mfma_f32_32x32x16_bf16 v[56:71], v[132:135], v[176:179], v[56:71]\n\t"
      "ds_read_b64_tr_b16 v[132:133], v16 offset:14336\n\t"
      "ds_read_b64_tr_b16 v[134:135], v16 offset:14592\n\t"
      "s_waitcnt lgkmcnt(11)\n\t"
      "v_mfma_f32_32x32x16_bf16 v[56:71], v[136:139], v[180:183], v[56:71]\n\t"
      "ds_read_b64_tr_b16 v[136:137], v16 offset:15360\n\t"
      "ds_read_b64_tr_b16 v[138:139], v16 offset:15616\n\t"
      "s_waitcnt lgkmcnt(10)\n\t"
      "v_mfma_f32_32x32x16_bf16 v[56:71], v[140:143], v[184:187], v[56:71]\n\t"
      "ds_read_b64_tr_b16 v[140:141], v16 offset:16384\n\t"
      "ds_read_b64_tr_b16 v[142:143], v16 offset:16640\n\t"
      "s_barrier\n\t"
      "ds_read_b128 v[32:35], v31 offset:0\n\t"
      "s_waitcnt lgkmcnt(11)\n\t"
      "v_mfma_f32_32x32x16_bf16 v[56:71], v[120:123], v[188:191], v[56:71]\n\t"
      "ds_read_b64_tr_b16 v[120:121], v16 offset:17408\n\t"
      "ds_read_b64_tr_b16 v[122:123], v16 offset:17664\n\t"
      "s_waitcnt lgkmcnt(11)\n\t"
      "v_mfma_f32_32x32x16_bf16 v[56:71], v[124:127], v[192:195], v[56:71]\n\t"
      "ds_read_b64_tr_b16 v[124:125], v16 offset:18432\n\t"
      "ds_read_b64_tr_b16 v[126:127], v16 offset:18688\n\t"
      "s_waitcnt lgkmcnt(11)\n\t"
      "v_mfma_f32_32x32x16_bf16 v[56:71], v[128:131], v[196:199], v[56:71]\n\t"
      "ds_read_b64_tr_b16 v[128:129], v16 offset:19456\n\t"
      "ds_read_b64_tr_b16 v[130:131], v16 offset:19712\n\t"
      "s_waitcnt lgkmcnt(11)\n\t"
      "v_mfma_f32_32x32x16_bf16 v[56:71], v[132:135], v[200:203], v[56:71]\n\t"
      "ds_read_b64_tr_b16 v[132:133], v16 offset:20480\n\t"
      "ds_read_b64_tr_b16 v[134:135], v16 offset:20736\n\t"
      "s_waitcnt lgkmcnt(11)\n\t"
      "v_mfma_f32_32x32x16_bf16 v[56:71], v[136:139], v[204:207], v[56:71]\n\t"
      "ds_read_b64_tr_b16 v[136:137], v16 offset:55296\n\t"
      "ds_read_b64_tr_b16 v[138:139], v16 offset:55552\n\t"
      "s_waitcnt lgkmcnt(11)\n\t"
      "v_mfma_f32_32x32x16_bf16 v[56:71], v[140:143], v[208:211], v[56:71]\n\t"
      "ds_read_b64_tr_b16 v[140:141], v16 offset:56320\n\t"
      "ds_read_b64_tr_b16 v[142:143], v16 offset:56576\n\t"
      "s_waitcnt lgkmcnt(10)\n\t"
      "v_mfma_f32_32x32x16_bf16 v[56:71], v[120:123], v[212:215], v[56:71]\n\t"
      "ds_read_b64_tr_b16 v[120:121], v16 offset:57344\n\t"
      "ds_read_b64_tr_b16 v[122:123], v16 offset:57600\n\t"
      "v_add_f32 v32, v32, v33\n\t"
      "v_add_f32 v34, v34, v35\n\t"
      "s_waitcnt lgkmcnt(10)\n\t"
      "v_mfma_f32_32x32x16_bf16 v[56:71], v[124:127], v[216:219], v[56:71]\n\t"
      "ds_read_b64_tr_b16 v[124:125], v16 offset:58368\n\t"
      "ds_read_b64_tr_b16 v[126:127], v16 offset:58624\n\t"
      "v_add_f32 v32, v32, v34\n\t"
      "v_mul_f32 v32, v24, v32\n\t"
      "s_waitcnt lgkmcnt(10)\n\t"
      "v_mfma_f32_32x32x16_bf16 v[56:71], v[128:131], v[220:223], v[56:71]\n\t"
      "ds_read_b64_tr_b16 v[128:129], v16 offset:59392\n\t"
      "ds_read_b64_tr_b16 v[130:131], v16 offset:59648\n\t"
      "v_cvt_pk_bf16_f32 v33, v32, 0\n\t"
      "v_lshlrev_b32 v34, 16, v33\n\t"
      "s_waitcnt lgkmcnt(10)\n\t"
      "v_mfma_f32_32x32x16_bf16 v[56:71], v[132:135], v[224:227], v[56:71]\n\t"
      "ds_read_b64_tr_b16 v[132:133], v16 offset:60416\n\t"
      "ds_read_b64_tr_b16 v[134:135], v16 offset:60672\n\t"
      "v_sub_f32 v34, v32, v34\n\t"
      "v_cvt_pk_bf16_f32 v34, v34, 0\n\t"
      "s_waitcnt lgkmcnt(10)\n\t"
      "v_mfma_f32_32x32x16_bf16 v[56:71], v[136:139], v[228:231], v[56:71]\n\t"
      "ds_read_b64_tr_b16 v[136:137], v16 offset:61440\n\t"
      "ds_read_b64_tr_b16 v[138:139], v16 offset:61696\n\t"
      "v_mov_b32 v32, v33\n\t"
      "v_mov_b32 v35, 0\n\t"
      "s_waitcnt lgkmcnt(10)\n\t"
      "v_mfma_f32_32x32x16_bf16 v[56:71], v[140:143], v[232:235], v[56:71]\n\t"
      "buffer_store_dwordx4 v[32:35], %[l16], %[ws], %[weo] offen offset:0 sc1\n\t"
      "s_waitcnt lgkmcnt(8)\n\t"
      "v_mfma_f32_32x32x16_bf16 v[56:71], v[120:123], v[236:239], v[56:71]\n\t"
      "s_waitcnt lgkmcnt(6)\n\t"
      "v_mfma_f32_32x32x16_bf16 v[56:71], v[124:127], v[240:243], v[56:71]\n\t"
      "s_waitcnt lgkmcnt(4)\n\t"
      "v_mfma_f32_32x32x16_bf16 v[56:71], v[128:131], v[244:247], v[56:71]\n\t"
      "s_waitcnt lgkmcnt(2)\n\t"
      "v_mfma_f32_32x32x16_bf16 v[56:71], v[132:135], v[248:251], v[56:71]\n\t"
      "s_waitcnt lgkmcnt(0)\n\t"
      "v_mfma_f32_32x32x16_bf16 v[56:71], v[136:139], v[252:255], v[56:71]\n\t"
      "v_mfma_f32_32x32x16_bf16 v[56:71], v[32:35], v[46:49], v[56:71]\n\t"
      "s_nop 11\n\t"
      "v_pk_mul_f32 v[56:57], v[56:57], v[52:53] op_sel_hi:[1,0]\n\t"
      "v_pk_mul_f32 v[58:59], v[58:59], v[52:53] op_sel_hi:[1,0]\n\t"
      "v_pk_mul_f32 v[60:61], v[60:61], v[52:53] op_sel_hi:[1,0]\n\t"
      "v_pk_mul_f32 v[62:63], v[62:63], v[52:53] op_sel_hi:[1,0]\n\t"
      "v_pk_mul_f32 v[64:65], v[64:65], v[52:53] op_sel_hi:[1,0]\n\t"
      "v_pk_mul_f32 v[66:67], v[66:67], v[52:53] op_sel_hi:[1,0]\n\t"
      "v_pk_mul_f32 v[68:69], v[68:69], v[52:53] op_sel_hi:[1,0]\n\t"
      "v_pk_mul_f32 v[70:71], v[70:71], v[52:53] op_sel_hi:[1,0]\n\t"
      "v_pk_mul_f32 v[32:33], v[56:57], v[56:57]\n\t"
      "v_pk_mul_f32 v[34:35], v[58:59], v[58:59]\n\t"
      "v_pk_fma_f32 v[32:33], v[60:61], v[60:61], v[32:33]\n\t"
      "v_pk_fma_f32 v[34:35], v[62:63], v[62:63], v[34:35]\n\t"
      "v_pk_fma_f32 v[32:33], v[64:65], v[64:65], v[32:33]\n\t"
      "v_pk_fma_f32 v[34:35], v[66:67], v[66:67], v[34:35]\n\t"
      "v_pk_fma_f32 v[32:33], v[68:69], v[68:69], v[32:33]\n\t"
      "v_pk_fma_f32 v[34:35], v[70:71], v[70:71], v[34:35]\n\t"
      "v_pk_add_f32 v[32:33], v[32:33], v[34:35]\n\t"
      "v_add_f32 v32, v32, v33\n\t"
      "v_cvt_pk_bf16_f32 v56, v56, v57\n\t"
      "v_cvt_pk_bf16_f32 v57, v58, v59\n\t"
      "v_cvt_pk_bf16_f32 v58, v60, v61\n\t"
      "v_cvt_pk_bf16_f32 v59, v62, v63\n\t"
      "v_cvt_pk_bf16_f32 v60, v64, v65\n\t"
      "v_cvt_pk_bf16_f32 v61, v66, v67\n\t"
      "v_cvt_pk_bf16_f32 v62, v68, v69\n\t"
      "v_cvt_pk_bf16_f32 v63, v70, v71\n\t"
      "buffer_store_dwordx4 v[56:59], %[l16], %[ws], %[qo] offen sc1\n\t"
      "buffer_store_dwordx4 v[60:63], %[l16], %[ws], %[qo] offen offset:1024 sc1\n\t"
      "v_mov_b32 v37, v32\n\t"
      "v_mov_b32 v38, v36\n\t"
      "v_mov_b32 v39, v37\n\t"
      "s_nop 0\n\t"
      "v_permlane32_swap_b32 v36, v38\n\t"
      "v_permlane32_swap_b32 v37, v39\n\t"
      "v_add_f32 v36, v36, v38\n\t"
      "v_add_f32 v37, v37, v39\n\t"
      "s_nop 0\n\t"
      "v_max_f32_dpp v36, v36, v36 quad_perm:[1,0,3,2] row_mask:0xf bank_mask:0xf\n\t"
      "v_max_f32_dpp v37, v37, v37 quad_perm:[1,0,3,2] row_mask:0xf bank_mask:0xf\n\t"
      "s_nop 0\n\t"
      "v_max_f32_dpp v36, v36, v36 quad_perm:[2,3,0,1] row_mask:0xf bank_mask:0xf\n\t"
      "v_max_f32_dpp v37, v37, v37 quad_perm:[2,3,0,1] row_mask:0xf bank_mask:0xf\n\t"
      "s_nop 0\n\t"
      "v_max_f32_dpp v36, v36, v36 row_half_mirror row_mask:0xf bank_mask:0xf\n\t"
      "v_max_f32_dpp v37, v37, v37 row_half_mirror row_mask:0xf bank_mask:0xf\n\t"
      "s_nop 0\n\t"
      "v_max_f32_dpp v36, v36, v36 row_mirror row_mask:0xf bank_mask:0xf\n\t"
      "v_max_f32_dpp v37, v37, v37 row_mirror row_mask:0xf bank_mask:0xf\n\t"
      "v_mov_b32 v38, v36\n\t"
      "v_mov_b32 v39, v37\n\t"
      "s_nop 0\n\t"
      "v_permlane16_swap_b32 v36, v38\n\t"
      "v_permlane16_swap_b32 v37, v39\n\t"
      "v_max_f32 v36, v36, v38\n\t"
      "v_max_f32 v37, v37, v39\n\t"
      "ds_write_b64 v27, v[36:37]\n\t"
      "s_waitcnt vmcnt(6)\n\t"
      : [ss] "+s"(ss), [wo] "+s"(wo), [tmp] "=&s"(tmp), "+{v[144:175]}"(xr0), "+{v[176:207]}"(xr1), "+{v[208:239]}"(xr2), "+{v[240:255]}"(xr3)
      : [l16] "v"(c.lane16), [ws] "s"(c.ws), [b0] "v"(b0), [ko] "s"(ko), [qo] "s"(qo), [x0] "v"(xa0), [c0] "v"(c0), "{v[16:23]}"(aux0), "{v[24:31]}"(aux1), [weo] "s"(weo), [sv] "v"(sv), [rs] "s"(rs), [sst] "s"(sst), [wv] "v"(wv), [rw] "s"(rqkv), [wm] "s"(wm), [r0] "v"(ra), [xo] "s"(xo), [rj] "s"(rj), [xw] "v"(xw), [pb1a] "s"(p.b1), [pb2a] "s"(p.b2), [poa] "s"(p.out)
      : "v32", "v33", "v34", "v35", "v36", "v37", "v38", "v39", "v40", "v41", "v42", "v43", "v44", "v45", "v46", "v47", "v48", "v49", "v50", "v51", "v52", "v53", "v54", "v55", "v56", "v57", "v58", "v59", "v60", "v61", "v62", "v63", "v64", "v65", "v66", "v67", "v68", "v69", "v70", "v71", "v72", "v73", "v74", "v75", "v76", "v77", "v78", "v79", "v80", "v81", "v82", "v83", "v84", "v85", "v86", "v87", "v88", "v89", "v90", "v91", "v92", "v93", "v94", "v95", "v96", "v97", "v98", "v99", "v100", "v101", "v102", "v103", "v104", "v105", "v106", "v107", "v108", "v109", "v110", "v111", "v112", "v113", "v114", "v115", "v116", "v117", "v118", "v119", "v120", "v121", "v122", "v123", "v124", "v125", "v126", "v127", "v128", "v129", "v130", "v131", "v132", "v133", "v134", "v135", "v136", "v137", "v138", "v139", "v140", "v141", "v142", "v143", "m0", "memory", "scc", ACC_FILE);
      asm volatile("" :: "v"(pfc));
    }
    const unsigned trl = 4 * 64 * c.half + trc;
    if (c.w == 3) {
      if (p0 < 14) {
#pragma unroll
        for (int i = 0; i < 6; ++i) {
          const unsigned f = p0 * 6 + i, t = f / 28, ks = f % 28;
          stx(trfrag(c.lds0 + (ks / 7) * WREG + ((t + 2) % 3) * 7168 + (ks % 7) * 1024 + trl), c.ws, c.lane16, WS_WQF + (h0 * 84 + f) * 1024);
        }
      }
    } else if (h0 < 14) {
      const unsigned dst = c.w == 0 ? WS_W1F + (h0 * 112 + p0 * 7) * 1024 : c.w == 1 ? WS_W2F + (h0 * 112 + p0 * 7) * 1024 : WS_WOF + (h0 * 32 + p0 * 2) * 1024;
#pragma unroll
      for (int i = 0; i < 7; ++i) {
        const unsigned ks = p0 * 7 + i, f = c.w == 1 ? ((ks >> 2) & 1) * 56 + (ks >> 3) * 4 + (ks & 3) - p0 * 7 : i;
        if (i < 2 || c.w != 2) stx(trfrag(reg + L_STG + trl + i * 1024), c.ws, c.lane16, U(dst + f * 1024));
      }
    }
    asm volatile(
      "v_mov_b32 v160, 0\n\t"
      "v_mov_b32 v161, 0\n\t"
      "v_mov_b32 v162, 0\n\t"
      "v_mov_b32 v163, 0\n\t"
      "v_mov_b32 v164, 0\n\t"
      "v_mov_b32 v165, 0\n\t"
      "v_mov_b32 v166, 0\n\t"
      "v_mov_b32 v167, 0\n\t"
      "v_mov_b32 v168, 0\n\t"
      "v_mov_b32 v169, 0\n\t"
      "v_mov_b32 v170, 0\n\t"
      "v_mov_b32 v171, 0\n\t"
      "v_mov_b32 v172, 0\n\t"
      "v_mov_b32 v173, 0\n\t"
      "v_mov_b32 v174, 0\n\t"
      "v_mov_b32 v175, 0\n\t"
      "v_mov_b32 v176, 0\n\t"
      "v_mov_b32 v177, 0\n\t"
      "v_mov_b32 v178, 0\n\t"
      "v_mov_b32 v179, 0\n\t"
      "v_mov_b32 v180, 0\n\t"
      "v_mov_b32 v181, 0\n\t"
      "v_mov_b32 v182, 0\n\t"
      "v_mov_b32 v183, 0\n\t"
      "v_mov_b32 v184, 0\n\t"
      "v_mov_b32 v185, 0\n\t"
      "v_mov_b32 v186, 0\n\t"
      "v_mov_b32 v187, 0\n\t"
      "v_mov_b32 v188, 0\n\t"
      "v_mov_b32 v189, 0\n\t"
      "v_mov_b32 v190, 0\n\t"
      "v_mov_b32 v191, 0\n\t"
      "v_mov_b32 v192, 0\n\t"
      "v_mov_b32 v193, 0\n\t"
      "v_mov_b32 v194, 0\n\t"
      "v_mov_b32 v195, 0\n\t"
      "v_mov_b32 v196, 0\n\t"
      "v_mov_b32 v197, 0\n\t"
      "v_mov_b32 v198, 0\n\t"
      "v_mov_b32 v199, 0\n\t"
      "v_mov_b32 v200, 0\n\t"
      "v_mov_b32 v201, 0\n\t"
      "v_mov_b32 v202, 0\n\t"
      "v_mov_b32 v203, 0\n\t"
      "v_mov_b32 v204, 0\n\t"
      "v_mov_b32 v205, 0\n\t"
      "v_mov_b32 v206, 0\n\t"
      "v_mov_b32 v207, 0\n\t"
      "v_mov_b32 v208, 0\n\t"
      "v_mov_b32 v209, 0\n\t"
      "v_mov_b32 v210, 0\n\t"
      "v_mov_b32 v211, 0\n\t"
      "v_mov_b32 v212, 0\n\t"
      "v_mov_b32 v213, 0\n\t"
      "v_mov_b32 v214, 0\n\t"
      "v_mov_b32 v215, 0\n\t"
      "v_mov_b32 v216, 0\n\t"
      "v_mov_b32 v217, 0\n\t"
      "v_mov_b32 v218, 0\n\t"
      "v_mov_b32 v219, 0\n\t"
      "v_mov_b32 v220, 0\n\t"
      "v_mov_b32 v221, 0\n\t"
      "v_mov_b32 v222, 0\n\t"
      "v_mov_b32 v223, 0\n\t"
      : "={v[160:191]}"(zo0), "={v[192:223]}"(zo1)
      :
      : "memory", ACC_FILE);
    publish_kv(c, 0, h0, p0, L_MISC0);
    {
      const u32x4 e = {~0u, ~0u, ~0u, ~0u};
      const unsigned rh = c.c >= 7 ? 1 : 0, c7 = c.c - 7 * rh, m5 = 2 * rh + (c.w >> 1), n5 = 2 * c7 + (c.w & 1), sv = c.half ? OOB : c.l31 * 8;
      const unsigned o = O_OF + ((c.c * 4 + c.w) * 32 + 2 * c.r) * 1024, x2 = act ? O_X2F + ((c.r * 4 + c.w) * 28 + 2 * c.c) * 1024 : OOB;
      const unsigned s2 = act ? O_ST2 + ((c.r * 14 + c.c) * 128 + c.w * 32) * 8 : OOB;
#pragma unroll
      for (int l = 0; l < 2; ++l) {
        const unsigned lb = WS_L0 + l * LSTRIDE;
        stx(e, c.ws, c.lane16, U(lb + o)); stx(e, c.ws, c.lane16 + 1024, U(lb + o));
        stx(e, c.ws, c.lane16, U(lb + x2)); stx(e, c.ws, c.lane16 + 1024, U(lb + x2));
        __builtin_amdgcn_raw_buffer_store_b64(u32x2{~0u, ~0u}, c.ws, sv, U(lb + s2), SC1);
      }
      const unsigned x = act ? WS_L0 + O_XF + ((c.r * 4 + m5) * 28 + 2 * n5) * 1024 : OOB;
      stx(e, c.ws, c.lane16, U(x)); stx(e, c.ws, c.lane16 + 1024, U(x));
      __builtin_amdgcn_raw_buffer_store_b64(u32x2{~0u, ~0u}, c.ws, sv, U(act ? WS_L0 + O_ST + ((c.r * 14 + n5) * 128 + m5 * 32) * 8 : OOB), SC1);
    }
  }

#pragma unroll 1
  for (int layer = 0; layer < 2; ++layer) {
    const unsigned lb = WS_L0 + layer * LSTRIDE;
    const int ph = layer * 5;
    refresh(c);
    {
      const unsigned hh = c.r, pr = c.c;
      const unsigned kvb = lb + O_KVF + (hh * 64 + c.w * 16) * 4096, qb = lb + O_QF + (hh * 16 + pr) * 4 * 2048;
      const unsigned c7 = c.c >= 14 ? 0u : c.c >= 7 ? c.c - 7 : c.c;
      const unsigned q0 = U(qb + (c.w ^ 1) * 2048), q1 = U(qb + (c.w ^ 2) * 2048), q2 = U(qb + (c.w ^ 3) * 2048), q3 = U(qb + c.w * 2048);
      const unsigned hm = c.half ? 0xffffu : 0u;
      unsigned kvo = U(kvb), w1o = U(WS_W1F + ((act ? c.c : 0u) * 112 + c.w * 28) * 1024);
      unsigned w2o = U(layer == 0 ? WS_W2F + ((2 * c7 + (c.w & 1)) * 112 + 14 * c.w) * 1024 : 0u), cnt = U(layer ? 3u : 0u);
      const unsigned w2p = U(WS_W2F + ((2 * c7 + 1 - (c.w & 1)) * 112 + 14 * c.w) * 1024);
      const unsigned m0b = U(c.lds0 + L_W1 + c.w * TILEB), oo = U(lb + O_OF + ((pr * 4 + c.w) * 32 + 2 * hh) * 1024), idx = 0xdddd;
      const unsigned pfv = c.lane * 128, pfo = U(act ? WS_WOF + c.c * 32768 + c.w * 8192 : OOB);
      const u64 tg = tag_of(c, 12);
      const unsigned tl = (unsigned)tg, th = (unsigned)(tg >> 32), z0 = c.lane < 8 ? c.lane16 : OOB, co = U(c.w == 0 ? line_of(12, c.r) + c.c * FS : OOB);
      RED_ARGS
      asm volatile("" :: "s"(tl), "s"(th), "v"(z0), "s"(co), "s"(w2p), "s"(cnt));
      asm volatile("" :: "s"(q0), "s"(q1), "s"(q2), "s"(q3), "v"(hm), "s"(kvo), "s"(w1o), "s"(w2o), "s"(m0b), "s"(oo), "v"(idx), "v"(pfv), "s"(pfo),
                   "v"(wr0), "v"(wr1), "v"(wr2), "v"(rd), "v"(c.lane16));
      {
        asm volatile("" : "={v[224:255]}"(warm));
        const unsigned q4 = c.lane >> 4, line = q4 == 0 ? hh : q4 == 1 ? c.c : 2 * c7 + q4 - 2;
        if (c.w == 1) {
          const unsigned mw = c.lds0 + L_SCR + ((min(c.lane, 3u) ^ 1) * 3) * 4096;
          poll(c, warm, line_of(ph, line) + (c.lane & 15) * FS, c.lane < (layer == 0 ? 64u : 16u), tag_of(c, ph));
          unsigned t, a;
    asm volatile(
      "v_readlane_b32 %[t], v248, %[pr]\n\t"
      "v_bfe_u32 %[a], v248, 8, 8\n\t"
      "s_nop 1\n\t"
      "v_max_u32_dpp %[a], %[a], %[a] quad_perm:[1,0,3,2] row_mask:0xf bank_mask:0xf\n\t"
      "s_nop 1\n\t"
      "v_max_u32_dpp %[a], %[a], %[a] quad_perm:[2,3,0,1] row_mask:0xf bank_mask:0xf\n\t"
      "s_nop 1\n\t"
      "v_max_u32_dpp %[a], %[a], %[a] row_half_mirror row_mask:0xf bank_mask:0xf\n\t"
      "s_nop 1\n\t"
      "v_max_u32_dpp %[a], %[a], %[a] row_mirror row_mask:0xf bank_mask:0xf\n\t"
      "s_and_b32 %[t], %[t], 0xff\n\t"
      "s_add_u32 %[t], %[t], 920\n\t"
      "s_lshl_b32 %[t], %[t], 20\n\t"
      "v_add_u32 %[a], 920, %[a]\n\t"
      "v_lshlrev_b32 %[a], 20, %[a]\n\t"
      "v_mul_f32 %[a], %[t], %[a]\n\t"
      "s_nop 0\n\t"
      "v_readfirstlane_b32 %[t], %[a]\n\t"
      "s_nop 1\n\t"
      "v_mov_b32 %[a], %[t]\n\t"
      "ds_write_b32 %[mw], %[a]\n\t"
      : [t] "=&s"(t), [a] "=&v"(a)
      : "{v[224:255]}"(warm), [pr] "s"(U(pr)), [mw] "v"(mw)
      : "scc", "memory", ACC_FILE);
        }
        bar();
      }
      u32x32 Qr; u32x8 K0, V0;
      unsigned ms, mb;
    asm volatile(
      "buffer_load_dwordx4 v[64:67], %[l16], %[ws], %[kv] offen offset:0 sc1\n\t"
      "buffer_load_dwordx4 v[32:35], %[l16], %[ws], %[q0] offen offset:0 sc1\n\t"
      "ds_read_b32 v96, %[wr0]\n\t"
      "buffer_load_dwordx4 v[68:71], %[l16], %[ws], %[kv] offen offset:1024 sc1\n\t"
      "buffer_load_dwordx4 v[36:39], %[l16], %[ws], %[q0] offen offset:1024 sc1\n\t"
      "buffer_load_dwordx4 v[40:43], %[l16], %[ws], %[q1] offen offset:0 sc1\n\t"
      "buffer_load_dwordx4 v[44:47], %[l16], %[ws], %[q1] offen offset:1024 sc1\n\t"
      "s_waitcnt lgkmcnt(0)\n\t"
      "v_readfirstlane_b32 %[ms], v96\n\t"
      "buffer_load_dwordx4 v[48:51], %[l16], %[ws], %[q2] offen offset:0 sc1\n\t"
      "buffer_load_dwordx4 v[52:55], %[l16], %[ws], %[q2] offen offset:1024 sc1\n\t"
      "v_cvt_pk_bf16_f32 v96, -%[ms], 0\n\t"
      "buffer_load_dwordx4 v[80:83], %[l16], %[ws], %[kv] offen offset:2048 sc1\n\t"
      "buffer_load_dwordx4 v[84:87], %[l16], %[ws], %[kv] offen offset:3072 sc1\n\t"
      "v_and_b32 %[mb], %[hm], v96\n\t"
      "buffer_load_dwordx4 v[56:59], %[l16], %[ws], %[q3] offen offset:0 sc1\n\t"
      "buffer_load_dwordx4 v[60:63], %[l16], %[ws], %[q3] offen offset:1024 sc1\n\t"
      : "=&{v[32:63]}"(Qr), "=&{v[64:71]}"(K0), "=&{v[80:87]}"(V0), [ms] "=&s"(ms), [mb] "=&v"(mb)
      : [l16] "v"(c.lane16), [ws] "s"(c.ws), [kv] "s"(kvo), [wr0] "v"(wr0), [hm] "v"(hm), [q0] "s"(q0), [q1] "s"(q1), [q2] "s"(q2), [q3] "s"(q3)
      : "v96", "memory", ACC_FILE);
      if (!(__uint_as_float(ms) <= 48.f)) {
        drain();
        asm volatile("" : "+{v[32:63]}"(Qr));
        float mx[4];
#pragma unroll
        for (int m = 0; m < 4; ++m) mx[m] = -INFINITY;
#pragma unroll 1
        for (int t = 0; t < 16; ++t) {
          const u32x4 k0 = ldx(c.ws, c.lane16, kvb + t * 4096), k1 = ldx(c.ws, c.lane16 + 1024, kvb + t * 4096);
#pragma unroll
          for (int m = 0; m < 4; ++m) {
            f32x16 s;
            asm volatile("s_nop 1\n\tv_mfma_f32_32x32x16_bf16 %0, %1, %2, 0\n\tv_mfma_f32_32x32x16_bf16 %0, %3, %4, %0\n\ts_nop 15"
                         : "=&v"(s) : "v"(k0), "v"(u32x4{Qr[8 * m], Qr[8 * m + 1], Qr[8 * m + 2], Qr[8 * m + 3]}), "v"(k1), "v"(u32x4{Qr[8 * m + 4], Qr[8 * m + 5], Qr[8 * m + 6], Qr[8 * m + 7]}));
#pragma unroll
            for (int i = 0; i < 16; ++i) mx[m] = fmaxf(mx[m], s[i]);
          }
        }
        LAS float* ex = (LAS float*)(c.lds0 + L_SCR + 4096);
#pragma unroll
        for (int m = 0; m < 4; ++m) { mx[m] = xhalf_max(mx[m]); ex[(c.w * 4 + (c.w ^ ((m + 1) & 3))) * 32 + c.l31] = mx[m]; }
        bar();
#pragma unroll
        for (int m = 0; m < 4; ++m) {
          const unsigned qt = c.w ^ ((m + 1) & 3);
          const float t = -fmaxf(fmaxf(ex[qt * 32 + c.l31], ex[(4 + qt) * 32 + c.l31]), fmaxf(ex[(8 + qt) * 32 + c.l31], ex[(12 + qt) * 32 + c.l31]));
          const unsigned h = pk2(t, 0.f) & 0xffffu; const float r1 = t - blo(h);
          const unsigned l = pk2(r1, 0.f) & 0xffffu; const float r2 = r1 - blo(l);
          if (c.half) { Qr[8 * m + 6] = h | (l << 16); Qr[8 * m + 7] = pk2(r2, 0.f) & 0xffffu; }
        }
        bar();
        mb = 0;
      }
    asm volatile(
      "s_mov_b32 m0, %[m0b]\n\t"
      "s_add_u32 %[kv], %[kv], 0x1000\n\t"
      "s_waitcnt vmcnt(8)\n\t"
      "v_or_b32 v38, v38, %[mb]\n\t"
      "v_mfma_f32_32x32x16_bf16 v[96:111], v[64:67], v[32:35], 0\n\t"
      "buffer_load_dwordx4 v[72:75], %[l16], %[ws], %[kv] offen offset:0 sc1\n\t"
      "v_mfma_f32_32x32x16_bf16 v[96:111], v[68:71], v[36:39], v[96:111]\n\t"
      "buffer_load_dwordx4 v[76:79], %[l16], %[ws], %[kv] offen offset:1024 sc1\n\t"
      "s_waitcnt vmcnt(8)\n\t"
      "v_or_b32 v46, v46, %[mb]\n\t"
      "s_nop 7\n\t"
      "v_mfma_f32_32x32x16_bf16 v[112:127], v[64:67], v[40:43], 0\n\t"
      "buffer_load_dwordx4 v[88:91], %[l16], %[ws], %[kv] offen offset:2048 sc1\n\t"
      "v_exp_f32 v96, v96\n\t"
      "v_exp_f32 v97, v97\n\t"
      "v_exp_f32 v98, v98\n\t"
      "v_exp_f32 v99, v99\n\t"
      "v_mfma_f32_32x32x16_bf16 v[112:127], v[68:71], v[44:47], v[112:127]\n\t"
      "buffer_load_dwordx4 v[92:95], %[l16], %[ws], %[kv] offen offset:3072 sc1\n\t"
      "s_add_u32 %[kv], %[kv], 0x1000\n\t"
      "v_exp_f32 v100, v100\n\t"
      "v_exp_f32 v101, v101\n\t"
      "v_exp_f32 v102, v102\n\t"
      "v_exp_f32 v103, v103\n\t"
      "buffer_load_dwordx4 v[224:227], %[l16], %[ws], %[kv] offen offset:0 sc1\n\t"
      "v_exp_f32 v104, v104\n\t"
      "v_exp_f32 v105, v105\n\t"
      "v_exp_f32 v106, v106\n\t"
      "v_exp_f32 v107, v107\n\t"
      "buffer_load_dwordx4 v[228:231], %[l16], %[ws], %[kv] offen offset:1024 sc1\n\t"
      "s_waitcnt vmcnt(10)\n\t"
      "v_or_b32 v54, v54, %[mb]\n\t"
      "v_exp_f32 v108, v108\n\t"
      "v_exp_f32 v109, v109\n\t"
      "v_exp_f32 v110, v110\n\t"
      "v_exp_f32 v111, v111\n\t"
      "v_mfma_f32_32x32x16_bf16 v[128:143], v[64:67], v[48:51], 0\n\t"
      "buffer_load_dwordx4 v[240:243], %[l16], %[ws], %[kv] offen offset:2048 sc1\n\t"
      "v_exp_f32 v112, v112\n\t"
      "v_exp_f32 v113, v113\n\t"
      "v_exp_f32 v114, v114\n\t"
      "v_exp_f32 v115, v115\n\t"
      "v_mfma_f32_32x32x16_bf16 v[128:143], v[68:71], v[52:55], v[128:143]\n\t"
      "buffer_load_dwordx4 v[244:247], %[l16], %[ws], %[kv] offen offset:3072 sc1\n\t"
      "s_add_u32 %[kv], %[kv], 0x1000\n\t"
      "s_waitcnt vmcnt(10)\n\t"
      "v_exp_f32 v116, v116\n\t"
      "v_exp_f32 v117, v117\n\t"
      "v_exp_f32 v118, v118\n\t"
      "v_exp_f32 v119, v119\n\t"
      "v_smfmac_f32_32x32x32_bf16 v[160:175], v[80:83], v[96:103], %[idx]\n\t"
      "buffer_load_dwordx4 %[l16], %[ws], %[w1o] offen offset:0 sc1 lds\n\t"
      "v_exp_f32 v120, v120\n\t"
      "v_exp_f32 v121, v121\n\t"
      "v_exp_f32 v122, v122\n\t"
      "v_exp_f32 v123, v123\n\t"
      "v_smfmac_f32_32x32x32_bf16 v[160:175], v[84:87], v[104:111], %[idx]\n\t"
      "buffer_load_dwordx4 v[232:235], %[l16], %[ws], %[kv] offen offset:0 sc1\n\t"
      "s_waitcnt vmcnt(10)\n\t"
      "v_or_b32 v62, v62, %[mb]\n\t"
      "v_exp_f32 v124, v124\n\t"
      "v_exp_f32 v125, v125\n\t"
      "v_exp_f32 v126, v126\n\t"
      "v_exp_f32 v127, v127\n\t"
      "v_mfma_f32_32x32x16_bf16 v[144:159], v[64:67], v[56:59], 0\n\t"
      "buffer_load_dwordx4 v[236:239], %[l16], %[ws], %[kv] offen offset:1024 sc1\n\t"
      "v_exp_f32 v128, v128\n\t"
      "v_exp_f32 v129, v129\n\t"
      "v_exp_f32 v130, v130\n\t"
      "v_exp_f32 v131, v131\n\t"
      "v_mfma_f32_32x32x16_bf16 v[144:159], v[68:71], v[60:63], v[144:159]\n\t"
      "buffer_load_dwordx4 %[l16], %[ws], %[w1o] offen offset:1024 sc1 lds\n\t"
      "v_exp_f32 v132, v132\n\t"
      "v_exp_f32 v133, v133\n\t"
      "v_exp_f32 v134, v134\n\t"
      "v_exp_f32 v135, v135\n\t"
      "v_smfmac_f32_32x32x32_bf16 v[176:191], v[80:83], v[112:119], %[idx]\n\t"
      "buffer_load_dwordx4 v[248:251], %[l16], %[ws], %[kv] offen offset:2048 sc1\n\t"
      "v_exp_f32 v136, v136\n\t"
      "v_exp_f32 v137, v137\n\t"
      "v_exp_f32 v138, v138\n\t"
      "v_exp_f32 v139, v139\n\t"
      "v_smfmac_f32_32x32x32_bf16 v[176:191], v[84:87], v[120:127], %[idx]\n\t"
      "buffer_load_dwordx4 v[252:255], %[l16], %[ws], %[kv] offen offset:3072 sc1\n\t"
      "s_add_u32 %[kv], %[kv], 0x1000\n\t"
      "s_waitcnt vmcnt(12)\n\t"
      "v_exp_f32 v140, v140\n\t"
      "v_exp_f32 v141, v141\n\t"
      "v_exp_f32 v142, v142\n\t"
      "v_exp_f32 v143, v143\n\t"
      "v_mfma_f32_32x32x16_bf16 v[96:111], v[72:75], v[32:35], 0\n\t"
      "buffer_load_dwordx4 %[l16], %[ws], %[w1o] offen offset:2048 sc1 lds\n\t"
      "v_exp_f32 v144, v144\n\t"
      "v_exp_f32 v145, v145\n\t"
      "v_exp_f32 v146, v146\n\t"
      "v_exp_f32 v147, v147\n\t"
      "v_mfma_f32_32x32x16_bf16 v[96:111], v[76:79], v[36:39], v[96:111]\n\t"
      "buffer_load_dwordx4 v[64:67], %[l16], %[ws], %[kv] offen offset:0 sc1\n\t"
      "v_exp_f32 v148, v148\n\t"
      "v_exp_f32 v149, v149\n\t"
      "v_exp_f32 v150, v150\n\t"
      "v_exp_f32 v151, v151\n\t"
      "v_smfmac_f32_32x32x32_bf16 v[192:207], v[80:83], v[128:135], %[idx]\n\t"
      "buffer_load_dwordx4 v[68:71], %[l16], %[ws], %[kv] offen offset:1024 sc1\n\t"
      "v_exp_f32 v152, v152\n\t"
      "v_exp_f32 v153, v153\n\t"
      "v_exp_f32 v154, v154\n\t"
      "v_exp_f32 v155, v155\n\t"
      "v_smfmac_f32_32x32x32_bf16 v[192:207], v[84:87], v[136:143], %[idx]\n\t"
      "buffer_load_dwordx4 %[l16], %[ws], %[w1o] offen offset:3072 sc1 lds\n\t"
      "s_add_u32 m0, m0, 0x1000\n\t"
      "s_add_u32 %[w1o], %[w1o], 0x1000\n\t"
      "v_exp_f32 v156, v156\n\t"
      "v_exp_f32 v157, v157\n\t"
      "v_exp_f32 v158, v158\n\t"
      "v_exp_f32 v159, v159\n\t"
      "s_waitcnt vmcnt(12)\n\t"
      "s_cmp_eq_u32 %[w2o], 0\n\t"
      "s_cbranch_scc1 2f\n\t"
      "v_mfma_f32_32x32x16_bf16 v[112:127], v[72:75], v[40:43], 0\n\t"
      "buffer_load_dwordx4 a[0:3], %[l16], %[ws], %[w2o] offen offset:0 sc1\n\t"
      "v_exp_f32 v96, v96\n\t"
      "v_exp_f32 v97, v97\n\t"
      "v_exp_f32 v98, v98\n\t"
      "v_exp_f32 v99, v99\n\t"
      "v_mfma_f32_32x32x16_bf16 v[112:127], v[76:79], v[44:47], v[112:127]\n\t"
      "buffer_load_dwordx4 a[4:7], %[l16], %[ws], %[w2o] offen offset:1024 sc1\n\t"
      "v_exp_f32 v100, v100\n\t"
      "v_exp_f32 v101, v101\n\t"
      "v_exp_f32 v102, v102\n\t"
      "v_exp_f32 v103, v103\n\t"
      "v_smfmac_f32_32x32x32_bf16 v[208:223], v[80:83], v[144:151], %[idx]\n\t"
      "buffer_load_dwordx4 v[80:83], %[l16], %[ws], %[kv] offen offset:2048 sc1\n\t"
      "v_exp_f32 v104, v104\n\t"
      "v_exp_f32 v105, v105\n\t"
      "v_exp_f32 v106, v106\n\t"
      "v_exp_f32 v107, v107\n\t"
      "v_smfmac_f32_32x32x32_bf16 v[208:223], v[84:87], v[152:159], %[idx]\n\t"
      "buffer_load_dwordx4 a[8:11], %[l16], %[ws], %[w2o] offen offset:2048 sc1\n\t"
      "v_exp_f32 v108, v108\n\t"
      "v_exp_f32 v109, v109\n\t"
      "v_exp_f32 v110, v110\n\t"
      "v_exp_f32 v111, v111\n\t"
      "v_mfma_f32_32x32x16_bf16 v[128:143], v[72:75], v[48:51], 0\n\t"
      "buffer_load_dwordx4 v[84:87], %[l16], %[ws], %[kv] offen offset:3072 sc1\n\t"
      "v_exp_f32 v112, v112\n\t"
      "v_exp_f32 v113, v113\n\t"
      "v_exp_f32 v114, v114\n\t"
      "v_exp_f32 v115, v115\n\t"
      "v_mfma_f32_32x32x16_bf16 v[128:143], v[76:79], v[52:55], v[128:143]\n\t"
      "v_exp_f32 v116, v116\n\t"
      "v_exp_f32 v117, v117\n\t"
      "v_exp_f32 v118, v118\n\t"
      "v_exp_f32 v119, v119\n\t"
      "v_smfmac_f32_32x32x32_bf16 v[160:175], v[88:91], v[96:103], %[idx]\n\t"
      "buffer_load_dwordx4 %[l16], %[ws], %[w1o] offen offset:0 sc1 lds\n\t"
      "v_exp_f32 v120, v120\n\t"
      "v_exp_f32 v121, v121\n\t"
      "v_exp_f32 v122, v122\n\t"
      "v_exp_f32 v123, v123\n\t"
      "v_smfmac_f32_32x32x32_bf16 v[160:175], v[92:95], v[104:111], %[idx]\n\t"
      "s_add_u32 %[kv], %[kv], 0x1000\n\t"
      "v_exp_f32 v124, v124\n\t"
      "v_exp_f32 v125, v125\n\t"
      "v_exp_f32 v126, v126\n\t"
      "v_exp_f32 v127, v127\n\t"
      "v_mfma_f32_32x32x16_bf16 v[144:159], v[72:75], v[56:59], 0\n\t"
      "buffer_load_dwordx4 v[72:75], %[l16], %[ws], %[kv] offen offset:0 sc1\n\t"
      "v_exp_f32 v128, v128\n\t"
      "v_exp_f32 v129, v129\n\t"
      "v_exp_f32 v130, v130\n\t"
      "v_exp_f32 v131, v131\n\t"
      "v_mfma_f32_32x32x16_bf16 v[144:159], v[76:79], v[60:63], v[144:159]\n\t"
      "buffer_load_dwordx4 a[12:15], %[l16], %[ws], %[w2o] offen offset:3072 sc1\n\t"
      "s_add_u32 %[w2o], %[w2o], 0x1000\n\t"
      "v_exp_f32 v132, v132\n\t"
      "v_exp_f32 v133, v133\n\t"
      "v_exp_f32 v134, v134\n\t"
      "v_exp_f32 v135, v135\n\t"
      "v_smfmac_f32_32x32x32_bf16 v[176:191], v[88:91], v[112:119], %[idx]\n\t"
      "buffer_load_dwordx4 v[76:79], %[l16], %[ws], %[kv] offen offset:1024 sc1\n\t"
      "v_exp_f32 v136, v136\n\t"
      "v_exp_f32 v137, v137\n\t"
      "v_exp_f32 v138, v138\n\t"
      "v_exp_f32 v139, v139\n\t"
      "v_smfmac_f32_32x32x32_bf16 v[176:191], v[92:95], v[120:127], %[idx]\n\t"
      "v_exp_f32 v140, v140\n\t"
      "v_exp_f32 v141, v141\n\t"
      "v_exp_f32 v142, v142\n\t"
      "v_exp_f32 v143, v143\n\t"
      "v_mfma_f32_32x32x16_bf16 v[96:111], v[224:227], v[32:35], 0\n\t"
      "buffer_load_dwordx4 %[l16], %[ws], %[w1o] offen offset:1024 sc1 lds\n\t"
      "v_exp_f32 v144, v144\n\t"
      "v_exp_f32 v145, v145\n\t"
      "v_exp_f32 v146, v146\n\t"
      "v_exp_f32 v147, v147\n\t"
      "v_mfma_f32_32x32x16_bf16 v[96:111], v[228:231], v[36:39], v[96:111]\n\t"
      "v_exp_f32 v148, v148\n\t"
      "v_exp_f32 v149, v149\n\t"
      "v_exp_f32 v150, v150\n\t"
      "v_exp_f32 v151, v151\n\t"
      "v_smfmac_f32_32x32x32_bf16 v[192:207], v[88:91], v[128:135], %[idx]\n\t"
      "v_exp_f32 v152, v152\n\t"
      "v_exp_f32 v153, v153\n\t"
      "v_exp_f32 v154, v154\n\t"
      "v_exp_f32 v155, v155\n\t"
      "v_smfmac_f32_32x32x32_bf16 v[192:207], v[92:95], v[136:143], %[idx]\n\t"
      "v_exp_f32 v156, v156\n\t"
      "v_exp_f32 v157, v157\n\t"
      "v_exp_f32 v158, v158\n\t"
      "v_exp_f32 v159, v159\n\t"
      "v_mfma_f32_32x32x16_bf16 v[112:127], v[224:227], v[40:43], 0\n\t"
      "buffer_load_dwordx4 a[16:19], %[l16], %[ws], %[w2o] offen offset:0 sc1\n\t"
      "v_exp_f32 v96, v96\n\t"
      "v_exp_f32 v97, v97\n\t"
      "v_exp_f32 v98, v98\n\t"
      "v_exp_f32 v99, v99\n\t"
      "v_mfma_f32_32x32x16_bf16 v[112:127], v[228:231], v[44:47], v[112:127]\n\t"
      "buffer_load_dwordx4 a[20:23], %[l16], %[ws], %[w2o] offen offset:1024 sc1\n\t"
      "v_exp_f32 v100, v100\n\t"
      "v_exp_f32 v101, v101\n\t"
      "v_exp_f32 v102, v102\n\t"
      "v_exp_f32 v103, v103\n\t"
      "v_smfmac_f32_32x32x32_bf16 v[208:223], v[88:91], v[144:151], %[idx]\n\t"
      "buffer_load_dwordx4 v[88:91], %[l16], %[ws], %[kv] offen offset:2048 sc1\n\t"
      "v_exp_f32 v104, v104\n\t"
      "v_exp_f32 v105, v105\n\t"
      "v_exp_f32 v106, v106\n\t"
      "v_exp_f32 v107, v107\n\t"
      "v_smfmac_f32_32x32x32_bf16 v[208:223], v[92:95], v[152:159], %[idx]\n\t"
      "buffer_load_dwordx4 a[24:27], %[l16], %[ws], %[w2o] offen offset:2048 sc1\n\t"
      "v_exp_f32 v108, v108\n\t"
      "v_exp_f32 v109, v109\n\t"
      "v_exp_f32 v110, v110\n\t"
      "v_exp_f32 v111, v111\n\t"
      "v_mfma_f32_32x32x16_bf16 v[128:143], v[224:227], v[48:51], 0\n\t"
      "buffer_load_dwordx4 v[92:95], %[l16], %[ws], %[kv] offen offset:3072 sc1\n\t"
      "v_exp_f32 v112, v112\n\t"
      "v_exp_f32 v113, v113\n\t"
      "v_exp_f32 v114, v114\n\t"
      "v_exp_f32 v115, v115\n\t"
      "v_mfma_f32_32x32x16_bf16 v[128:143], v[228:231], v[52:55], v[128:143]\n\t"
      "s_waitcnt vmcnt(25)\n\t"
      "v_exp_f32 v116, v116\n\t"
      "v_exp_f32 v117, v117\n\t"
      "v_exp_f32 v118, v118\n\t"
      "v_exp_f32 v119, v119\n\t"
      "v_smfmac_f32_32x32x32_bf16 v[160:175], v[240:243], v[96:103], %[idx]\n\t"
      "buffer_load_dwordx4 %[l16], %[ws], %[w1o] offen offset:2048 sc1 lds\n\t"
      "v_exp_f32 v120, v120\n\t"
      "v_exp_f32 v121, v121\n\t"
      "v_exp_f32 v122, v122\n\t"
      "v_exp_f32 v123, v123\n\t"
      "v_smfmac_f32_32x32x32_bf16 v[160:175], v[244:247], v[104:111], %[idx]\n\t"
      "s_add_u32 %[kv], %[kv], 0x1000\n\t"
      "v_exp_f32 v124, v124\n\t"
      "v_exp_f32 v125, v125\n\t"
      "v_exp_f32 v126, v126\n\t"
      "v_exp_f32 v127, v127\n\t"
      "v_mfma_f32_32x32x16_bf16 v[144:159], v[224:227], v[56:59], 0\n\t"
      "buffer_load_dwordx4 v[224:227], %[l16], %[ws], %[kv] offen offset:0 sc1\n\t"
      "v_exp_f32 v128, v128\n\t"
      "v_exp_f32 v129, v129\n\t"
      "v_exp_f32 v130, v130\n\t"
      "v_exp_f32 v131, v131\n\t"
      "v_mfma_f32_32x32x16_bf16 v[144:159], v[228:231], v[60:63], v[144:159]\n\t"
      "buffer_load_dwordx4 a[28:31], %[l16], %[ws], %[w2o] offen offset:3072 sc1\n\t"
      "s_add_u32 %[w2o], %[w2o], 0x1000\n\t"
      "v_exp_f32 v132, v132\n\t"
      "v_exp_f32 v133, v133\n\t"
      "v_exp_f32 v134, v134\n\t"
      "v_exp_f32 v135, v135\n\t"
      "v_smfmac_f32_32x32x32_bf16 v[176:191], v[240:243], v[112:119], %[idx]\n\t"
      "buffer_load_dwordx4 v[228:231], %[l16], %[ws], %[kv] offen offset:1024 sc1\n\t"
      "v_exp_f32 v136, v136\n\t"
      "v_exp_f32 v137, v137\n\t"
      "v_exp_f32 v138, v138\n\t"
      "v_exp_f32 v139, v139\n\t"
      "v_smfmac_f32_32x32x32_bf16 v[176:191], v[244:247], v[120:127], %[idx]\n\t"
      "s_waitcnt vmcnt(26)\n\t"
      "v_exp_f32 v140, v140\n\t"
      "v_exp_f32 v141, v141\n\t"
      "v_exp_f32 v142, v142\n\t"
      "v_exp_f32 v143, v143\n\t"
      "v_mfma_f32_32x32x16_bf16 v[96:111], v[232:235], v[32:35], 0\n\t"
      "buffer_load_dwordx4 %[l16], %[ws], %[w1o] offen offset:3072 sc1 lds\n\t"
      "v_exp_f32 v144, v144\n\t"
      "v_exp_f32 v145, v145\n\t"
      "v_exp_f32 v146, v146\n\t"
      "v_exp_f32 v147, v147\n\t"
      "v_mfma_f32_32x32x16_bf16 v[96:111], v[236:239], v[36:39], v[96:111]\n\t"
      "s_add_u32 m0, m0, 0x1000\n\t"
      "v_exp_f32 v148, v148\n\t"
      "v_exp_f32 v149, v149\n\t"
      "v_exp_f32 v150, v150\n\t"
      "v_exp_f32 v151, v151\n\t"
      "v_smfmac_f32_32x32x32_bf16 v[192:207], v[240:243], v[128:135], %[idx]\n\t"
      "s_add_u32 %[w1o], %[w1o], 0x1000\n\t"
      "v_exp_f32 v152, v152\n\t"
      "v_exp_f32 v153, v153\n\t"
      "v_exp_f32 v154, v154\n\t"
      "v_exp_f32 v155, v155\n\t"
      "v_smfmac_f32_32x32x32_bf16 v[192:207], v[244:247], v[136:143], %[idx]\n\t"
      "v_exp_f32 v156, v156\n\t"
      "v_exp_f32 v157, v157\n\t"
      "v_exp_f32 v158, v158\n\t"
      "v_exp_f32 v159, v159\n\t"
      "v_mfma_f32_32x32x16_bf16 v[112:127], v[232:235], v[40:43], 0\n\t"
      "buffer_load_dwordx4 a[32:35], %[l16], %[ws], %[w2o] offen offset:0 sc1\n\t"
      "v_exp_f32 v96, v96\n\t"
      "v_exp_f32 v97, v97\n\t"
      "v_exp_f32 v98, v98\n\t"
      "v_exp_f32 v99, v99\n\t"
      "v_mfma_f32_32x32x16_bf16 v[112:127], v[236:239], v[44:47], v[112:127]\n\t"
      "buffer_load_dwordx4 a[36:39], %[l16], %[ws], %[w2o] offen offset:1024 sc1\n\t"
      "v_exp_f32 v100, v100\n\t"
      "v_exp_f32 v101, v101\n\t"
      "v_exp_f32 v102, v102\n\t"
      "v_exp_f32 v103, v103\n\t"
      "v_smfmac_f32_32x32x32_bf16 v[208:223], v[240:243], v[144:151], %[idx]\n\t"
      "buffer_load_dwordx4 v[240:243], %[l16], %[ws], %[kv] offen offset:2048 sc1\n\t"
      "v_exp_f32 v104, v104\n\t"
      "v_exp_f32 v105, v105\n\t"
      "v_exp_f32 v106, v106\n\t"
      "v_exp_f32 v107, v107\n\t"
      "v_smfmac_f32_32x32x32_bf16 v[208:223], v[244:247], v[152:159], %[idx]\n\t"
      "buffer_load_dwordx4 a[40:43], %[l16], %[ws], %[w2o] offen offset:2048 sc1\n\t"
      "v_exp_f32 v108, v108\n\t"
      "v_exp_f32 v109, v109\n\t"
      "v_exp_f32 v110, v110\n\t"
      "v_exp_f32 v111, v111\n\t"
      "v_mfma_f32_32x32x16_bf16 v[128:143], v[232:235], v[48:51], 0\n\t"
      "buffer_load_dwordx4 v[244:247], %[l16], %[ws], %[kv] offen offset:3072 sc1\n\t"
      "v_exp_f32 v112, v112\n\t"
      "v_exp_f32 v113, v113\n\t"
      "v_exp_f32 v114, v114\n\t"
      "v_exp_f32 v115, v115\n\t"
      "v_mfma_f32_32x32x16_bf16 v[128:143], v[236:239], v[52:55], v[128:143]\n\t"
      "s_waitcnt vmcnt(29)\n\t"
      "v_exp_f32 v116, v116\n\t"
      "v_exp_f32 v117, v117\n\t"
      "v_exp_f32 v118, v118\n\t"
      "v_exp_f32 v119, v119\n\t"
      "v_smfmac_f32_32x32x32_bf16 v[160:175], v[248:251], v[96:103], %[idx]\n\t"
      "buffer_load_dwordx4 %[l16], %[ws], %[w1o] offen offset:0 sc1 lds\n\t"
      "v_exp_f32 v120, v120\n\t"
      "v_exp_f32 v121, v121\n\t"
      "v_exp_f32 v122, v122\n\t"
      "v_exp_f32 v123, v123\n\t"
      "v_smfmac_f32_32x32x32_bf16 v[160:175], v[252:255], v[104:111], %[idx]\n\t"
      "s_add_u32 %[kv], %[kv], 0x1000\n\t"
      "v_exp_f32 v124, v124\n\t"
      "v_exp_f32 v125, v125\n\t"
      "v_exp_f32 v126, v126\n\t"
      "v_exp_f32 v127, v127\n\t"
      "v_mfma_f32_32x32x16_bf16 v[144:159], v[232:235], v[56:59], 0\n\t"
      "buffer_load_dwordx4 v[232:235], %[l16], %[ws], %[kv] offen offset:0 sc1\n\t"
      "v_exp_f32 v128, v128\n\t"
      "v_exp_f32 v129, v129\n\t"
      "v_exp_f32 v130, v130\n\t"
      "v_exp_f32 v131, v131\n\t"
      "v_mfma_f32_32x32x16_bf16 v[144:159], v[236:239], v[60:63], v[144:159]\n\t"
      "buffer_load_dwordx4 a[44:47], %[l16], %[ws], %[w2o] offen offset:3072 sc1\n\t"
      "s_add_u32 %[w2o], %[w2o], 0x1000\n\t"
      "v_exp_f32 v132, v132\n\t"
      "v_exp_f32 v133, v133\n\t"
      "v_exp_f32 v134, v134\n\t"
      "v_exp_f32 v135, v135\n\t"
      "v_smfmac_f32_32x32x32_bf16 v[176:191], v[248:251], v[112:119], %[idx]\n\t"
      "buffer_load_dwordx4 v[236:239], %[l16], %[ws], %[kv] offen offset:1024 sc1\n\t"
      "v_exp_f32 v136, v136\n\t"
      "v_exp_f32 v137, v137\n\t"
      "v_exp_f32 v138, v138\n\t"
      "v_exp_f32 v139, v139\n\t"
      "v_smfmac_f32_32x32x32_bf16 v[176:191], v[252:255], v[120:127], %[idx]\n\t"
      "s_waitcnt vmcnt(30)\n\t"
      "v_exp_f32 v140, v140\n\t"
      "v_exp_f32 v141, v141\n\t"
      "v_exp_f32 v142, v142\n\t"
      "v_exp_f32 v143, v143\n\t"
      "v_mfma_f32_32x32x16_bf16 v[96:111], v[64:67], v[32:35], 0\n\t"
      "buffer_load_dwordx4 %[l16], %[ws], %[w1o] offen offset:1024 sc1 lds\n\t"
      "v_exp_f32 v144, v144\n\t"
      "v_exp_f32 v145, v145\n\t"
      "v_exp_f32 v146, v146\n\t"
      "v_exp_f32 v147, v147\n\t"
      "v_mfma_f32_32x32x16_bf16 v[96:111], v[68:71], v[36:39], v[96:111]\n\t"
      "v_exp_f32 v148, v148\n\t"
      "v_exp_f32 v149, v149\n\t"
      "v_exp_f32 v150, v150\n\t"
      "v_exp_f32 v151, v151\n\t"
      "v_smfmac_f32_32x32x32_bf16 v[192:207], v[248:251], v[128:135], %[idx]\n\t"
      "v_exp_f32 v152, v152\n\t"
      "v_exp_f32 v153, v153\n\t"
      "v_exp_f32 v154, v154\n\t"
      "v_exp_f32 v155, v155\n\t"
      "v_smfmac_f32_32x32x32_bf16 v[192:207], v[252:255], v[136:143], %[idx]\n\t"
      "v_exp_f32 v156, v156\n\t"
      "v_exp_f32 v157, v157\n\t"
      "v_exp_f32 v158, v158\n\t"
      "v_exp_f32 v159, v159\n\t"
      "v_mfma_f32_32x32x16_bf16 v[112:127], v[64:67], v[40:43], 0\n\t"
      "buffer_load_dwordx4 a[48:51], %[l16], %[ws], %[w2o] offen offset:0 sc1\n\t"
      "v_exp_f32 v96, v96\n\t"
      "v_exp_f32 v97, v97\n\t"
      "v_exp_f32 v98, v98\n\t"
      "v_exp_f32 v99, v99\n\t"
      "v_mfma_f32_32x32x16_bf16 v[112:127], v[68:71], v[44:47], v[112:127]\n\t"
      "buffer_load_dwordx4 a[52:55], %[l16], %[ws], %[w2o] offen offset:1024 sc1\n\t"
      "s_add_u32 %[w2o], %[w2o], 0xb000\n\t"
      "v_exp_f32 v100, v100\n\t"
      "v_exp_f32 v101, v101\n\t"
      "v_exp_f32 v102, v102\n\t"
      "v_exp_f32 v103, v103\n\t"
      "v_smfmac_f32_32x32x32_bf16 v[208:223], v[248:251], v[144:151], %[idx]\n\t"
      "buffer_load_dwordx4 v[248:251], %[l16], %[ws], %[kv] offen offset:2048 sc1\n\t"
      "v_exp_f32 v104, v104\n\t"
      "v_exp_f32 v105, v105\n\t"
      "v_exp_f32 v106, v106\n\t"
      "v_exp_f32 v107, v107\n\t"
      "v_smfmac_f32_32x32x32_bf16 v[208:223], v[252:255], v[152:159], %[idx]\n\t"
      "buffer_load_dwordx4 a[56:59], %[l16], %[ws], %[w2o] offen offset:0 sc1\n\t"
      "v_exp_f32 v108, v108\n\t"
      "v_exp_f32 v109, v109\n\t"
      "v_exp_f32 v110, v110\n\t"
      "v_exp_f32 v111, v111\n\t"
      "v_mfma_f32_32x32x16_bf16 v[128:143], v[64:67], v[48:51], 0\n\t"
      "buffer_load_dwordx4 v[252:255], %[l16], %[ws], %[kv] offen offset:3072 sc1\n\t"
      "v_exp_f32 v112, v112\n\t"
      "v_exp_f32 v113, v113\n\t"
      "v_exp_f32 v114, v114\n\t"
      "v_exp_f32 v115, v115\n\t"
      "v_mfma_f32_32x32x16_bf16 v[128:143], v[68:71], v[52:55], v[128:143]\n\t"
      "s_waitcnt vmcnt(30)\n\t"
      "v_exp_f32 v116, v116\n\t"
      "v_exp_f32 v117, v117\n\t"
      "v_exp_f32 v118, v118\n\t"
      "v_exp_f32 v119, v119\n\t"
      "v_smfmac_f32_32x32x32_bf16 v[160:175], v[80:83], v[96:103], %[idx]\n\t"
      "buffer_load_dwordx4 %[l16], %[ws], %[w1o] offen offset:2048 sc1 lds\n\t"
      "v_exp_f32 v120, v120\n\t"
      "v_exp_f32 v121, v121\n\t"
      "v_exp_f32 v122, v122\n\t"
      "v_exp_f32 v123, v123\n\t"
      "v_smfmac_f32_32x32x32_bf16 v[160:175], v[84:87], v[104:111], %[idx]\n\t"
      "s_add_u32 %[kv], %[kv], 0x1000\n\t"
      "v_exp_f32 v124, v124\n\t"
      "v_exp_f32 v125, v125\n\t"
      "v_exp_f32 v126, v126\n\t"
      "v_exp_f32 v127, v127\n\t"
      "v_mfma_f32_32x32x16_bf16 v[144:159], v[64:67], v[56:59], 0\n\t"
      "buffer_load_dwordx4 v[64:67], %[l16], %[ws], %[kv] offen offset:0 sc1\n\t"
      "v_exp_f32 v128, v128\n\t"
      "v_exp_f32 v129, v129\n\t"
      "v_exp_f32 v130, v130\n\t"
      "v_exp_f32 v131, v131\n\t"
      "v_mfma_f32_32x32x16_bf16 v[144:159], v[68:71], v[60:63], v[144:159]\n\t"
      "buffer_load_dwordx4 a[60:63], %[l16], %[ws], %[w2o] offen offset:1024 sc1\n\t"
      "v_exp_f32 v132, v132\n\t"
      "v_exp_f32 v133, v133\n\t"
      "v_exp_f32 v134, v134\n\t"
      "v_exp_f32 v135, v135\n\t"
      "v_smfmac_f32_32x32x32_bf16 v[176:191], v[80:83], v[112:119], %[idx]\n\t"
      "buffer_load_dwordx4 v[68:71], %[l16], %[ws], %[kv] offen offset:1024 sc1\n\t"
      "v_exp_f32 v136, v136\n\t"
      "v_exp_f32 v137, v137\n\t"
      "v_exp_f32 v138, v138\n\t"
      "v_exp_f32 v139, v139\n\t"
      "v_smfmac_f32_32x32x32_bf16 v[176:191], v[84:87], v[120:127], %[idx]\n\t"
      "s_waitcnt vmcnt(30)\n\t"
      "v_exp_f32 v140, v140\n\t"
      "v_exp_f32 v141, v141\n\t"
      "v_exp_f32 v142, v142\n\t"
      "v_exp_f32 v143, v143\n\t"
      "v_mfma_f32_32x32x16_bf16 v[96:111], v[72:75], v[32:35], 0\n\t"
      "buffer_load_dwordx4 %[l16], %[ws], %[w1o] offen offset:3072 sc1 lds\n\t"
      "v_exp_f32 v144, v144\n\t"
      "v_exp_f32 v145, v145\n\t"
      "v_exp_f32 v146, v146\n\t"
      "v_exp_f32 v147, v147\n\t"
      "v_mfma_f32_32x32x16_bf16 v[96:111], v[76:79], v[36:39], v[96:111]\n\t"
      "s_add_u32 m0, m0, 0x1000\n\t"
      "v_exp_f32 v148, v148\n\t"
      "v_exp_f32 v149, v149\n\t"
      "v_exp_f32 v150, v150\n\t"
      "v_exp_f32 v151, v151\n\t"
      "v_smfmac_f32_32x32x32_bf16 v[192:207], v[80:83], v[128:135], %[idx]\n\t"
      "s_add_u32 %[w1o], %[w1o], 0x1000\n\t"
      "v_exp_f32 v152, v152\n\t"
      "v_exp_f32 v153, v153\n\t"
      "v_exp_f32 v154, v154\n\t"
      "v_exp_f32 v155, v155\n\t"
      "v_smfmac_f32_32x32x32_bf16 v[192:207], v[84:87], v[136:143], %[idx]\n\t"
      "v_exp_f32 v156, v156\n\t"
      "v_exp_f32 v157, v157\n\t"
      "v_exp_f32 v158, v158\n\t"
      "v_exp_f32 v159, v159\n\t"
      "v_mfma_f32_32x32x16_bf16 v[112:127], v[72:75], v[40:43], 0\n\t"
      "buffer_load_dwordx4 a[64:67], %[l16], %[ws], %[w2o] offen offset:2048 sc1\n\t"
      "v_exp_f32 v96, v96\n\t"
      "v_exp_f32 v97, v97\n\t"
      "v_exp_f32 v98, v98\n\t"
      "v_exp_f32 v99, v99\n\t"
      "v_mfma_f32_32x32x16_bf16 v[112:127], v[76:79], v[44:47], v[112:127]\n\t"
      "buffer_load_dwordx4 a[68:71], %[l16], %[ws], %[w2o] offen offset:3072 sc1\n\t"
      "s_add_u32 %[w2o], %[w2o], 0x1000\n\t"
      "v_exp_f32 v100, v100\n\t"
      "v_exp_f32 v101, v101\n\t"
      "v_exp_f32 v102, v102\n\t"
      "v_exp_f32 v103, v103\n\t"
      "v_smfmac_f32_32x32x32_bf16 v[208:223], v[80:83], v[144:151], %[idx]\n\t"
      "buffer_load_dwordx4 v[80:83], %[l16], %[ws], %[kv] offen offset:2048 sc1\n\t"
      "v_exp_f32 v104, v104\n\t"
      "v_exp_f32 v105, v105\n\t"
      "v_exp_f32 v106, v106\n\t"
      "v_exp_f32 v107, v107\n\t"
      "v_smfmac_f32_32x32x32_bf16 v[208:223], v[84:87], v[152:159], %[idx]\n\t"
      "buffer_load_dwordx4 a[72:75], %[l16], %[ws], %[w2o] offen offset:0 sc1\n\t"
      "v_exp_f32 v108, v108\n\t"
      "v_exp_f32 v109, v109\n\t"
      "v_exp_f32 v110, v110\n\t"
      "v_exp_f32 v111, v111\n\t"
      "v_mfma_f32_32x32x16_bf16 v[128:143], v[72:75], v[48:51], 0\n\t"
      "buffer_load_dwordx4 v[84:87], %[l16], %[ws], %[kv] offen offset:3072 sc1\n\t"
      "v_exp_f32 v112, v112\n\t"
      "v_exp_f32 v113, v113\n\t"
      "v_exp_f32 v114, v114\n\t"
      "v_exp_f32 v115, v115\n\t"
      "v_mfma_f32_32x32x16_bf16 v[128:143], v[76:79], v[52:55], v[128:143]\n\t"
      "s_waitcnt vmcnt(30)\n\t"
      "v_exp_f32 v116, v116\n\t"
      "v_exp_f32 v117, v117\n\t"
      "v_exp_f32 v118, v118\n\t"
      "v_exp_f32 v119, v119\n\t"
      "v_smfmac_f32_32x32x32_bf16 v[160:175], v[88:91], v[96:103], %[idx]\n\t"
      "buffer_load_dwordx4 %[l16], %[ws], %[w1o] offen offset:0 sc1 lds\n\t"
      "v_exp_f32 v120, v120\n\t"
      "v_exp_f32 v121, v121\n\t"
      "v_exp_f32 v122, v122\n\t"
      "v_exp_f32 v123, v123\n\t"
      "v_smfmac_f32_32x32x32_bf16 v[160:175], v[92:95], v[104:111], %[idx]\n\t"
      "s_add_u32 %[kv], %[kv], 0x1000\n\t"
      "v_exp_f32 v124, v124\n\t"
      "v_exp_f32 v125, v125\n\t"
      "v_exp_f32 v126, v126\n\t"
      "v_exp_f32 v127, v127\n\t"
      "v_mfma_f32_32x32x16_bf16 v[144:159], v[72:75], v[56:59], 0\n\t"
      "buffer_load_dwordx4 v[72:75], %[l16], %[ws], %[kv] offen offset:0 sc1\n\t"
      "v_exp_f32 v128, v128\n\t"
      "v_exp_f32 v129, v129\n\t"
      "v_exp_f32 v130, v130\n\t"
      "v_exp_f32 v131, v131\n\t"
      "v_mfma_f32_32x32x16_bf16 v[144:159], v[76:79], v[60:63], v[144:159]\n\t"
      "buffer_load_dwordx4 a[76:79], %[l16], %[ws], %[w2o] offen offset:1024 sc1\n\t"
      "v_exp_f32 v132, v132\n\t"
      "v_exp_f32 v133, v133\n\t"
      "v_exp_f32 v134, v134\n\t"
      "v_exp_f32 v135, v135\n\t"
      "v_smfmac_f32_32x32x32_bf16 v[176:191], v[88:91], v[112:119], %[idx]\n\t"
      "buffer_load_dwordx4 v[76:79], %[l16], %[ws], %[kv] offen offset:1024 sc1\n\t"
      "v_exp_f32 v136, v136\n\t"
      "v_exp_f32 v137, v137\n\t"
      "v_exp_f32 v138, v138\n\t"
      "v_exp_f32 v139, v139\n\t"
      "v_smfmac_f32_32x32x32_bf16 v[176:191], v[92:95], v[120:127], %[idx]\n\t"
      "s_waitcnt vmcnt(30)\n\t"
      "v_exp_f32 v140, v140\n\t"
      "v_exp_f32 v141, v141\n\t"
      "v_exp_f32 v142, v142\n\t"
      "v_exp_f32 v143, v143\n\t"
      "v_mfma_f32_32x32x16_bf16 v[96:111], v[224:227], v[32:35], 0\n\t"
      "buffer_load_dwordx4 %[l16], %[ws], %[w1o] offen offset:1024 sc1 lds\n\t"
      "v_exp_f32 v144, v144\n\t"
      "v_exp_f32 v145, v145\n\t"
      "v_exp_f32 v146, v146\n\t"
      "v_exp_f32 v147, v147\n\t"
      "v_mfma_f32_32x32x16_bf16 v[96:111], v[228:231], v[36:39], v[96:111]\n\t"
      "v_exp_f32 v148, v148\n\t"
      "v_exp_f32 v149, v149\n\t"
      "v_exp_f32 v150, v150\n\t"
      "v_exp_f32 v151, v151\n\t"
      "v_smfmac_f32_32x32x32_bf16 v[192:207], v[88:91], v[128:135], %[idx]\n\t"
      "v_exp_f32 v152, v152\n\t"
      "v_exp_f32 v153, v153\n\t"
      "v_exp_f32 v154, v154\n\t"
      "v_exp_f32 v155, v155\n\t"
      "v_smfmac_f32_32x32x32_bf16 v[192:207], v[92:95], v[136:143], %[idx]\n\t"
      "v_exp_f32 v156, v156\n\t"
      "v_exp_f32 v157, v157\n\t"
      "v_exp_f32 v158, v158\n\t"
      "v_exp_f32 v159, v159\n\t"
      "v_mfma_f32_32x32x16_bf16 v[112:127], v[224:227], v[40:43], 0\n\t"
      "buffer_load_dwordx4 a[80:83], %[l16], %[ws], %[w2o] offen offset:2048 sc1\n\t"
      "v_exp_f32 v96, v96\n\t"
      "v_exp_f32 v97, v97\n\t"
      "v_exp_f32 v98, v98\n\t"
      "v_exp_f32 v99, v99\n\t"
      "v_mfma_f32_32x32x16_bf16 v[112:127], v[228:231], v[44:47], v[112:127]\n\t"
      "buffer_load_dwordx4 a[84:87], %[l16], %[ws], %[w2o] offen offset:3072 sc1\n\t"
      "s_add_u32 %[w2o], %[w2o], 0x1000\n\t"
      "v_exp_f32 v100, v100\n\t"
      "v_exp_f32 v101, v101\n\t"
      "v_exp_f32 v102, v102\n\t"
      "v_exp_f32 v103, v103\n\t"
      "v_smfmac_f32_32x32x32_bf16 v[208:223], v[88:91], v[144:151], %[idx]\n\t"
      "buffer_load_dwordx4 v[88:91], %[l16], %[ws], %[kv] offen offset:2048 sc1\n\t"
      "v_exp_f32 v104, v104\n\t"
      "v_exp_f32 v105, v105\n\t"
      "v_exp_f32 v106, v106\n\t"
      "v_exp_f32 v107, v107\n\t"
      "v_smfmac_f32_32x32x32_bf16 v[208:223], v[92:95], v[152:159], %[idx]\n\t"
      "buffer_load_dwordx4 a[88:91], %[l16], %[ws], %[w2o] offen offset:0 sc1\n\t"
      "v_exp_f32 v108, v108\n\t"
      "v_exp_f32 v109, v109\n\t"
      "v_exp_f32 v110, v110\n\t"
      "v_exp_f32 v111, v111\n\t"
      "v_mfma_f32_32x32x16_bf16 v[128:143], v[224:227], v[48:51], 0\n\t"
      "buffer_load_dwordx4 v[92:95], %[l16], %[ws], %[kv] offen offset:3072 sc1\n\t"
      "v_exp_f32 v112, v112\n\t"
      "v_exp_f32 v113, v113\n\t"
      "v_exp_f32 v114, v114\n\t"
      "v_exp_f32 v115, v115\n\t"
      "v_mfma_f32_32x32x16_bf16 v[128:143], v[228:231], v[52:55], v[128:143]\n\t"
      "s_waitcnt vmcnt(30)\n\t"
      "v_exp_f32 v116, v116\n\t"
      "v_exp_f32 v117, v117\n\t"
      "v_exp_f32 v118, v118\n\t"
      "v_exp_f32 v119, v119\n\t"
      "v_smfmac_f32_32x32x32_bf16 v[160:175], v[240:243], v[96:103], %[idx]\n\t"
      "buffer_load_dwordx4 %[l16], %[ws], %[w1o] offen offset:2048 sc1 lds\n\t"
      "v_exp_f32 v120, v120\n\t"
      "v_exp_f32 v121, v121\n\t"
      "v_exp_f32 v122, v122\n\t"
      "v_exp_f32 v123, v123\n\t"
      "v_smfmac_f32_32x32x32_bf16 v[160:175], v[244:247], v[104:111], %[idx]\n\t"
      "s_add_u32 %[kv], %[kv], 0x1000\n\t"
      "v_exp_f32 v124, v124\n\t"
      "v_exp_f32 v125, v125\n\t"
      "v_exp_f32 v126, v126\n\t"
      "v_exp_f32 v127, v127\n\t"
      "v_mfma_f32_32x32x16_bf16 v[144:159], v[224:227], v[56:59], 0\n\t"
      "buffer_load_dwordx4 v[224:227], %[l16], %[ws], %[kv] offen offset:0 sc1\n\t"
      "v_exp_f32 v128, v128\n\t"
      "v_exp_f32 v129, v129\n\t"
      "v_exp_f32 v130, v130\n\t"
      "v_exp_f32 v131, v131\n\t"
      "v_mfma_f32_32x32x16_bf16 v[144:159], v[228:231], v[60:63], v[144:159]\n\t"
      "buffer_load_dwordx4 a[92:95], %[l16], %[ws], %[w2o] offen offset:1024 sc1\n\t"
      "v_exp_f32 v132, v132\n\t"
      "v_exp_f32 v133, v133\n\t"
      "v_exp_f32 v134, v134\n\t"
      "v_exp_f32 v135, v135\n\t"
      "v_smfmac_f32_32x32x32_bf16 v[176:191], v[240:243], v[112:119], %[idx]\n\t"
      "buffer_load_dwordx4 v[228:231], %[l16], %[ws], %[kv] offen offset:1024 sc1\n\t"
      "v_exp_f32 v136, v136\n\t"
      "v_exp_f32 v137, v137\n\t"
      "v_exp_f32 v138, v138\n\t"
      "v_exp_f32 v139, v139\n\t"
      "v_smfmac_f32_32x32x32_bf16 v[176:191], v[244:247], v[120:127], %[idx]\n\t"
      "s_waitcnt vmcnt(30)\n\t"
      "v_exp_f32 v140, v140\n\t"
      "v_exp_f32 v141, v141\n\t"
      "v_exp_f32 v142, v142\n\t"
      "v_exp_f32 v143, v143\n\t"
      "v_mfma_f32_32x32x16_bf16 v[96:111], v[232:235], v[32:35], 0\n\t"
      "buffer_load_dwordx4 %[l16], %[ws], %[w1o] offen offset:3072 sc1 lds\n\t"
      "v_exp_f32 v144, v144\n\t"
      "v_exp_f32 v145, v145\n\t"
      "v_exp_f32 v146, v146\n\t"
      "v_exp_f32 v147, v147\n\t"
      "v_mfma_f32_32x32x16_bf16 v[96:111], v[236:239], v[36:39], v[96:111]\n\t"
      "s_add_u32 m0, m0, 0x1000\n\t"
      "v_exp_f32 v148, v148\n\t"
      "v_exp_f32 v149, v149\n\t"
      "v_exp_f32 v150, v150\n\t"
      "v_exp_f32 v151, v151\n\t"
      "v_smfmac_f32_32x32x32_bf16 v[192:207], v[240:243], v[128:135], %[idx]\n\t"
      "s_add_u32 %[w1o], %[w1o], 0x1000\n\t"
      "v_exp_f32 v152, v152\n\t"
      "v_exp_f32 v153, v153\n\t"
      "v_exp_f32 v154, v154\n\t"
      "v_exp_f32 v155, v155\n\t"
      "v_smfmac_f32_32x32x32_bf16 v[192:207], v[244:247], v[136:143], %[idx]\n\t"
      "v_exp_f32 v156, v156\n\t"
      "v_exp_f32 v157, v157\n\t"
      "v_exp_f32 v158, v158\n\t"
      "v_exp_f32 v159, v159\n\t"
      "v_mfma_f32_32x32x16_bf16 v[112:127], v[232:235], v[40:43], 0\n\t"
      "buffer_load_dwordx4 a[96:99], %[l16], %[ws], %[w2o] offen offset:2048 sc1\n\t"
      "v_exp_f32 v96, v96\n\t"
      "v_exp_f32 v97, v97\n\t"
      "v_exp_f32 v98, v98\n\t"
      "v_exp_f32 v99, v99\n\t"
      "v_mfma_f32_32x32x16_bf16 v[112:127], v[236:239], v[44:47], v[112:127]\n\t"
      "buffer_load_dwordx4 a[100:103], %[l16], %[ws], %[w2o] offen offset:3072 sc1\n\t"
      "s_add_u32 %[w2o], %[w2o], 0x1000\n\t"
      "v_exp_f32 v100, v100\n\t"
      "v_exp_f32 v101, v101\n\t"
      "v_exp_f32 v102, v102\n\t"
      "v_exp_f32 v103, v103\n\t"
      "v_smfmac_f32_32x32x32_bf16 v[208:223], v[240:243], v[144:151], %[idx]\n\t"
      "buffer_load_dwordx4 v[240:243], %[l16], %[ws], %[kv] offen offset:2048 sc1\n\t"
      "v_exp_f32 v104, v104\n\t"
      "v_exp_f32 v105, v105\n\t"
      "v_exp_f32 v106, v106\n\t"
      "v_exp_f32 v107, v107\n\t"
      "v_smfmac_f32_32x32x32_bf16 v[208:223], v[244:247], v[152:159], %[idx]\n\t"
      "buffer_load_dwordx4 a[104:107], %[l16], %[ws], %[w2o] offen offset:0 sc1\n\t"
      "v_exp_f32 v108, v108\n\t"
      "v_exp_f32 v109, v109\n\t"
      "v_exp_f32 v110, v110\n\t"
      "v_exp_f32 v111, v111\n\t"
      "v_mfma_f32_32x32x16_bf16 v[128:143], v[232:235], v[48:51], 0\n\t"
      "buffer_load_dwordx4 v[244:247], %[l16], %[ws], %[kv] offen offset:3072 sc1\n\t"
      "v_exp_f32 v112, v112\n\t"
      "v_exp_f32 v113, v113\n\t"
      "v_exp_f32 v114, v114\n\t"
      "v_exp_f32 v115, v115\n\t"
      "v_mfma_f32_32x32x16_bf16 v[128:143], v[236:239], v[52:55], v[128:143]\n\t"
      "s_waitcnt vmcnt(30)\n\t"
      "v_exp_f32 v116, v116\n\t"
      "v_exp_f32 v117, v117\n\t"
      "v_exp_f32 v118, v118\n\t"
      "v_exp_f32 v119, v119\n\t"
      "v_smfmac_f32_32x32x32_bf16 v[160:175], v[248:251], v[96:103], %[idx]\n\t"
      "buffer_load_dwordx4 %[l16], %[ws], %[w1o] offen offset:0 sc1 lds\n\t"
      "v_exp_f32 v120, v120\n\t"
      "v_exp_f32 v121, v121\n\t"
      "v_exp_f32 v122, v122\n\t"
      "v_exp_f32 v123, v123\n\t"
      "v_smfmac_f32_32x32x32_bf16 v[160:175], v[252:255], v[104:111], %[idx]\n\t"
      "s_add_u32 %[kv], %[kv], 0x1000\n\t"
      "v_exp_f32 v124, v124\n\t"
      "v_exp_f32 v125, v125\n\t"
      "v_exp_f32 v126, v126\n\t"
      "v_exp_f32 v127, v127\n\t"
      "v_mfma_f32_32x32x16_bf16 v[144:159], v[232:235], v[56:59], 0\n\t"
      "buffer_load_dwordx4 v[232:235], %[l16], %[ws], %[kv] offen offset:0 sc1\n\t"
      "v_exp_f32 v128, v128\n\t"
      "v_exp_f32 v129, v129\n\t"
      "v_exp_f32 v130, v130\n\t"
      "v_exp_f32 v131, v131\n\t"
      "v_mfma_f32_32x32x16_bf16 v[144:159], v[236:239], v[60:63], v[144:159]\n\t"
      "buffer_load_dwordx4 a[108:111], %[l16], %[ws], %[w2o] offen offset:1024 sc1\n\t"
      "s_mov_b32 %[w2o], %[w2p]\n\t"
      "v_exp_f32 v132, v132\n\t"
      "v_exp_f32 v133, v133\n\t"
      "v_exp_f32 v134, v134\n\t"
      "v_exp_f32 v135, v135\n\t"
      "v_smfmac_f32_32x32x32_bf16 v[176:191], v[248:251], v[112:119], %[idx]\n\t"
      "buffer_load_dwordx4 v[236:239], %[l16], %[ws], %[kv] offen offset:1024 sc1\n\t"
      "v_exp_f32 v136, v136\n\t"
      "v_exp_f32 v137, v137\n\t"
      "v_exp_f32 v138, v138\n\t"
      "v_exp_f32 v139, v139\n\t"
      "v_smfmac_f32_32x32x32_bf16 v[176:191], v[252:255], v[120:127], %[idx]\n\t"
      "s_waitcnt vmcnt(30)\n\t"
      "v_exp_f32 v140, v140\n\t"
      "v_exp_f32 v141, v141\n\t"
      "v_exp_f32 v142, v142\n\t"
      "v_exp_f32 v143, v143\n\t"
      "v_mfma_f32_32x32x16_bf16 v[96:111], v[64:67], v[32:35], 0\n\t"
      "buffer_load_dwordx4 %[l16], %[ws], %[w1o] offen offset:1024 sc1 lds\n\t"
      "v_exp_f32 v144, v144\n\t"
      "v_exp_f32 v145, v145\n\t"
      "v_exp_f32 v146, v146\n\t"
      "v_exp_f32 v147, v147\n\t"
      "v_mfma_f32_32x32x16_bf16 v[96:111], v[68:71], v[36:39], v[96:111]\n\t"
      "v_exp_f32 v148, v148\n\t"
      "v_exp_f32 v149, v149\n\t"
      "v_exp_f32 v150, v150\n\t"
      "v_exp_f32 v151, v151\n\t"
      "v_smfmac_f32_32x32x32_bf16 v[192:207], v[248:251], v[128:135], %[idx]\n\t"
      "v_exp_f32 v152, v152\n\t"
      "v_exp_f32 v153, v153\n\t"
      "v_exp_f32 v154, v154\n\t"
      "v_exp_f32 v155, v155\n\t"
      "v_smfmac_f32_32x32x32_bf16 v[192:207], v[252:255], v[136:143], %[idx]\n\t"
      "v_exp_f32 v156, v156\n\t"
      "v_exp_f32 v157, v157\n\t"
      "v_exp_f32 v158, v158\n\t"
      "v_exp_f32 v159, v159\n\t"
      "v_mfma_f32_32x32x16_bf16 v[112:127], v[64:67], v[40:43], 0\n\t"
      "buffer_load_dwordx4 a[112:115], %[l16], %[ws], %[w2o] offen offset:0 sc1\n\t"
      "v_exp_f32 v96, v96\n\t"
      "v_exp_f32 v97, v97\n\t"
      "v_exp_f32 v98, v98\n\t"
      "v_exp_f32 v99, v99\n\t"
      "v_mfma_f32_32x32x16_bf16 v[112:127], v[68:71], v[44:47], v[112:127]\n\t"
      "buffer_load_dwordx4 a[116:119], %[l16], %[ws], %[w2o] offen offset:1024 sc1\n\t"
      "v_exp_f32 v100, v100\n\t"
      "v_exp_f32 v101, v101\n\t"
      "v_exp_f32 v102, v102\n\t"
      "v_exp_f32 v103, v103\n\t"
      "v_smfmac_f32_32x32x32_bf16 v[208:223], v[248:251], v[144:151], %[idx]\n\t"
      "buffer_load_dwordx4 v[248:251], %[l16], %[ws], %[kv] offen offset:2048 sc1\n\t"
      "v_exp_f32 v104, v104\n\t"
      "v_exp_f32 v105, v105\n\t"
      "v_exp_f32 v106, v106\n\t"
      "v_exp_f32 v107, v107\n\t"
      "v_smfmac_f32_32x32x32_bf16 v[208:223], v[252:255], v[152:159], %[idx]\n\t"
      "buffer_load_dwordx4 a[120:123], %[l16], %[ws], %[w2o] offen offset:2048 sc1\n\t"
      "v_exp_f32 v108, v108\n\t"
      "v_exp_f32 v109, v109\n\t"
      "v_exp_f32 v110, v110\n\t"
      "v_exp_f32 v111, v111\n\t"
      "v_mfma_f32_32x32x16_bf16 v[128:143], v[64:67], v[48:51], 0\n\t"
      "buffer_load_dwordx4 v[252:255], %[l16], %[ws], %[kv] offen offset:3072 sc1\n\t"
      "v_exp_f32 v112, v112\n\t"
      "v_exp_f32 v113, v113\n\t"
      "v_exp_f32 v114, v114\n\t"
      "v_exp_f32 v115, v115\n\t"
      "v_mfma_f32_32x32x16_bf16 v[128:143], v[68:71], v[52:55], v[128:143]\n\t"
      "s_waitcnt vmcnt(30)\n\t"
      "v_exp_f32 v116, v116\n\t"
      "v_exp_f32 v117, v117\n\t"
      "v_exp_f32 v118, v118\n\t"
      "v_exp_f32 v119, v119\n\t"
      "v_smfmac_f32_32x32x32_bf16 v[160:175], v[80:83], v[96:103], %[idx]\n\t"
      "buffer_load_dwordx4 %[l16], %[ws], %[w1o] offen offset:2048 sc1 lds\n\t"
      "v_exp_f32 v120, v120\n\t"
      "v_exp_f32 v121, v121\n\t"
      "v_exp_f32 v122, v122\n\t"
      "v_exp_f32 v123, v123\n\t"
      "v_smfmac_f32_32x32x32_bf16 v[160:175], v[84:87], v[104:111], %[idx]\n\t"
      "s_add_u32 %[kv], %[kv], 0x1000\n\t"
      "v_exp_f32 v124, v124\n\t"
      "v_exp_f32 v125, v125\n\t"
      "v_exp_f32 v126, v126\n\t"
      "v_exp_f32 v127, v127\n\t"
      "v_mfma_f32_32x32x16_bf16 v[144:159], v[64:67], v[56:59], 0\n\t"
      "buffer_load_dwordx4 v[64:67], %[l16], %[ws], %[kv] offen offset:0 sc1\n\t"
      "v_exp_f32 v128, v128\n\t"
      "v_exp_f32 v129, v129\n\t"
      "v_exp_f32 v130, v130\n\t"
      "v_exp_f32 v131, v131\n\t"
      "v_mfma_f32_32x32x16_bf16 v[144:159], v[68:71], v[60:63], v[144:159]\n\t"
      "buffer_load_dwordx4 a[124:127], %[l16], %[ws], %[w2o] offen offset:3072 sc1\n\t"
      "s_add_u32 %[w2o], %[w2o], 0x1000\n\t"
      "v_exp_f32 v132, v132\n\t"
      "v_exp_f32 v133, v133\n\t"
      "v_exp_f32 v134, v134\n\t"
      "v_exp_f32 v135, v135\n\t"
      "v_smfmac_f32_32x32x32_bf16 v[176:191], v[80:83], v[112:119], %[idx]\n\t"
      "buffer_load_dwordx4 v[68:71], %[l16], %[ws], %[kv] offen offset:1024 sc1\n\t"
      "v_exp_f32 v136, v136\n\t"
      "v_exp_f32 v137, v137\n\t"
      "v_exp_f32 v138, v138\n\t"
      "v_exp_f32 v139, v139\n\t"
      "v_smfmac_f32_32x32x32_bf16 v[176:191], v[84:87], v[120:127], %[idx]\n\t"
      "s_waitcnt vmcnt(30)\n\t"
      "v_exp_f32 v140, v140\n\t"
      "v_exp_f32 v141, v141\n\t"
      "v_exp_f32 v142, v142\n\t"
      "v_exp_f32 v143, v143\n\t"
      "v_mfma_f32_32x32x16_bf16 v[96:111], v[72:75], v[32:35], 0\n\t"
      "buffer_load_dwordx4 %[l16], %[ws], %[w1o] offen offset:3072 sc1 lds\n\t"
      "v_exp_f32 v144, v144\n\t"
      "v_exp_f32 v145, v145\n\t"
      "v_exp_f32 v146, v146\n\t"
      "v_exp_f32 v147, v147\n\t"
      "v_mfma_f32_32x32x16_bf16 v[96:111], v[76:79], v[36:39], v[96:111]\n\t"
      "s_add_u32 m0, m0, 0x1000\n\t"
      "v_exp_f32 v148, v148\n\t"
      "v_exp_f32 v149, v149\n\t"
      "v_exp_f32 v150, v150\n\t"
      "v_exp_f32 v151, v151\n\t"
      "v_smfmac_f32_32x32x32_bf16 v[192:207], v[80:83], v[128:135], %[idx]\n\t"
      "s_add_u32 %[w1o], %[w1o], 0x1000\n\t"
      "v_exp_f32 v152, v152\n\t"
      "v_exp_f32 v153, v153\n\t"
      "v_exp_f32 v154, v154\n\t"
      "v_exp_f32 v155, v155\n\t"
      "v_smfmac_f32_32x32x32_bf16 v[192:207], v[84:87], v[136:143], %[idx]\n\t"
      "v_exp_f32 v156, v156\n\t"
      "v_exp_f32 v157, v157\n\t"
      "v_exp_f32 v158, v158\n\t"
      "v_exp_f32 v159, v159\n\t"
      "v_mfma_f32_32x32x16_bf16 v[112:127], v[72:75], v[40:43], 0\n\t"
      "buffer_load_dwordx4 a[128:131], %[l16], %[ws], %[w2o] offen offset:0 sc1\n\t"
      "v_exp_f32 v96, v96\n\t"
      "v_exp_f32 v97, v97\n\t"
      "v_exp_f32 v98, v98\n\t"
      "v_exp_f32 v99, v99\n\t"
      "v_mfma_f32_32x32x16_bf16 v[112:127], v[76:79], v[44:47], v[112:127]\n\t"
      "buffer_load_dwordx4 a[132:135], %[l16], %[ws], %[w2o] offen offset:1024 sc1\n\t"
      "v_exp_f32 v100, v100\n\t"
      "v_exp_f32 v101, v101\n\t"
      "v_exp_f32 v102, v102\n\t"
      "v_exp_f32 v103, v103\n\t"
      "v_smfmac_f32_32x32x32_bf16 v[208:223], v[80:83], v[144:151], %[idx]\n\t"
      "buffer_load_dwordx4 v[80:83], %[l16], %[ws], %[kv] offen offset:2048 sc1\n\t"
      "v_exp_f32 v104, v104\n\t"
      "v_exp_f32 v105, v105\n\t"
      "v_exp_f32 v106, v106\n\t"
      "v_exp_f32 v107, v107\n\t"
      "v_smfmac_f32_32x32x32_bf16 v[208:223], v[84:87], v[152:159], %[idx]\n\t"
      "buffer_load_dwordx4 a[136:139], %[l16], %[ws], %[w2o] offen offset:2048 sc1\n\t"
      "v_exp_f32 v108, v108\n\t"
      "v_exp_f32 v109, v109\n\t"
      "v_exp_f32 v110, v110\n\t"
      "v_exp_f32 v111, v111\n\t"
      "v_mfma_f32_32x32x16_bf16 v[128:143], v[72:75], v[48:51], 0\n\t"
      "buffer_load_dwordx4 v[84:87], %[l16], %[ws], %[kv] offen offset:3072 sc1\n\t"
      "v_exp_f32 v112, v112\n\t"
      "v_exp_f32 v113, v113\n\t"
      "v_exp_f32 v114, v114\n\t"
      "v_exp_f32 v115, v115\n\t"
      "v_mfma_f32_32x32x16_bf16 v[128:143], v[76:79], v[52:55], v[128:143]\n\t"
      "s_waitcnt vmcnt(30)\n\t"
      "v_exp_f32 v116, v116\n\t"
      "v_exp_f32 v117, v117\n\t"
      "v_exp_f32 v118, v118\n\t"
      "v_exp_f32 v119, v119\n\t"
      "v_smfmac_f32_32x32x32_bf16 v[160:175], v[88:91], v[96:103], %[idx]\n\t"
      "buffer_load_dwordx4 %[l16], %[ws], %[w1o] offen offset:0 sc1 lds\n\t"
      "v_exp_f32 v120, v120\n\t"
      "v_exp_f32 v121, v121\n\t"
      "v_exp_f32 v122, v122\n\t"
      "v_exp_f32 v123, v123\n\t"
      "v_smfmac_f32_32x32x32_bf16 v[160:175], v[92:95], v[104:111], %[idx]\n\t"
      "s_add_u32 %[kv], %[kv], 0x1000\n\t"
      "v_exp_f32 v124, v124\n\t"
      "v_exp_f32 v125, v125\n\t"
      "v_exp_f32 v126, v126\n\t"
      "v_exp_f32 v127, v127\n\t"
      "v_mfma_f32_32x32x16_bf16 v[144:159], v[72:75], v[56:59], 0\n\t"
      "buffer_load_dwordx4 v[72:75], %[l16], %[ws], %[kv] offen offset:0 sc1\n\t"
      "v_exp_f32 v128, v128\n\t"
      "v_exp_f32 v129, v129\n\t"
      "v_exp_f32 v130, v130\n\t"
      "v_exp_f32 v131, v131\n\t"
      "v_mfma_f32_32x32x16_bf16 v[144:159], v[76:79], v[60:63], v[144:159]\n\t"
      "buffer_load_dwordx4 a[140:143], %[l16], %[ws], %[w2o] offen offset:3072 sc1\n\t"
      "s_add_u32 %[w2o], %[w2o], 0x1000\n\t"
      "v_exp_f32 v132, v132\n\t"
      "v_exp_f32 v133, v133\n\t"
      "v_exp_f32 v134, v134\n\t"
      "v_exp_f32 v135, v135\n\t"
      "v_smfmac_f32_32x32x32_bf16 v[176:191], v[88:91], v[112:119], %[idx]\n\t"
      "buffer_load_dwordx4 v[76:79], %[l16], %[ws], %[kv] offen offset:1024 sc1\n\t"
      "v_exp_f32 v136, v136\n\t"
      "v_exp_f32 v137, v137\n\t"
      "v_exp_f32 v138, v138\n\t"
      "v_exp_f32 v139, v139\n\t"
      "v_smfmac_f32_32x32x32_bf16 v[176:191], v[92:95], v[120:127], %[idx]\n\t"
      "s_waitcnt vmcnt(30)\n\t"
      "v_exp_f32 v140, v140\n\t"
      "v_exp_f32 v141, v141\n\t"
      "v_exp_f32 v142, v142\n\t"
      "v_exp_f32 v143, v143\n\t"
      "v_mfma_f32_32x32x16_bf16 v[96:111], v[224:227], v[32:35], 0\n\t"
      "buffer_load_dwordx4 %[l16], %[ws], %[w1o] offen offset:1024 sc1 lds\n\t"
      "v_exp_f32 v144, v144\n\t"
      "v_exp_f32 v145, v145\n\t"
      "v_exp_f32 v146, v146\n\t"
      "v_exp_f32 v147, v147\n\t"
      "v_mfma_f32_32x32x16_bf16 v[96:111], v[228:231], v[36:39], v[96:111]\n\t"
      "v_exp_f32 v148, v148\n\t"
      "v_exp_f32 v149, v149\n\t"
      "v_exp_f32 v150, v150\n\t"
      "v_exp_f32 v151, v151\n\t"
      "v_smfmac_f32_32x32x32_bf16 v[192:207], v[88:91], v[128:135], %[idx]\n\t"
      "v_exp_f32 v152, v152\n\t"
      "v_exp_f32 v153, v153\n\t"
      "v_exp_f32 v154, v154\n\t"
      "v_exp_f32 v155, v155\n\t"
      "v_smfmac_f32_32x32x32_bf16 v[192:207], v[92:95], v[136:143], %[idx]\n\t"
      "v_exp_f32 v156, v156\n\t"
      "v_exp_f32 v157, v157\n\t"
      "v_exp_f32 v158, v158\n\t"
      "v_exp_f32 v159, v159\n\t"
      "v_mfma_f32_32x32x16_bf16 v[112:127], v[224:227], v[40:43], 0\n\t"
      "buffer_load_dwordx4 a[144:147], %[l16], %[ws], %[w2o] offen offset:0 sc1\n\t"
      "v_exp_f32 v96, v96\n\t"
      "v_exp_f32 v97, v97\n\t"
      "v_exp_f32 v98, v98\n\t"
      "v_exp_f32 v99, v99\n\t"
      "v_mfma_f32_32x32x16_bf16 v[112:127], v[228:231], v[44:47], v[112:127]\n\t"
      "buffer_load_dwordx4 a[148:151], %[l16], %[ws], %[w2o] offen offset:1024 sc1\n\t"
      "v_exp_f32 v100, v100\n\t"
      "v_exp_f32 v101, v101\n\t"
      "v_exp_f32 v102, v102\n\t"
      "v_exp_f32 v103, v103\n\t"
      "v_smfmac_f32_32x32x32_bf16 v[208:223], v[88:91], v[144:151], %[idx]\n\t"
      "buffer_load_dwordx4 v[88:91], %[l16], %[ws], %[kv] offen offset:2048 sc1\n\t"
      "v_exp_f32 v104, v104\n\t"
      "v_exp_f32 v105, v105\n\t"
      "v_exp_f32 v106, v106\n\t"
      "v_exp_f32 v107, v107\n\t"
      "v_smfmac_f32_32x32x32_bf16 v[208:223], v[92:95], v[152:159], %[idx]\n\t"
      "buffer_load_dwordx4 a[152:155], %[l16], %[ws], %[w2o] offen offset:2048 sc1\n\t"
      "v_exp_f32 v108, v108\n\t"
      "v_exp_f32 v109, v109\n\t"
      "v_exp_f32 v110, v110\n\t"
      "v_exp_f32 v111, v111\n\t"
      "v_mfma_f32_32x32x16_bf16 v[128:143], v[224:227], v[48:51], 0\n\t"
      "buffer_load_dwordx4 v[92:95], %[l16], %[ws], %[kv] offen offset:3072 sc1\n\t"
      "v_exp_f32 v112, v112\n\t"
      "v_exp_f32 v113, v113\n\t"
      "v_exp_f32 v114, v114\n\t"
      "v_exp_f32 v115, v115\n\t"
      "v_mfma_f32_32x32x16_bf16 v[128:143], v[228:231], v[52:55], v[128:143]\n\t"
      "s_waitcnt vmcnt(30)\n\t"
      "v_exp_f32 v116, v116\n\t"
      "v_exp_f32 v117, v117\n\t"
      "v_exp_f32 v118, v118\n\t"
      "v_exp_f32 v119, v119\n\t"
      "v_smfmac_f32_32x32x32_bf16 v[160:175], v[240:243], v[96:103], %[idx]\n\t"
      "buffer_load_dwordx4 %[l16], %[ws], %[w1o] offen offset:2048 sc1 lds\n\t"
      "v_exp_f32 v120, v120\n\t"
      "v_exp_f32 v121, v121\n\t"
      "v_exp_f32 v122, v122\n\t"
      "v_exp_f32 v123, v123\n\t"
      "v_smfmac_f32_32x32x32_bf16 v[160:175], v[244:247], v[104:111], %[idx]\n\t"
      "s_add_u32 %[kv], %[kv], 0x1000\n\t"
      "v_exp_f32 v124, v124\n\t"
      "v_exp_f32 v125, v125\n\t"
      "v_exp_f32 v126, v126\n\t"
      "v_exp_f32 v127, v127\n\t"
      "v_mfma_f32_32x32x16_bf16 v[144:159], v[224:227], v[56:59], 0\n\t"
      "buffer_load_dwordx4 v[224:227], %[l16], %[ws], %[kv] offen offset:0 sc1\n\t"
      "v_exp_f32 v128, v128\n\t"
      "v_exp_f32 v129, v129\n\t"
      "v_exp_f32 v130, v130\n\t"
      "v_exp_f32 v131, v131\n\t"
      "v_mfma_f32_32x32x16_bf16 v[144:159], v[228:231], v[60:63], v[144:159]\n\t"
      "buffer_load_dwordx4 a[156:159], %[l16], %[ws], %[w2o] offen offset:3072 sc1\n\t"
      "s_add_u32 %[w2o], %[w2o], 0x1000\n\t"
      "v_exp_f32 v132, v132\n\t"
      "v_exp_f32 v133, v133\n\t"
      "v_exp_f32 v134, v134\n\t"
      "v_exp_f32 v135, v135\n\t"
      "v_smfmac_f32_32x32x32_bf16 v[176:191], v[240:243], v[112:119], %[idx]\n\t"
      "buffer_load_dwordx4 v[228:231], %[l16], %[ws], %[kv] offen offset:1024 sc1\n\t"
      "v_exp_f32 v136, v136\n\t"
      "v_exp_f32 v137, v137\n\t"
      "v_exp_f32 v138, v138\n\t"
      "v_exp_f32 v139, v139\n\t"
      "v_smfmac_f32_32x32x32_bf16 v[176:191], v[244:247], v[120:127], %[idx]\n\t"
      "s_waitcnt vmcnt(30)\n\t"
      "v_exp_f32 v140, v140\n\t"
      "v_exp_f32 v141, v141\n\t"
      "v_exp_f32 v142, v142\n\t"
      "v_exp_f32 v143, v143\n\t"
      "v_mfma_f32_32x32x16_bf16 v[96:111], v[232:235], v[32:35], 0\n\t"
      "buffer_load_dwordx4 %[l16], %[ws], %[w1o] offen offset:3072 sc1 lds\n\t"
      "v_exp_f32 v144, v144\n\t"
      "v_exp_f32 v145, v145\n\t"
      "v_exp_f32 v146, v146\n\t"
      "v_exp_f32 v147, v147\n\t"
      "v_mfma_f32_32x32x16_bf16 v[96:111], v[236:239], v[36:39], v[96:111]\n\t"
      "s_add_u32 m0, m0, 0x1000\n\t"
      "v_exp_f32 v148, v148\n\t"
      "v_exp_f32 v149, v149\n\t"
      "v_exp_f32 v150, v150\n\t"
      "v_exp_f32 v151, v151\n\t"
      "v_smfmac_f32_32x32x32_bf16 v[192:207], v[240:243], v[128:135], %[idx]\n\t"
      "s_add_u32 %[w1o], %[w1o], 0x1000\n\t"
      "v_exp_f32 v152, v152\n\t"
      "v_exp_f32 v153, v153\n\t"
      "v_exp_f32 v154, v154\n\t"
      "v_exp_f32 v155, v155\n\t"
      "v_smfmac_f32_32x32x32_bf16 v[192:207], v[244:247], v[136:143], %[idx]\n\t"
      "v_exp_f32 v156, v156\n\t"
      "v_exp_f32 v157, v157\n\t"
      "v_exp_f32 v158, v158\n\t"
      "v_exp_f32 v159, v159\n\t"
      "v_mfma_f32_32x32x16_bf16 v[112:127], v[232:235], v[40:43], 0\n\t"
      "buffer_load_dwordx4 a[160:163], %[l16], %[ws], %[w2o] offen offset:0 sc1\n\t"
      "v_exp_f32 v96, v96\n\t"
      "v_exp_f32 v97, v97\n\t"
      "v_exp_f32 v98, v98\n\t"
      "v_exp_f32 v99, v99\n\t"
      "v_mfma_f32_32x32x16_bf16 v[112:127], v[236:239], v[44:47], v[112:127]\n\t"
      "buffer_load_dwordx4 a[164:167], %[l16], %[ws], %[w2o] offen offset:1024 sc1\n\t"
      "s_add_u32 %[w2o], %[w2o], 0xb000\n\t"
      "v_exp_f32 v100, v100\n\t"
      "v_exp_f32 v101, v101\n\t"
      "v_exp_f32 v102, v102\n\t"
      "v_exp_f32 v103, v103\n\t"
      "v_smfmac_f32_32x32x32_bf16 v[208:223], v[240:243], v[144:151], %[idx]\n\t"
      "buffer_load_dwordx4 v[240:243], %[l16], %[ws], %[kv] offen offset:2048 sc1\n\t"
      "v_exp_f32 v104, v104\n\t"
      "v_exp_f32 v105, v105\n\t"
      "v_exp_f32 v106, v106\n\t"
      "v_exp_f32 v107, v107\n\t"
      "v_smfmac_f32_32x32x32_bf16 v[208:223], v[244:247], v[152:159], %[idx]\n\t"
      "buffer_load_dwordx4 a[168:171], %[l16], %[ws], %[w2o] offen offset:0 sc1\n\t"
      "v_exp_f32 v108, v108\n\t"
      "v_exp_f32 v109, v109\n\t"
      "v_exp_f32 v110, v110\n\t"
      "v_exp_f32 v111, v111\n\t"
      "v_mfma_f32_32x32x16_bf16 v[128:143], v[232:235], v[48:51], 0\n\t"
      "buffer_load_dwordx4 v[244:247], %[l16], %[ws], %[kv] offen offset:3072 sc1\n\t"
      "v_exp_f32 v112, v112\n\t"
      "v_exp_f32 v113, v113\n\t"
      "v_exp_f32 v114, v114\n\t"
      "v_exp_f32 v115, v115\n\t"
      "v_mfma_f32_32x32x16_bf16 v[128:143], v[236:239], v[52:55], v[128:143]\n\t"
      "s_waitcnt vmcnt(30)\n\t"
      "v_exp_f32 v116, v116\n\t"
      "v_exp_f32 v117, v117\n\t"
      "v_exp_f32 v118, v118\n\t"
      "v_exp_f32 v119, v119\n\t"
      "v_smfmac_f32_32x32x32_bf16 v[160:175], v[248:251], v[96:103], %[idx]\n\t"
      "buffer_load_dwordx4 %[l16], %[ws], %[w1o] offen offset:0 sc1 lds\n\t"
      "v_exp_f32 v120, v120\n\t"
      "v_exp_f32 v121, v121\n\t"
      "v_exp_f32 v122, v122\n\t"
      "v_exp_f32 v123, v123\n\t"
      "v_smfmac_f32_32x32x32_bf16 v[160:175], v[252:255], v[104:111], %[idx]\n\t"
      "s_add_u32 %[kv], %[kv], 0x1000\n\t"
      "v_exp_f32 v124, v124\n\t"
      "v_exp_f32 v125, v125\n\t"
      "v_exp_f32 v126, v126\n\t"
      "v_exp_f32 v127, v127\n\t"
      "v_mfma_f32_32x32x16_bf16 v[144:159], v[232:235], v[56:59], 0\n\t"
      "buffer_load_dwordx4 v[232:235], %[l16], %[ws], %[kv] offen offset:0 sc1\n\t"
      "v_exp_f32 v128, v128\n\t"
      "v_exp_f32 v129, v129\n\t"
      "v_exp_f32 v130, v130\n\t"
      "v_exp_f32 v131, v131\n\t"
      "v_mfma_f32_32x32x16_bf16 v[144:159], v[236:239], v[60:63], v[144:159]\n\t"
      "buffer_load_dwordx4 a[172:175], %[l16], %[ws], %[w2o] offen offset:1024 sc1\n\t"
      "v_exp_f32 v132, v132\n\t"
      "v_exp_f32 v133, v133\n\t"
      "v_exp_f32 v134, v134\n\t"
      "v_exp_f32 v135, v135\n\t"
      "v_smfmac_f32_32x32x32_bf16 v[176:191], v[248:251], v[112:119], %[idx]\n\t"
      "buffer_load_dwordx4 v[236:239], %[l16], %[ws], %[kv] offen offset:1024 sc1\n\t"
      "v_exp_f32 v136, v136\n\t"
      "v_exp_f32 v137, v137\n\t"
      "v_exp_f32 v138, v138\n\t"
      "v_exp_f32 v139, v139\n\t"
      "v_smfmac_f32_32x32x32_bf16 v[176:191], v[252:255], v[120:127], %[idx]\n\t"
      "s_waitcnt vmcnt(30)\n\t"
      "v_exp_f32 v140, v140\n\t"
      "v_exp_f32 v141, v141\n\t"
      "v_exp_f32 v142, v142\n\t"
      "v_exp_f32 v143, v143\n\t"
      "v_mfma_f32_32x32x16_bf16 v[96:111], v[64:67], v[32:35], 0\n\t"
      "buffer_load_dwordx4 %[l16], %[ws], %[w1o] offen offset:1024 sc1 lds\n\t"
      "v_exp_f32 v144, v144\n\t"
      "v_exp_f32 v145, v145\n\t"
      "v_exp_f32 v146, v146\n\t"
      "v_exp_f32 v147, v147\n\t"
      "v_mfma_f32_32x32x16_bf16 v[96:111], v[68:71], v[36:39], v[96:111]\n\t"
      "v_exp_f32 v148, v148\n\t"
      "v_exp_f32 v149, v149\n\t"
      "v_exp_f32 v150, v150\n\t"
      "v_exp_f32 v151, v151\n\t"
      "v_smfmac_f32_32x32x32_bf16 v[192:207], v[248:251], v[128:135], %[idx]\n\t"
      "v_exp_f32 v152, v152\n\t"
      "v_exp_f32 v153, v153\n\t"
      "v_exp_f32 v154, v154\n\t"
      "v_exp_f32 v155, v155\n\t"
      "v_smfmac_f32_32x32x32_bf16 v[192:207], v[252:255], v[136:143], %[idx]\n\t"
      "v_exp_f32 v156, v156\n\t"
      "v_exp_f32 v157, v157\n\t"
      "v_exp_f32 v158, v158\n\t"
      "v_exp_f32 v159, v159\n\t"
      "v_mfma_f32_32x32x16_bf16 v[112:127], v[64:67], v[40:43], 0\n\t"
      "buffer_load_dwordx4 a[176:179], %[l16], %[ws], %[w2o] offen offset:2048 sc1\n\t"
      "v_exp_f32 v96, v96\n\t"
      "v_exp_f32 v97, v97\n\t"
      "v_exp_f32 v98, v98\n\t"
      "v_exp_f32 v99, v99\n\t"
      "v_mfma_f32_32x32x16_bf16 v[112:127], v[68:71], v[44:47], v[112:127]\n\t"
      "buffer_load_dwordx4 a[180:183], %[l16], %[ws], %[w2o] offen offset:3072 sc1\n\t"
      "s_add_u32 %[w2o], %[w2o], 0x1000\n\t"
      "v_exp_f32 v100, v100\n\t"
      "v_exp_f32 v101, v101\n\t"
      "v_exp_f32 v102, v102\n\t"
      "v_exp_f32 v103, v103\n\t"
      "v_smfmac_f32_32x32x32_bf16 v[208:223], v[248:251], v[144:151], %[idx]\n\t"
      "buffer_load_dwordx4 v[248:251], %[l16], %[ws], %[kv] offen offset:2048 sc1\n\t"
      "v_exp_f32 v104, v104\n\t"
      "v_exp_f32 v105, v105\n\t"
      "v_exp_f32 v106, v106\n\t"
      "v_exp_f32 v107, v107\n\t"
      "v_smfmac_f32_32x32x32_bf16 v[208:223], v[252:255], v[152:159], %[idx]\n\t"
      "buffer_load_dwordx4 a[184:187], %[l16], %[ws], %[w2o] offen offset:0 sc1\n\t"
      "v_exp_f32 v108, v108\n\t"
      "v_exp_f32 v109, v109\n\t"
      "v_exp_f32 v110, v110\n\t"
      "v_exp_f32 v111, v111\n\t"
      "v_mfma_f32_32x32x16_bf16 v[128:143], v[64:67], v[48:51], 0\n\t"
      "buffer_load_dwordx4 v[252:255], %[l16], %[ws], %[kv] offen offset:3072 sc1\n\t"
      "v_exp_f32 v112, v112\n\t"
      "v_exp_f32 v113, v113\n\t"
      "v_exp_f32 v114, v114\n\t"
      "v_exp_f32 v115, v115\n\t"
      "v_mfma_f32_32x32x16_bf16 v[128:143], v[68:71], v[52:55], v[128:143]\n\t"
      "s_waitcnt vmcnt(30)\n\t"
      "v_exp_f32 v116, v116\n\t"
      "v_exp_f32 v117, v117\n\t"
      "v_exp_f32 v118, v118\n\t"
      "v_exp_f32 v119, v119\n\t"
      "v_smfmac_f32_32x32x32_bf16 v[160:175], v[80:83], v[96:103], %[idx]\n\t"
      "buffer_load_dwordx4 %[l16], %[ws], %[w1o] offen offset:2048 sc1 lds\n\t"
      "v_exp_f32 v120, v120\n\t"
      "v_exp_f32 v121, v121\n\t"
      "v_exp_f32 v122, v122\n\t"
      "v_exp_f32 v123, v123\n\t"
      "v_smfmac_f32_32x32x32_bf16 v[160:175], v[84:87], v[104:111], %[idx]\n\t"
      "s_add_u32 %[kv], %[kv], 0x1000\n\t"
      "v_exp_f32 v124, v124\n\t"
      "v_exp_f32 v125, v125\n\t"
      "v_exp_f32 v126, v126\n\t"
      "v_exp_f32 v127, v127\n\t"
      "v_mfma_f32_32x32x16_bf16 v[144:159], v[64:67], v[56:59], 0\n\t"
      "buffer_load_dwordx4 v[64:67], %[l16], %[ws], %[kv] offen offset:0 sc1\n\t"
      "v_exp_f32 v128, v128\n\t"
      "v_exp_f32 v129, v129\n\t"
      "v_exp_f32 v130, v130\n\t"
      "v_exp_f32 v131, v131\n\t"
      "v_mfma_f32_32x32x16_bf16 v[144:159], v[68:71], v[60:63], v[144:159]\n\t"
      "buffer_load_dwordx4 a[188:191], %[l16], %[ws], %[w2o] offen offset:1024 sc1\n\t"
      "v_exp_f32 v132, v132\n\t"
      "v_exp_f32 v133, v133\n\t"
      "v_exp_f32 v134, v134\n\t"
      "v_exp_f32 v135, v135\n\t"
      "v_smfmac_f32_32x32x32_bf16 v[176:191], v[80:83], v[112:119], %[idx]\n\t"
      "buffer_load_dwordx4 v[68:71], %[l16], %[ws], %[kv] offen offset:1024 sc1\n\t"
      "v_exp_f32 v136, v136\n\t"
      "v_exp_f32 v137, v137\n\t"
      "v_exp_f32 v138, v138\n\t"
      "v_exp_f32 v139, v139\n\t"
      "v_smfmac_f32_32x32x32_bf16 v[176:191], v[84:87], v[120:127], %[idx]\n\t"
      "s_waitcnt vmcnt(30)\n\t"
      "v_exp_f32 v140, v140\n\t"
      "v_exp_f32 v141, v141\n\t"
      "v_exp_f32 v142, v142\n\t"
      "v_exp_f32 v143, v143\n\t"
      "v_mfma_f32_32x32x16_bf16 v[96:111], v[72:75], v[32:35], 0\n\t"
      "buffer_load_dwordx4 %[l16], %[ws], %[w1o] offen offset:3072 sc1 lds\n\t"
      "v_exp_f32 v144, v144\n\t"
      "v_exp_f32 v145, v145\n\t"
      "v_exp_f32 v146, v146\n\t"
      "v_exp_f32 v147, v147\n\t"
      "v_mfma_f32_32x32x16_bf16 v[96:111], v[76:79], v[36:39], v[96:111]\n\t"
      "s_add_u32 m0, m0, 0x1000\n\t"
      "v_exp_f32 v148, v148\n\t"
      "v_exp_f32 v149, v149\n\t"
      "v_exp_f32 v150, v150\n\t"
      "v_exp_f32 v151, v151\n\t"
      "v_smfmac_f32_32x32x32_bf16 v[192:207], v[80:83], v[128:135], %[idx]\n\t"
      "s_add_u32 %[w1o], %[w1o], 0x1000\n\t"
      "v_exp_f32 v152, v152\n\t"
      "v_exp_f32 v153, v153\n\t"
      "v_exp_f32 v154, v154\n\t"
      "v_exp_f32 v155, v155\n\t"
      "v_smfmac_f32_32x32x32_bf16 v[192:207], v[84:87], v[136:143], %[idx]\n\t"
      "v_exp_f32 v156, v156\n\t"
      "v_exp_f32 v157, v157\n\t"
      "v_exp_f32 v158, v158\n\t"
      "v_exp_f32 v159, v159\n\t"
      "v_mfma_f32_32x32x16_bf16 v[112:127], v[72:75], v[40:43], 0\n\t"
      "buffer_load_dword v64, %[pfv], %[ws], %[pfo] offen sc1\n\t"
      "v_exp_f32 v96, v96\n\t"
      "v_exp_f32 v97, v97\n\t"
      "v_exp_f32 v98, v98\n\t"
      "v_exp_f32 v99, v99\n\t"
      "v_mfma_f32_32x32x16_bf16 v[112:127], v[76:79], v[44:47], v[112:127]\n\t"
      "v_exp_f32 v100, v100\n\t"
      "v_exp_f32 v101, v101\n\t"
      "v_exp_f32 v102, v102\n\t"
      "v_exp_f32 v103, v103\n\t"
      "v_smfmac_f32_32x32x32_bf16 v[208:223], v[80:83], v[144:151], %[idx]\n\t"
      "buffer_load_dwordx4 a[192:195], %[l16], %[ws], %[w2o] offen offset:2048 sc1\n\t"
      "v_exp_f32 v104, v104\n\t"
      "v_exp_f32 v105, v105\n\t"
      "v_exp_f32 v106, v106\n\t"
      "v_exp_f32 v107, v107\n\t"
      "v_smfmac_f32_32x32x32_bf16 v[208:223], v[84:87], v[152:159], %[idx]\n\t"
      "v_exp_f32 v108, v108\n\t"
      "v_exp_f32 v109, v109\n\t"
      "v_exp_f32 v110, v110\n\t"
      "v_exp_f32 v111, v111\n\t"
      "v_mfma_f32_32x32x16_bf16 v[128:143], v[72:75], v[48:51], 0\n\t"
      "buffer_load_dwordx4 a[196:199], %[l16], %[ws], %[w2o] offen offset:3072 sc1\n\t"
      "s_add_u32 %[w2o], %[w2o], 0x1000\n\t"
      "v_exp_f32 v112, v112\n\t"
      "v_exp_f32 v113, v113\n\t"
      "v_exp_f32 v114, v114\n\t"
      "v_exp_f32 v115, v115\n\t"
      "v_mfma_f32_32x32x16_bf16 v[128:143], v[76:79], v[52:55], v[128:143]\n\t"
      "s_waitcnt vmcnt(28)\n\t"
      "v_exp_f32 v116, v116\n\t"
      "v_exp_f32 v117, v117\n\t"
      "v_exp_f32 v118, v118\n\t"
      "v_exp_f32 v119, v119\n\t"
      "v_smfmac_f32_32x32x32_bf16 v[160:175], v[88:91], v[96:103], %[idx]\n\t"
      "buffer_load_dwordx4 a[200:203], %[l16], %[ws], %[w2o] offen offset:0 sc1\n\t"
      "v_exp_f32 v120, v120\n\t"
      "v_exp_f32 v121, v121\n\t"
      "v_exp_f32 v122, v122\n\t"
      "v_exp_f32 v123, v123\n\t"
      "v_smfmac_f32_32x32x32_bf16 v[160:175], v[92:95], v[104:111], %[idx]\n\t"
      "v_exp_f32 v124, v124\n\t"
      "v_exp_f32 v125, v125\n\t"
      "v_exp_f32 v126, v126\n\t"
      "v_exp_f32 v127, v127\n\t"
      "v_mfma_f32_32x32x16_bf16 v[144:159], v[72:75], v[56:59], 0\n\t"
      "buffer_load_dwordx4 a[204:207], %[l16], %[ws], %[w2o] offen offset:1024 sc1\n\t"
      "v_exp_f32 v128, v128\n\t"
      "v_exp_f32 v129, v129\n\t"
      "v_exp_f32 v130, v130\n\t"
      "v_exp_f32 v131, v131\n\t"
      "v_mfma_f32_32x32x16_bf16 v[144:159], v[76:79], v[60:63], v[144:159]\n\t"
      "v_exp_f32 v132, v132\n\t"
      "v_exp_f32 v133, v133\n\t"
      "v_exp_f32 v134, v134\n\t"
      "v_exp_f32 v135, v135\n\t"
      "v_smfmac_f32_32x32x32_bf16 v[176:191], v[88:91], v[112:119], %[idx]\n\t"
      "v_exp_f32 v136, v136\n\t"
      "v_exp_f32 v137, v137\n\t"
      "v_exp_f32 v138, v138\n\t"
      "v_exp_f32 v139, v139\n\t"
      "v_smfmac_f32_32x32x32_bf16 v[176:191], v[92:95], v[120:127], %[idx]\n\t"
      "s_waitcnt vmcnt(26)\n\t"
      "v_exp_f32 v140, v140\n\t"
      "v_exp_f32 v141, v141\n\t"
      "v_exp_f32 v142, v142\n\t"
      "v_exp_f32 v143, v143\n\t"
      "v_mfma_f32_32x32x16_bf16 v[96:111], v[224:227], v[32:35], 0\n\t"
      "v_exp_f32 v144, v144\n\t"
      "v_exp_f32 v145, v145\n\t"
      "v_exp_f32 v146, v146\n\t"
      "v_exp_f32 v147, v147\n\t"
      "v_mfma_f32_32x32x16_bf16 v[96:111], v[228:231], v[36:39], v[96:111]\n\t"
      "v_exp_f32 v148, v148\n\t"
      "v_exp_f32 v149, v149\n\t"
      "v_exp_f32 v150, v150\n\t"
      "v_exp_f32 v151, v151\n\t"
      "v_smfmac_f32_32x32x32_bf16 v[192:207], v[88:91], v[128:135], %[idx]\n\t"
      "v_exp_f32 v152, v152\n\t"
      "v_exp_f32 v153, v153\n\t"
      "v_exp_f32 v154, v154\n\t"
      "v_exp_f32 v155, v155\n\t"
      "v_smfmac_f32_32x32x32_bf16 v[192:207], v[92:95], v[136:143], %[idx]\n\t"
      "v_exp_f32 v156, v156\n\t"
      "v_exp_f32 v157, v157\n\t"
      "v_exp_f32 v158, v158\n\t"
      "v_exp_f32 v159, v159\n\t"
      "v_mfma_f32_32x32x16_bf16 v[112:127], v[224:227], v[40:43], 0\n\t"
      "s_waitcnt vmcnt(6)\n\t"
      "v_mov_b32 v68, %[tl]\n\t"
      "v_mov_b32 v69, %[th]\n\t"
      "v_mov_b32 v70, %[tl]\n\t"
      "v_mov_b32 v71, %[th]\n\t"
      "v_exp_f32 v96, v96\n\t"
      "v_exp_f32 v97, v97\n\t"
      "v_exp_f32 v98, v98\n\t"
      "v_exp_f32 v99, v99\n\t"
      "v_mfma_f32_32x32x16_bf16 v[112:127], v[228:231], v[44:47], v[112:127]\n\t"
      "s_barrier\n\t"
      "buffer_store_dwordx4 v[68:71], %[z0], %[ws], %[co] offen sc1\n\t"
      "v_exp_f32 v100, v100\n\t"
      "v_exp_f32 v101, v101\n\t"
      "v_exp_f32 v102, v102\n\t"
      "v_exp_f32 v103, v103\n\t"
      "v_smfmac_f32_32x32x32_bf16 v[208:223], v[88:91], v[144:151], %[idx]\n\t"
      "buffer_load_dwordx4 a[208:211], %[l16], %[ws], %[w2o] offen offset:2048 sc1\n\t"
      "v_exp_f32 v104, v104\n\t"
      "v_exp_f32 v105, v105\n\t"
      "v_exp_f32 v106, v106\n\t"
      "v_exp_f32 v107, v107\n\t"
      "v_smfmac_f32_32x32x32_bf16 v[208:223], v[92:95], v[152:159], %[idx]\n\t"
      "v_exp_f32 v108, v108\n\t"
      "v_exp_f32 v109, v109\n\t"
      "v_exp_f32 v110, v110\n\t"
      "v_exp_f32 v111, v111\n\t"
      "v_mfma_f32_32x32x16_bf16 v[128:143], v[224:227], v[48:51], 0\n\t"
      "buffer_load_dwordx4 a[212:215], %[l16], %[ws], %[w2o] offen offset:3072 sc1\n\t"
      "s_add_u32 %[w2o], %[w2o], 0x1000\n\t"
      "v_exp_f32 v112, v112\n\t"
      "v_exp_f32 v113, v113\n\t"
      "v_exp_f32 v114, v114\n\t"
      "v_exp_f32 v115, v115\n\t"
      "v_mfma_f32_32x32x16_bf16 v[128:143], v[228:231], v[52:55], v[128:143]\n\t"
      "v_exp_f32 v116, v116\n\t"
      "v_exp_f32 v117, v117\n\t"
      "v_exp_f32 v118, v118\n\t"
      "v_exp_f32 v119, v119\n\t"
      "v_smfmac_f32_32x32x32_bf16 v[160:175], v[240:243], v[96:103], %[idx]\n\t"
      "buffer_load_dwordx4 a[216:219], %[l16], %[ws], %[w2o] offen offset:0 sc1\n\t"
      "v_exp_f32 v120, v120\n\t"
      "v_exp_f32 v121, v121\n\t"
      "v_exp_f32 v122, v122\n\t"
      "v_exp_f32 v123, v123\n\t"
      "v_smfmac_f32_32x32x32_bf16 v[160:175], v[244:247], v[104:111], %[idx]\n\t"
      "v_exp_f32 v124, v124\n\t"
      "v_exp_f32 v125, v125\n\t"
      "v_exp_f32 v126, v126\n\t"
      "v_exp_f32 v127, v127\n\t"
      "v_mfma_f32_32x32x16_bf16 v[144:159], v[224:227], v[56:59], 0\n\t"
      "buffer_load_dwordx4 a[220:223], %[l16], %[ws], %[w2o] offen offset:1024 sc1\n\t"
      "v_exp_f32 v128, v128\n\t"
      "v_exp_f32 v129, v129\n\t"
      "v_exp_f32 v130, v130\n\t"
      "v_exp_f32 v131, v131\n\t"
      "v_mfma_f32_32x32x16_bf16 v[144:159], v[228:231], v[60:63], v[144:159]\n\t"
      "v_exp_f32 v132, v132\n\t"
      "v_exp_f32 v133, v133\n\t"
      "v_exp_f32 v134, v134\n\t"
      "v_exp_f32 v135, v135\n\t"
      "v_smfmac_f32_32x32x32_bf16 v[176:191], v[240:243], v[112:119], %[idx]\n\t"
      "v_exp_f32 v136, v136\n\t"
      "v_exp_f32 v137, v137\n\t"
      "v_exp_f32 v138, v138\n\t"
      "v_exp_f32 v139, v139\n\t"
      "v_smfmac_f32_32x32x32_bf16 v[176:191], v[244:247], v[120:127], %[idx]\n\t"
      "v_exp_f32 v140, v140\n\t"
      "v_exp_f32 v141, v141\n\t"
      "v_exp_f32 v142, v142\n\t"
      "v_exp_f32 v143, v143\n\t"
      "v_mfma_f32_32x32x16_bf16 v[96:111], v[232:235], v[32:35], 0\n\t"
      "v_exp_f32 v144, v144\n\t"
      "v_exp_f32 v145, v145\n\t"
      "v_exp_f32 v146, v146\n\t"
      "v_exp_f32 v147, v147\n\t"
      "v_mfma_f32_32x32x16_bf16 v[96:111], v[236:239], v[36:39], v[96:111]\n\t"
      "v_exp_f32 v148, v148\n\t"
      "v_exp_f32 v149, v149\n\t"
      "v_exp_f32 v150, v150\n\t"
      "v_exp_f32 v151, v151\n\t"
      "v_smfmac_f32_32x32x32_bf16 v[192:207], v[240:243], v[128:135], %[idx]\n\t"
      "v_exp_f32 v152, v152\n\t"
      "v_exp_f32 v153, v153\n\t"
      "v_exp_f32 v154, v154\n\t"
      "v_exp_f32 v155, v155\n\t"
      "v_smfmac_f32_32x32x32_bf16 v[192:207], v[244:247], v[136:143], %[idx]\n\t"
      "v_exp_f32 v156, v156\n\t"
      "v_exp_f32 v157, v157\n\t"
      "v_exp_f32 v158, v158\n\t"
      "v_exp_f32 v159, v159\n\t"
      "v_mfma_f32_32x32x16_bf16 v[112:127], v[232:235], v[40:43], 0\n\t"
      "v_exp_f32 v96, v96\n\t"
      "v_exp_f32 v97, v97\n\t"
      "v_exp_f32 v98, v98\n\t"
      "v_exp_f32 v99, v99\n\t"
      "v_mfma_f32_32x32x16_bf16 v[112:127], v[236:239], v[44:47], v[112:127]\n\t"
      "v_exp_f32 v100, v100\n\t"
      "v_exp_f32 v101, v101\n\t"
      "v_exp_f32 v102, v102\n\t"
      "v_exp_f32 v103, v103\n\t"
      "v_smfmac_f32_32x32x32_bf16 v[208:223], v[240:243], v[144:151], %[idx]\n\t"
      "v_exp_f32 v104, v104\n\t"
      "v_exp_f32 v105, v105\n\t"
      "v_exp_f32 v106, v106\n\t"
      "v_exp_f32 v107, v107\n\t"
      "v_smfmac_f32_32x32x32_bf16 v[208:223], v[244:247], v[152:159], %[idx]\n\t"
      "v_exp_f32 v108, v108\n\t"
      "v_exp_f32 v109, v109\n\t"
      "v_exp_f32 v110, v110\n\t"
      "v_exp_f32 v111, v111\n\t"
      "v_mfma_f32_32x32x16_bf16 v[128:143], v[232:235], v[48:51], 0\n\t"
      "v_exp_f32 v112, v112\n\t"
      "v_exp_f32 v113, v113\n\t"
      "v_exp_f32 v114, v114\n\t"
      "v_exp_f32 v115, v115\n\t"
      "v_mfma_f32_32x32x16_bf16 v[128:143], v[236:239], v[52:55], v[128:143]\n\t"
      "v_exp_f32 v116, v116\n\t"
      "v_exp_f32 v117, v117\n\t"
      "v_exp_f32 v118, v118\n\t"
      "v_exp_f32 v119, v119\n\t"
      "v_smfmac_f32_32x32x32_bf16 v[160:175], v[248:251], v[96:103], %[idx]\n\t"
      "v_exp_f32 v120, v120\n\t"
      "v_exp_f32 v121, v121\n\t"
      "v_exp_f32 v122, v122\n\t"
      "v_exp_f32 v123, v123\n\t"
      "v_smfmac_f32_32x32x32_bf16 v[160:175], v[252:255], v[104:111], %[idx]\n\t"
      "v_exp_f32 v124, v124\n\t"
      "v_exp_f32 v125, v125\n\t"
      "v_exp_f32 v126, v126\n\t"
      "v_exp_f32 v127, v127\n\t"
      "v_mfma_f32_32x32x16_bf16 v[144:159], v[232:235], v[56:59], 0\n\t"
      "v_exp_f32 v128, v128\n\t"
      "v_exp_f32 v129, v129\n\t"
      "v_exp_f32 v130, v130\n\t"
      "v_exp_f32 v131, v131\n\t"
      "v_mfma_f32_32x32x16_bf16 v[144:159], v[236:239], v[60:63], v[144:159]\n\t"
      "s_waitcnt vmcnt(9)\n\t"
      "v_exp_f32 v132, v132\n\t"
      "v_exp_f32 v133, v133\n\t"
      "v_exp_f32 v134, v134\n\t"
      "v_exp_f32 v135, v135\n\t"
      "v_smfmac_f32_32x32x32_bf16 v[176:191], v[248:251], v[112:119], %[idx]\n\t"
      "ds_write_b128 %[wr0], v[160:163] offset:0\n\t"
      "v_exp_f32 v136, v136\n\t"
      "v_exp_f32 v137, v137\n\t"
      "v_exp_f32 v138, v138\n\t"
      "v_exp_f32 v139, v139\n\t"
      "v_smfmac_f32_32x32x32_bf16 v[176:191], v[252:255], v[120:127], %[idx]\n\t"
      "ds_write_b128 %[wr0], v[164:167] offset:1024\n\t"
      "v_exp_f32 v140, v140\n\t"
      "v_exp_f32 v141, v141\n\t"
      "v_exp_f32 v142, v142\n\t"
      "v_exp_f32 v143, v143\n\t"
      "ds_write_b128 %[wr0], v[168:171] offset:2048\n\t"
      "ds_write_b128 %[wr0], v[172:175] offset:3072\n\t"
      "v_exp_f32 v144, v144\n\t"
      "v_exp_f32 v145, v145\n\t"
      "v_exp_f32 v146, v146\n\t"
      "v_exp_f32 v147, v147\n\t"
      "ds_write_b128 %[wr1], v[176:179] offset:0\n\t"
      "ds_write_b128 %[wr1], v[180:183] offset:1024\n\t"
      "v_exp_f32 v148, v148\n\t"
      "v_exp_f32 v149, v149\n\t"
      "v_exp_f32 v150, v150\n\t"
      "v_exp_f32 v151, v151\n\t"
      "v_smfmac_f32_32x32x32_bf16 v[192:207], v[248:251], v[128:135], %[idx]\n\t"
      "ds_write_b128 %[wr1], v[184:187] offset:2048\n\t"
      "v_exp_f32 v152, v152\n\t"
      "v_exp_f32 v153, v153\n\t"
      "v_exp_f32 v154, v154\n\t"
      "v_exp_f32 v155, v155\n\t"
      "v_smfmac_f32_32x32x32_bf16 v[192:207], v[252:255], v[136:143], %[idx]\n\t"
      "ds_write_b128 %[wr1], v[188:191] offset:3072\n\t"
      "v_exp_f32 v156, v156\n\t"
      "v_exp_f32 v157, v157\n\t"
      "v_exp_f32 v158, v158\n\t"
      "v_exp_f32 v159, v159\n\t"
      "3:\n\t"
      "s_nop 1\n\t"
      "v_smfmac_f32_32x32x32_bf16 v[208:223], v[248:251], v[144:151], %[idx]\n\t"
      "v_smfmac_f32_32x32x32_bf16 v[208:223], v[252:255], v[152:159], %[idx]\n\t"
      "s_nop 3\n\t"
      "ds_write_b128 %[wr2], v[192:195] offset:0\n\t"
      "ds_write_b128 %[wr2], v[196:199] offset:1024\n\t"
      "ds_write_b128 %[wr2], v[200:203] offset:2048\n\t"
      "ds_write_b128 %[wr2], v[204:207] offset:3072\n\t"
      "s_nop 3\n\t"
      "s_waitcnt lgkmcnt(0)\n\t"
      "s_barrier\n\t"
      "ds_read_b128 v[96:99], %[rd] offset:0\n\t"
      "ds_read_b128 v[100:103], %[rd] offset:1024\n\t"
      "ds_read_b128 v[104:107], %[rd] offset:2048\n\t"
      "ds_read_b128 v[108:111], %[rd] offset:3072\n\t"
      "ds_read_b128 v[112:115], %[rd] offset:4096\n\t"
      "ds_read_b128 v[116:119], %[rd] offset:5120\n\t"
      "ds_read_b128 v[120:123], %[rd] offset:6144\n\t"
      "ds_read_b128 v[124:127], %[rd] offset:7168\n\t"
      "ds_read_b128 v[128:131], %[rd] offset:8192\n\t"
      "ds_read_b128 v[132:135], %[rd] offset:9216\n\t"
      "ds_read_b128 v[136:139], %[rd] offset:10240\n\t"
      "ds_read_b128 v[140:143], %[rd] offset:11264\n\t"
      "s_waitcnt lgkmcnt(11)\n\t"
      "v_pk_add_f32 v[208:209], v[208:209], v[96:97]\n\t"
      "v_pk_add_f32 v[210:211], v[210:211], v[98:99]\n\t"
      "s_waitcnt lgkmcnt(10)\n\t"
      "v_pk_add_f32 v[212:213], v[212:213], v[100:101]\n\t"
      "v_pk_add_f32 v[214:215], v[214:215], v[102:103]\n\t"
      "s_waitcnt lgkmcnt(9)\n\t"
      "v_pk_add_f32 v[216:217], v[216:217], v[104:105]\n\t"
      "v_pk_add_f32 v[218:219], v[218:219], v[106:107]\n\t"
      "s_waitcnt lgkmcnt(8)\n\t"
      "v_pk_add_f32 v[220:221], v[220:221], v[108:109]\n\t"
      "v_pk_add_f32 v[222:223], v[222:223], v[110:111]\n\t"
      "s_waitcnt lgkmcnt(7)\n\t"
      "v_pk_add_f32 v[208:209], v[208:209], v[112:113]\n\t"
      "v_pk_add_f32 v[210:211], v[210:211], v[114:115]\n\t"
      "s_waitcnt lgkmcnt(6)\n\t"
      "v_pk_add_f32 v[212:213], v[212:213], v[116:117]\n\t"
      "v_pk_add_f32 v[214:215], v[214:215], v[118:119]\n\t"
      "s_waitcnt lgkmcnt(5)\n\t"
      "v_pk_add_f32 v[216:217], v[216:217], v[120:121]\n\t"
      "v_pk_add_f32 v[218:219], v[218:219], v[122:123]\n\t"
      "s_waitcnt lgkmcnt(4)\n\t"
      "v_pk_add_f32 v[220:221], v[220:221], v[124:125]\n\t"
      "v_pk_add_f32 v[222:223], v[222:223], v[126:127]\n\t"
      "s_waitcnt lgkmcnt(3)\n\t"
      "v_pk_add_f32 v[208:209], v[208:209], v[128:129]\n\t"
      "v_pk_add_f32 v[210:211], v[210:211], v[130:131]\n\t"
      "s_waitcnt lgkmcnt(2)\n\t"
      "v_pk_add_f32 v[212:213], v[212:213], v[132:133]\n\t"
      "v_pk_add_f32 v[214:215], v[214:215], v[134:135]\n\t"
      "s_waitcnt lgkmcnt(1)\n\t"
      "v_pk_add_f32 v[216:217], v[216:217], v[136:137]\n\t"
      "v_pk_add_f32 v[218:219], v[218:219], v[138:139]\n\t"
      "s_waitcnt lgkmcnt(0)\n\t"
      "v_pk_add_f32 v[220:221], v[220:221], v[140:141]\n\t"
      "v_pk_add_f32 v[222:223], v[222:223], v[142:143]\n\t"
      "v_mov_b32 v32, v220\n\t"
      "v_mov_b32 v33, v220\n\t"
      "s_nop 1\n\t"
      "v_permlane32_swap_b32 v32, v33\n\t"
      "v_rcp_f32 v32, v33\n\t"
      "s_nop 0\n\t"
      "v_pk_mul_f32 v[208:209], v[208:209], v[32:33] op_sel_hi:[1,0]\n\t"
      "v_pk_mul_f32 v[210:211], v[210:211], v[32:33] op_sel_hi:[1,0]\n\t"
      "v_pk_mul_f32 v[212:213], v[212:213], v[32:33] op_sel_hi:[1,0]\n\t"
      "v_pk_mul_f32 v[214:215], v[214:215], v[32:33] op_sel_hi:[1,0]\n\t"
      "v_pk_mul_f32 v[216:217], v[216:217], v[32:33] op_sel_hi:[1,0]\n\t"
      "v_pk_mul_f32 v[218:219], v[218:219], v[32:33] op_sel_hi:[1,0]\n\t"
      "v_pk_mul_f32 v[220:221], v[220:221], v[32:33] op_sel_hi:[1,0]\n\t"
      "v_pk_mul_f32 v[222:223], v[222:223], v[32:33] op_sel_hi:[1,0]\n\t"
      "v_cvt_pk_bf16_f32 v40, v208, v209\n\t"
      "v_cvt_pk_bf16_f32 v41, v210, v211\n\t"
      "v_cvt_pk_bf16_f32 v42, v212, v213\n\t"
      "v_cvt_pk_bf16_f32 v43, v214, v215\n\t"
      "v_cvt_pk_bf16_f32 v44, v216, v217\n\t"
      "v_cvt_pk_bf16_f32 v45, v218, v219\n\t"
      "v_cvt_pk_bf16_f32 v46, v220, v221\n\t"
      "v_cvt_pk_bf16_f32 v47, v222, v223\n\t"
      "buffer_store_dwordx4 v[40:43], %[l16], %[ws], %[oo] offen sc1\n\t"
      "buffer_store_dwordx4 v[44:47], %[l16], %[ws], %[oo] offen offset:1024 sc1\n\t"
      "s_branch 4f\n\t"
      "2:\n\t"
      "1:\n\t"
      "v_mfma_f32_32x32x16_bf16 v[112:127], v[72:75], v[40:43], 0\n\t"
      "v_exp_f32 v96, v96\n\t"
      "v_exp_f32 v97, v97\n\t"
      "v_exp_f32 v98, v98\n\t"
      "v_exp_f32 v99, v99\n\t"
      "v_mfma_f32_32x32x16_bf16 v[112:127], v[76:79], v[44:47], v[112:127]\n\t"
      "v_exp_f32 v100, v100\n\t"
      "v_exp_f32 v101, v101\n\t"
      "v_exp_f32 v102, v102\n\t"
      "v_exp_f32 v103, v103\n\t"
      "v_smfmac_f32_32x32x32_bf16 v[208:223], v[80:83], v[144:151], %[idx]\n\t"
      "buffer_load_dwordx4 v[80:83], %[l16], %[ws], %[kv] offen offset:2048 sc1\n\t"
      "v_exp_f32 v104, v104\n\t"
      "v_exp_f32 v105, v105\n\t"
      "v_exp_f32 v106, v106\n\t"
      "v_exp_f32 v107, v107\n\t"
      "v_smfmac_f32_32x32x32_bf16 v[208:223], v[84:87], v[152:159], %[idx]\n\t"
      "v_exp_f32 v108, v108\n\t"
      "v_exp_f32 v109, v109\n\t"
      "v_exp_f32 v110, v110\n\t"
      "v_exp_f32 v111, v111\n\t"
      "v_mfma_f32_32x32x16_bf16 v[128:143], v[72:75], v[48:51], 0\n\t"
      "buffer_load_dwordx4 v[84:87], %[l16], %[ws], %[kv] offen offset:3072 sc1\n\t"
      "v_exp_f32 v112, v112\n\t"
      "v_exp_f32 v113, v113\n\t"
      "v_exp_f32 v114, v114\n\t"
      "v_exp_f32 v115, v115\n\t"
      "v_mfma_f32_32x32x16_bf16 v[128:143], v[76:79], v[52:55], v[128:143]\n\t"
      "s_waitcnt vmcnt(18)\n\t"
      "v_exp_f32 v116, v116\n\t"
      "v_exp_f32 v117, v117\n\t"
      "v_exp_f32 v118, v118\n\t"
      "v_exp_f32 v119, v119\n\t"
      "v_smfmac_f32_32x32x32_bf16 v[160:175], v[88:91], v[96:103], %[idx]\n\t"
      "buffer_load_dwordx4 %[l16], %[ws], %[w1o] offen offset:0 sc1 lds\n\t"
      "v_exp_f32 v120, v120\n\t"
      "v_exp_f32 v121, v121\n\t"
      "v_exp_f32 v122, v122\n\t"
      "v_exp_f32 v123, v123\n\t"
      "v_smfmac_f32_32x32x32_bf16 v[160:175], v[92:95], v[104:111], %[idx]\n\t"
      "s_add_u32 %[kv], %[kv], 0x1000\n\t"
      "v_exp_f32 v124, v124\n\t"
      "v_exp_f32 v125, v125\n\t"
      "v_exp_f32 v126, v126\n\t"
      "v_exp_f32 v127, v127\n\t"
      "v_mfma_f32_32x32x16_bf16 v[144:159], v[72:75], v[56:59], 0\n\t"
      "buffer_load_dwordx4 v[72:75], %[l16], %[ws], %[kv] offen offset:0 sc1\n\t"
      "v_exp_f32 v128, v128\n\t"
      "v_exp_f32 v129, v129\n\t"
      "v_exp_f32 v130, v130\n\t"
      "v_exp_f32 v131, v131\n\t"
      "v_mfma_f32_32x32x16_bf16 v[144:159], v[76:79], v[60:63], v[144:159]\n\t"
      "v_exp_f32 v132, v132\n\t"
      "v_exp_f32 v133, v133\n\t"
      "v_exp_f32 v134, v134\n\t"
      "v_exp_f32 v135, v135\n\t"
      "v_smfmac_f32_32x32x32_bf16 v[176:191], v[88:91], v[112:119], %[idx]\n\t"
      "buffer_load_dwordx4 v[76:79], %[l16], %[ws], %[kv] offen offset:1024 sc1\n\t"
      "v_exp_f32 v136, v136\n\t"
      "v_exp_f32 v137, v137\n\t"
      "v_exp_f32 v138, v138\n\t"
      "v_exp_f32 v139, v139\n\t"
      "v_smfmac_f32_32x32x32_bf16 v[176:191], v[92:95], v[120:127], %[idx]\n\t"
      "s_waitcnt vmcnt(18)\n\t"
      "v_exp_f32 v140, v140\n\t"
      "v_exp_f32 v141, v141\n\t"
      "v_exp_f32 v142, v142\n\t"
      "v_exp_f32 v143, v143\n\t"
      "v_mfma_f32_32x32x16_bf16 v[96:111], v[224:227], v[32:35], 0\n\t"
      "buffer_load_dwordx4 %[l16], %[ws], %[w1o] offen offset:1024 sc1 lds\n\t"
      "v_exp_f32 v144, v144\n\t"
      "v_exp_f32 v145, v145\n\t"
      "v_exp_f32 v146, v146\n\t"
      "v_exp_f32 v147, v147\n\t"
      "v_mfma_f32_32x32x16_bf16 v[96:111], v[228:231], v[36:39], v[96:111]\n\t"
      "v_exp_f32 v148, v148\n\t"
      "v_exp_f32 v149, v149\n\t"
      "v_exp_f32 v150, v150\n\t"
      "v_exp_f32 v151, v151\n\t"
      "v_smfmac_f32_32x32x32_bf16 v[192:207], v[88:91], v[128:135], %[idx]\n\t"
      "v_exp_f32 v152, v152\n\t"
      "v_exp_f32 v153, v153\n\t"
      "v_exp_f32 v154, v154\n\t"
      "v_exp_f32 v155, v155\n\t"
      "v_smfmac_f32_32x32x32_bf16 v[192:207], v[92:95], v[136:143], %[idx]\n\t"
      "v_exp_f32 v156, v156\n\t"
      "v_exp_f32 v157, v157\n\t"
      "v_exp_f32 v158, v158\n\t"
      "v_exp_f32 v159, v159\n\t"
      "v_mfma_f32_32x32x16_bf16 v[112:127], v[224:227], v[40:43], 0\n\t"
      "v_exp_f32 v96, v96\n\t"
      "v_exp_f32 v97, v97\n\t"
      "v_exp_f32 v98, v98\n\t"
      "v_exp_f32 v99, v99\n\t"
      "v_mfma_f32_32x32x16_bf16 v[112:127], v[228:231], v[44:47], v[112:127]\n\t"
      "v_exp_f32 v100, v100\n\t"
      "v_exp_f32 v101, v101\n\t"
      "v_exp_f32 v102, v102\n\t"
      "v_exp_f32 v103, v103\n\t"
      "v_smfmac_f32_32x32x32_bf16 v[208:223], v[88:91], v[144:151], %[idx]\n\t"
      "buffer_load_dwordx4 v[88:91], %[l16], %[ws], %[kv] offen offset:2048 sc1\n\t"
      "v_exp_f32 v104, v104\n\t"
      "v_exp_f32 v105, v105\n\t"
      "v_exp_f32 v106, v106\n\t"
      "v_exp_f32 v107, v107\n\t"
      "v_smfmac_f32_32x32x32_bf16 v[208:223], v[92:95], v[152:159], %[idx]\n\t"
      "v_exp_f32 v108, v108\n\t"
      "v_exp_f32 v109, v109\n\t"
      "v_exp_f32 v110, v110\n\t"
      "v_exp_f32 v111, v111\n\t"
      "v_mfma_f32_32x32x16_bf16 v[128:143], v[224:227], v[48:51], 0\n\t"
      "buffer_load_dwordx4 v[92:95], %[l16], %[ws], %[kv] offen offset:3072 sc1\n\t"
      "v_exp_f32 v112, v112\n\t"
      "v_exp_f32 v113, v113\n\t"
      "v_exp_f32 v114, v114\n\t"
      "v_exp_f32 v115, v115\n\t"
      "v_mfma_f32_32x32x16_bf16 v[128:143], v[228:231], v[52:55], v[128:143]\n\t"
      "s_waitcnt vmcnt(18)\n\t"
      "v_exp_f32 v116, v116\n\t"
      "v_exp_f32 v117, v117\n\t"
      "v_exp_f32 v118, v118\n\t"
      "v_exp_f32 v119, v119\n\t"
      "v_smfmac_f32_32x32x32_bf16 v[160:175], v[240:243], v[96:103], %[idx]\n\t"
      "buffer_load_dwordx4 %[l16], %[ws], %[w1o] offen offset:2048 sc1 lds\n\t"
      "v_exp_f32 v120, v120\n\t"
      "v_exp_f32 v121, v121\n\t"
      "v_exp_f32 v122, v122\n\t"
      "v_exp_f32 v123, v123\n\t"
      "v_smfmac_f32_32x32x32_bf16 v[160:175], v[244:247], v[104:111], %[idx]\n\t"
      "s_add_u32 %[kv], %[kv], 0x1000\n\t"
      "v_exp_f32 v124, v124\n\t"
      "v_exp_f32 v125, v125\n\t"
      "v_exp_f32 v126, v126\n\t"
      "v_exp_f32 v127, v127\n\t"
      "v_mfma_f32_32x32x16_bf16 v[144:159], v[224:227], v[56:59], 0\n\t"
      "buffer_load_dwordx4 v[224:227], %[l16], %[ws], %[kv] offen offset:0 sc1\n\t"
      "v_exp_f32 v128, v128\n\t"
      "v_exp_f32 v129, v129\n\t"
      "v_exp_f32 v130, v130\n\t"
      "v_exp_f32 v131, v131\n\t"
      "v_mfma_f32_32x32x16_bf16 v[144:159], v[228:231], v[60:63], v[144:159]\n\t"
      "v_exp_f32 v132, v132\n\t"
      "v_exp_f32 v133, v133\n\t"
      "v_exp_f32 v134, v134\n\t"
      "v_exp_f32 v135, v135\n\t"
      "v_smfmac_f32_32x32x32_bf16 v[176:191], v[240:243], v[112:119], %[idx]\n\t"
      "buffer_load_dwordx4 v[228:231], %[l16], %[ws], %[kv] offen offset:1024 sc1\n\t"
      "v_exp_f32 v136, v136\n\t"
      "v_exp_f32 v137, v137\n\t"
      "v_exp_f32 v138, v138\n\t"
      "v_exp_f32 v139, v139\n\t"
      "v_smfmac_f32_32x32x32_bf16 v[176:191], v[244:247], v[120:127], %[idx]\n\t"
      "s_waitcnt vmcnt(18)\n\t"
      "v_exp_f32 v140, v140\n\t"
      "v_exp_f32 v141, v141\n\t"
      "v_exp_f32 v142, v142\n\t"
      "v_exp_f32 v143, v143\n\t"
      "v_mfma_f32_32x32x16_bf16 v[96:111], v[232:235], v[32:35], 0\n\t"
      "buffer_load_dwordx4 %[l16], %[ws], %[w1o] offen offset:3072 sc1 lds\n\t"
      "v_exp_f32 v144, v144\n\t"
      "v_exp_f32 v145, v145\n\t"
      "v_exp_f32 v146, v146\n\t"
      "v_exp_f32 v147, v147\n\t"
      "v_mfma_f32_32x32x16_bf16 v[96:111], v[236:239], v[36:39], v[96:111]\n\t"
      "s_add_u32 m0, m0, 0x1000\n\t"
      "v_exp_f32 v148, v148\n\t"
      "v_exp_f32 v149, v149\n\t"
      "v_exp_f32 v150, v150\n\t"
      "v_exp_f32 v151, v151\n\t"
      "v_smfmac_f32_32x32x32_bf16 v[192:207], v[240:243], v[128:135], %[idx]\n\t"
      "s_add_u32 %[w1o], %[w1o], 0x1000\n\t"
      "v_exp_f32 v152, v152\n\t"
      "v_exp_f32 v153, v153\n\t"
      "v_exp_f32 v154, v154\n\t"
      "v_exp_f32 v155, v155\n\t"
      "v_smfmac_f32_32x32x32_bf16 v[192:207], v[244:247], v[136:143], %[idx]\n\t"
      "v_exp_f32 v156, v156\n\t"
      "v_exp_f32 v157, v157\n\t"
      "v_exp_f32 v158, v158\n\t"
      "v_exp_f32 v159, v159\n\t"
      "v_mfma_f32_32x32x16_bf16 v[112:127], v[232:235], v[40:43], 0\n\t"
      "v_exp_f32 v96, v96\n\t"
      "v_exp_f32 v97, v97\n\t"
      "v_exp_f32 v98, v98\n\t"
      "v_exp_f32 v99, v99\n\t"
      "v_mfma_f32_32x32x16_bf16 v[112:127], v[236:239], v[44:47], v[112:127]\n\t"
      "v_exp_f32 v100, v100\n\t"
      "v_exp_f32 v101, v101\n\t"
      "v_exp_f32 v102, v102\n\t"
      "v_exp_f32 v103, v103\n\t"
      "v_smfmac_f32_32x32x32_bf16 v[208:223], v[240:243], v[144:151], %[idx]\n\t"
      "buffer_load_dwordx4 v[240:243], %[l16], %[ws], %[kv] offen offset:2048 sc1\n\t"
      "v_exp_f32 v104, v104\n\t"
      "v_exp_f32 v105, v105\n\t"
      "v_exp_f32 v106, v106\n\t"
      "v_exp_f32 v107, v107\n\t"
      "v_smfmac_f32_32x32x32_bf16 v[208:223], v[244:247], v[152:159], %[idx]\n\t"
      "v_exp_f32 v108, v108\n\t"
      "v_exp_f32 v109, v109\n\t"
      "v_exp_f32 v110, v110\n\t"
      "v_exp_f32 v111, v111\n\t"
      "v_mfma_f32_32x32x16_bf16 v[128:143], v[232:235], v[48:51], 0\n\t"
      "buffer_load_dwordx4 v[244:247], %[l16], %[ws], %[kv] offen offset:3072 sc1\n\t"
      "v_exp_f32 v112, v112\n\t"
      "v_exp_f32 v113, v113\n\t"
      "v_exp_f32 v114, v114\n\t"
      "v_exp_f32 v115, v115\n\t"
      "v_mfma_f32_32x32x16_bf16 v[128:143], v[236:239], v[52:55], v[128:143]\n\t"
      "s_waitcnt vmcnt(18)\n\t"
      "v_exp_f32 v116, v116\n\t"
      "v_exp_f32 v117, v117\n\t"
      "v_exp_f32 v118, v118\n\t"
      "v_exp_f32 v119, v119\n\t"
      "v_smfmac_f32_32x32x32_bf16 v[160:175], v[248:251], v[96:103], %[idx]\n\t"
      "buffer_load_dwordx4 %[l16], %[ws], %[w1o] offen offset:0 sc1 lds\n\t"
      "v_exp_f32 v120, v120\n\t"
      "v_exp_f32 v121, v121\n\t"
      "v_exp_f32 v122, v122\n\t"
      "v_exp_f32 v123, v123\n\t"
      "v_smfmac_f32_32x32x32_bf16 v[160:175], v[252:255], v[104:111], %[idx]\n\t"
      "s_add_u32 %[kv], %[kv], 0x1000\n\t"
      "v_exp_f32 v124, v124\n\t"
      "v_exp_f32 v125, v125\n\t"
      "v_exp_f32 v126, v126\n\t"
      "v_exp_f32 v127, v127\n\t"
      "v_mfma_f32_32x32x16_bf16 v[144:159], v[232:235], v[56:59], 0\n\t"
      "buffer_load_dwordx4 v[232:235], %[l16], %[ws], %[kv] offen offset:0 sc1\n\t"
      "v_exp_f32 v128, v128\n\t"
      "v_exp_f32 v129, v129\n\t"
      "v_exp_f32 v130, v130\n\t"
      "v_exp_f32 v131, v131\n\t"
      "v_mfma_f32_32x32x16_bf16 v[144:159], v[236:239], v[60:63], v[144:159]\n\t"
      "v_exp_f32 v132, v132\n\t"
      "v_exp_f32 v133, v133\n\t"
      "v_exp_f32 v134, v134\n\t"
      "v_exp_f32 v135, v135\n\t"
      "v_smfmac_f32_32x32x32_bf16 v[176:191], v[248:251], v[112:119], %[idx]\n\t"
      "buffer_load_dwordx4 v[236:239], %[l16], %[ws], %[kv] offen offset:1024 sc1\n\t"
      "v_exp_f32 v136, v136\n\t"
      "v_exp_f32 v137, v137\n\t"
      "v_exp_f32 v138, v138\n\t"
      "v_exp_f32 v139, v139\n\t"
      "v_smfmac_f32_32x32x32_bf16 v[176:191], v[252:255], v[120:127], %[idx]\n\t"
      "s_waitcnt vmcnt(18)\n\t"
      "v_exp_f32 v140, v140\n\t"
      "v_exp_f32 v141, v141\n\t"
      "v_exp_f32 v142, v142\n\t"
      "v_exp_f32 v143, v143\n\t"
      "v_mfma_f32_32x32x16_bf16 v[96:111], v[64:67], v[32:35], 0\n\t"
      "buffer_load_dwordx4 %[l16], %[ws], %[w1o] offen offset:1024 sc1 lds\n\t"
      "v_exp_f32 v144, v144\n\t"
      "v_exp_f32 v145, v145\n\t"
      "v_exp_f32 v146, v146\n\t"
      "v_exp_f32 v147, v147\n\t"
      "v_mfma_f32_32x32x16_bf16 v[96:111], v[68:71], v[36:39], v[96:111]\n\t"
      "v_exp_f32 v148, v148\n\t"
      "v_exp_f32 v149, v149\n\t"
      "v_exp_f32 v150, v150\n\t"
      "v_exp_f32 v151, v151\n\t"
      "v_smfmac_f32_32x32x32_bf16 v[192:207], v[248:251], v[128:135], %[idx]\n\t"
      "v_exp_f32 v152, v152\n\t"
      "v_exp_f32 v153, v153\n\t"
      "v_exp_f32 v154, v154\n\t"
      "v_exp_f32 v155, v155\n\t"
      "v_smfmac_f32_32x32x32_bf16 v[192:207], v[252:255], v[136:143], %[idx]\n\t"
      "v_exp_f32 v156, v156\n\t"
      "v_exp_f32 v157, v157\n\t"
      "v_exp_f32 v158, v158\n\t"
      "v_exp_f32 v159, v159\n\t"
      "v_mfma_f32_32x32x16_bf16 v[112:127], v[64:67], v[40:43], 0\n\t"
      "v_exp_f32 v96, v96\n\t"
      "v_exp_f32 v97, v97\n\t"
      "v_exp_f32 v98, v98\n\t"
      "v_exp_f32 v99, v99\n\t"
      "v_mfma_f32_32x32x16_bf16 v[112:127], v[68:71], v[44:47], v[112:127]\n\t"
      "s_sub_u32 %[cnt], %[cnt], 1\n\t"
      "v_exp_f32 v100, v100\n\t"
      "v_exp_f32 v101, v101\n\t"
      "v_exp_f32 v102, v102\n\t"
      "v_exp_f32 v103, v103\n\t"
      "v_smfmac_f32_32x32x32_bf16 v[208:223], v[248:251], v[144:151], %[idx]\n\t"
      "buffer_load_dwordx4 v[248:251], %[l16], %[ws], %[kv] offen offset:2048 sc1\n\t"
      "v_exp_f32 v104, v104\n\t"
      "v_exp_f32 v105, v105\n\t"
      "v_exp_f32 v106, v106\n\t"
      "v_exp_f32 v107, v107\n\t"
      "v_smfmac_f32_32x32x32_bf16 v[208:223], v[252:255], v[152:159], %[idx]\n\t"
      "v_exp_f32 v108, v108\n\t"
      "v_exp_f32 v109, v109\n\t"
      "v_exp_f32 v110, v110\n\t"
      "v_exp_f32 v111, v111\n\t"
      "v_mfma_f32_32x32x16_bf16 v[128:143], v[64:67], v[48:51], 0\n\t"
      "buffer_load_dwordx4 v[252:255], %[l16], %[ws], %[kv] offen offset:3072 sc1\n\t"
      "v_exp_f32 v112, v112\n\t"
      "v_exp_f32 v113, v113\n\t"
      "v_exp_f32 v114, v114\n\t"
      "v_exp_f32 v115, v115\n\t"
      "v_mfma_f32_32x32x16_bf16 v[128:143], v[68:71], v[52:55], v[128:143]\n\t"
      "s_waitcnt vmcnt(18)\n\t"
      "v_exp_f32 v116, v116\n\t"
      "v_exp_f32 v117, v117\n\t"
      "v_exp_f32 v118, v118\n\t"
      "v_exp_f32 v119, v119\n\t"
      "v_smfmac_f32_32x32x32_bf16 v[160:175], v[80:83], v[96:103], %[idx]\n\t"
      "buffer_load_dwordx4 %[l16], %[ws], %[w1o] offen offset:2048 sc1 lds\n\t"
      "v_exp_f32 v120, v120\n\t"
      "v_exp_f32 v121, v121\n\t"
      "v_exp_f32 v122, v122\n\t"
      "v_exp_f32 v123, v123\n\t"
      "v_smfmac_f32_32x32x32_bf16 v[160:175], v[84:87], v[104:111], %[idx]\n\t"
      "s_add_u32 %[kv], %[kv], 0x1000\n\t"
      "v_exp_f32 v124, v124\n\t"
      "v_exp_f32 v125, v125\n\t"
      "v_exp_f32 v126, v126\n\t"
      "v_exp_f32 v127, v127\n\t"
      "v_mfma_f32_32x32x16_bf16 v[144:159], v[64:67], v[56:59], 0\n\t"
      "buffer_load_dwordx4 v[64:67], %[l16], %[ws], %[kv] offen offset:0 sc1\n\t"
      "v_exp_f32 v128, v128\n\t"
      "v_exp_f32 v129, v129\n\t"
      "v_exp_f32 v130, v130\n\t"
      "v_exp_f32 v131, v131\n\t"
      "v_mfma_f32_32x32x16_bf16 v[144:159], v[68:71], v[60:63], v[144:159]\n\t"
      "v_exp_f32 v132, v132\n\t"
      "v_exp_f32 v133, v133\n\t"
      "v_exp_f32 v134, v134\n\t"
      "v_exp_f32 v135, v135\n\t"
      "v_smfmac_f32_32x32x32_bf16 v[176:191], v[80:83], v[112:119], %[idx]\n\t"
      "buffer_load_dwordx4 v[68:71], %[l16], %[ws], %[kv] offen offset:1024 sc1\n\t"
      "v_exp_f32 v136, v136\n\t"
      "v_exp_f32 v137, v137\n\t"
      "v_exp_f32 v138, v138\n\t"
      "v_exp_f32 v139, v139\n\t"
      "v_smfmac_f32_32x32x32_bf16 v[176:191], v[84:87], v[120:127], %[idx]\n\t"
      "s_waitcnt vmcnt(18)\n\t"
      "v_exp_f32 v140, v140\n\t"
      "v_exp_f32 v141, v141\n\t"
      "v_exp_f32 v142, v142\n\t"
      "v_exp_f32 v143, v143\n\t"
      "v_mfma_f32_32x32x16_bf16 v[96:111], v[72:75], v[32:35], 0\n\t"
      "buffer_load_dwordx4 %[l16], %[ws], %[w1o] offen offset:3072 sc1 lds\n\t"
      "v_exp_f32 v144, v144\n\t"
      "v_exp_f32 v145, v145\n\t"
      "v_exp_f32 v146, v146\n\t"
      "v_exp_f32 v147, v147\n\t"
      "v_mfma_f32_32x32x16_bf16 v[96:111], v[76:79], v[36:39], v[96:111]\n\t"
      "s_add_u32 m0, m0, 0x1000\n\t"
      "v_exp_f32 v148, v148\n\t"
      "v_exp_f32 v149, v149\n\t"
      "v_exp_f32 v150, v150\n\t"
      "v_exp_f32 v151, v151\n\t"
      "v_smfmac_f32_32x32x32_bf16 v[192:207], v[80:83], v[128:135], %[idx]\n\t"
      "s_add_u32 %[w1o], %[w1o], 0x1000\n\t"
      "v_exp_f32 v152, v152\n\t"
      "v_exp_f32 v153, v153\n\t"
      "v_exp_f32 v154, v154\n\t"
      "v_exp_f32 v155, v155\n\t"
      "v_smfmac_f32_32x32x32_bf16 v[192:207], v[84:87], v[136:143], %[idx]\n\t"
      "s_cmp_lg_u32 %[cnt], 0\n\t"
      "v_exp_f32 v156, v156\n\t"
      "v_exp_f32 v157, v157\n\t"
      "v_exp_f32 v158, v158\n\t"
      "v_exp_f32 v159, v159\n\t"
      "s_cbranch_scc1 1b\n\t"
      "v_mfma_f32_32x32x16_bf16 v[112:127], v[72:75], v[40:43], 0\n\t"
      "buffer_load_dword v64, %[pfv], %[ws], %[pfo] offen sc1\n\t"
      "v_exp_f32 v96, v96\n\t"
      "v_exp_f32 v97, v97\n\t"
      "v_exp_f32 v98, v98\n\t"
      "v_exp_f32 v99, v99\n\t"
      "v_mfma_f32_32x32x16_bf16 v[112:127], v[76:79], v[44:47], v[112:127]\n\t"
      "v_exp_f32 v100, v100\n\t"
      "v_exp_f32 v101, v101\n\t"
      "v_exp_f32 v102, v102\n\t"
      "v_exp_f32 v103, v103\n\t"
      "v_smfmac_f32_32x32x32_bf16 v[208:223], v[80:83], v[144:151], %[idx]\n\t"
      "v_exp_f32 v104, v104\n\t"
      "v_exp_f32 v105, v105\n\t"
      "v_exp_f32 v106, v106\n\t"
      "v_exp_f32 v107, v107\n\t"
      "v_smfmac_f32_32x32x32_bf16 v[208:223], v[84:87], v[152:159], %[idx]\n\t"
      "v_exp_f32 v108, v108\n\t"
      "v_exp_f32 v109, v109\n\t"
      "v_exp_f32 v110, v110\n\t"
      "v_exp_f32 v111, v111\n\t"
      "v_mfma_f32_32x32x16_bf16 v[128:143], v[72:75], v[48:51], 0\n\t"
      "v_exp_f32 v112, v112\n\t"
      "v_exp_f32 v113, v113\n\t"
      "v_exp_f32 v114, v114\n\t"
      "v_exp_f32 v115, v115\n\t"
      "v_mfma_f32_32x32x16_bf16 v[128:143], v[76:79], v[52:55], v[128:143]\n\t"
      "s_waitcnt vmcnt(17)\n\t"
      "v_exp_f32 v116, v116\n\t"
      "v_exp_f32 v117, v117\n\t"
      "v_exp_f32 v118, v118\n\t"
      "v_exp_f32 v119, v119\n\t"
      "v_smfmac_f32_32x32x32_bf16 v[160:175], v[88:91], v[96:103], %[idx]\n\t"
      "v_exp_f32 v120, v120\n\t"
      "v_exp_f32 v121, v121\n\t"
      "v_exp_f32 v122, v122\n\t"
      "v_exp_f32 v123, v123\n\t"
      "v_smfmac_f32_32x32x32_bf16 v[160:175], v[92:95], v[104:111], %[idx]\n\t"
      "v_exp_f32 v124, v124\n\t"
      "v_exp_f32 v125, v125\n\t"
      "v_exp_f32 v126, v126\n\t"
      "v_exp_f32 v127, v127\n\t"
      "v_mfma_f32_32x32x16_bf16 v[144:159], v[72:75], v[56:59], 0\n\t"
      "v_exp_f32 v128, v128\n\t"
      "v_exp_f32 v129, v129\n\t"
      "v_exp_f32 v130, v130\n\t"
      "v_exp_f32 v131, v131\n\t"
      "v_mfma_f32_32x32x16_bf16 v[144:159], v[76:79], v[60:63], v[144:159]\n\t"
      "v_exp_f32 v132, v132\n\t"
      "v_exp_f32 v133, v133\n\t"
      "v_exp_f32 v134, v134\n\t"
      "v_exp_f32 v135, v135\n\t"
      "v_smfmac_f32_32x32x32_bf16 v[176:191], v[88:91], v[112:119], %[idx]\n\t"
      "v_exp_f32 v136, v136\n\t"
      "v_exp_f32 v137, v137\n\t"
      "v_exp_f32 v138, v138\n\t"
      "v_exp_f32 v139, v139\n\t"
      "v_smfmac_f32_32x32x32_bf16 v[176:191], v[92:95], v[120:127], %[idx]\n\t"
      "s_waitcnt vmcnt(14)\n\t"
      "v_exp_f32 v140, v140\n\t"
      "v_exp_f32 v141, v141\n\t"
      "v_exp_f32 v142, v142\n\t"
      "v_exp_f32 v143, v143\n\t"
      "v_mfma_f32_32x32x16_bf16 v[96:111], v[224:227], v[32:35], 0\n\t"
      "v_exp_f32 v144, v144\n\t"
      "v_exp_f32 v145, v145\n\t"
      "v_exp_f32 v146, v146\n\t"
      "v_exp_f32 v147, v147\n\t"
      "v_mfma_f32_32x32x16_bf16 v[96:111], v[228:231], v[36:39], v[96:111]\n\t"
      "v_exp_f32 v148, v148\n\t"
      "v_exp_f32 v149, v149\n\t"
      "v_exp_f32 v150, v150\n\t"
      "v_exp_f32 v151, v151\n\t"
      "v_smfmac_f32_32x32x32_bf16 v[192:207], v[88:91], v[128:135], %[idx]\n\t"
      "v_exp_f32 v152, v152\n\t"
      "v_exp_f32 v153, v153\n\t"
      "v_exp_f32 v154, v154\n\t"
      "v_exp_f32 v155, v155\n\t"
      "v_smfmac_f32_32x32x32_bf16 v[192:207], v[92:95], v[136:143], %[idx]\n\t"
      "v_exp_f32 v156, v156\n\t"
      "v_exp_f32 v157, v157\n\t"
      "v_exp_f32 v158, v158\n\t"
      "v_exp_f32 v159, v159\n\t"
      "v_mfma_f32_32x32x16_bf16 v[112:127], v[224:227], v[40:43], 0\n\t"
      "v_exp_f32 v96, v96\n\t"
      "v_exp_f32 v97, v97\n\t"
      "v_exp_f32 v98, v98\n\t"
      "v_exp_f32 v99, v99\n\t"
      "v_mfma_f32_32x32x16_bf16 v[112:127], v[228:231], v[44:47], v[112:127]\n\t"
      "v_exp_f32 v100, v100\n\t"
      "v_exp_f32 v101, v101\n\t"
      "v_exp_f32 v102, v102\n\t"
      "v_exp_f32 v103, v103\n\t"
      "v_smfmac_f32_32x32x32_bf16 v[208:223], v[88:91], v[144:151], %[idx]\n\t"
      "v_exp_f32 v104, v104\n\t"
      "v_exp_f32 v105, v105\n\t"
      "v_exp_f32 v106, v106\n\t"
      "v_exp_f32 v107, v107\n\t"
      "v_smfmac_f32_32x32x32_bf16 v[208:223], v[92:95], v[152:159], %[idx]\n\t"
      "v_exp_f32 v108, v108\n\t"
      "v_exp_f32 v109, v109\n\t"
      "v_exp_f32 v110, v110\n\t"
      "v_exp_f32 v111, v111\n\t"
      "v_mfma_f32_32x32x16_bf16 v[128:143], v[224:227], v[48:51], 0\n\t"
      "v_exp_f32 v112, v112\n\t"
      "v_exp_f32 v113, v113\n\t"
      "v_exp_f32 v114, v114\n\t"
      "v_exp_f32 v115, v115\n\t"
      "v_mfma_f32_32x32x16_bf16 v[128:143], v[228:231], v[52:55], v[128:143]\n\t"
      "s_waitcnt vmcnt(11)\n\t"
      "v_exp_f32 v116, v116\n\t"
      "v_exp_f32 v117, v117\n\t"
      "v_exp_f32 v118, v118\n\t"
      "v_exp_f32 v119, v119\n\t"
      "v_smfmac_f32_32x32x32_bf16 v[160:175], v[240:243], v[96:103], %[idx]\n\t"
      "v_exp_f32 v120, v120\n\t"
      "v_exp_f32 v121, v121\n\t"
      "v_exp_f32 v122, v122\n\t"
      "v_exp_f32 v123, v123\n\t"
      "v_smfmac_f32_32x32x32_bf16 v[160:175], v[244:247], v[104:111], %[idx]\n\t"
      "v_exp_f32 v124, v124\n\t"
      "v_exp_f32 v125, v125\n\t"
      "v_exp_f32 v126, v126\n\t"
      "v_exp_f32 v127, v127\n\t"
      "v_mfma_f32_32x32x16_bf16 v[144:159], v[224:227], v[56:59], 0\n\t"
      "v_exp_f32 v128, v128\n\t"
      "v_exp_f32 v129, v129\n\t"
      "v_exp_f32 v130, v130\n\t"
      "v_exp_f32 v131, v131\n\t"
      "v_mfma_f32_32x32x16_bf16 v[144:159], v[228:231], v[60:63], v[144:159]\n\t"
      "v_exp_f32 v132, v132\n\t"
      "v_exp_f32 v133, v133\n\t"
      "v_exp_f32 v134, v134\n\t"
      "v_exp_f32 v135, v135\n\t"
      "v_smfmac_f32_32x32x32_bf16 v[176:191], v[240:243], v[112:119], %[idx]\n\t"
      "v_exp_f32 v136, v136\n\t"
      "v_exp_f32 v137, v137\n\t"
      "v_exp_f32 v138, v138\n\t"
      "v_exp_f32 v139, v139\n\t"
      "v_smfmac_f32_32x32x32_bf16 v[176:191], v[244:247], v[120:127], %[idx]\n\t"
      "s_waitcnt vmcnt(8)\n\t"
      "v_exp_f32 v140, v140\n\t"
      "v_exp_f32 v141, v141\n\t"
      "v_exp_f32 v142, v142\n\t"
      "v_exp_f32 v143, v143\n\t"
      "v_mfma_f32_32x32x16_bf16 v[96:111], v[232:235], v[32:35], 0\n\t"
      "v_exp_f32 v144, v144\n\t"
      "v_exp_f32 v145, v145\n\t"
      "v_exp_f32 v146, v146\n\t"
      "v_exp_f32 v147, v147\n\t"
      "v_mfma_f32_32x32x16_bf16 v[96:111], v[236:239], v[36:39], v[96:111]\n\t"
      "v_exp_f32 v148, v148\n\t"
      "v_exp_f32 v149, v149\n\t"
      "v_exp_f32 v150, v150\n\t"
      "v_exp_f32 v151, v151\n\t"
      "v_smfmac_f32_32x32x32_bf16 v[192:207], v[240:243], v[128:135], %[idx]\n\t"
      "v_exp_f32 v152, v152\n\t"
      "v_exp_f32 v153, v153\n\t"
      "v_exp_f32 v154, v154\n\t"
      "v_exp_f32 v155, v155\n\t"
      "v_smfmac_f32_32x32x32_bf16 v[192:207], v[244:247], v[136:143], %[idx]\n\t"
      "v_exp_f32 v156, v156\n\t"
      "v_exp_f32 v157, v157\n\t"
      "v_exp_f32 v158, v158\n\t"
      "v_exp_f32 v159, v159\n\t"
      "v_mfma_f32_32x32x16_bf16 v[112:127], v[232:235], v[40:43], 0\n\t"
      "v_exp_f32 v96, v96\n\t"
      "v_exp_f32 v97, v97\n\t"
      "v_exp_f32 v98, v98\n\t"
      "v_exp_f32 v99, v99\n\t"
      "v_mfma_f32_32x32x16_bf16 v[112:127], v[236:239], v[44:47], v[112:127]\n\t"
      "v_exp_f32 v100, v100\n\t"
      "v_exp_f32 v101, v101\n\t"
      "v_exp_f32 v102, v102\n\t"
      "v_exp_f32 v103, v103\n\t"
      "v_smfmac_f32_32x32x32_bf16 v[208:223], v[240:243], v[144:151], %[idx]\n\t"
      "v_exp_f32 v104, v104\n\t"
      "v_exp_f32 v105, v105\n\t"
      "v_exp_f32 v106, v106\n\t"
      "v_exp_f32 v107, v107\n\t"
      "v_smfmac_f32_32x32x32_bf16 v[208:223], v[244:247], v[152:159], %[idx]\n\t"
      "v_exp_f32 v108, v108\n\t"
      "v_exp_f32 v109, v109\n\t"
      "v_exp_f32 v110, v110\n\t"
      "v_exp_f32 v111, v111\n\t"
      "v_mfma_f32_32x32x16_bf16 v[128:143], v[232:235], v[48:51], 0\n\t"
      "v_exp_f32 v112, v112\n\t"
      "v_exp_f32 v113, v113\n\t"
      "v_exp_f32 v114, v114\n\t"
      "v_exp_f32 v115, v115\n\t"
      "v_mfma_f32_32x32x16_bf16 v[128:143], v[236:239], v[52:55], v[128:143]\n\t"
      "s_waitcnt vmcnt(5)\n\t"
      "v_exp_f32 v116, v116\n\t"
      "v_exp_f32 v117, v117\n\t"
      "v_exp_f32 v118, v118\n\t"
      "v_exp_f32 v119, v119\n\t"
      "v_smfmac_f32_32x32x32_bf16 v[160:175], v[248:251], v[96:103], %[idx]\n\t"
      "v_exp_f32 v120, v120\n\t"
      "v_exp_f32 v121, v121\n\t"
      "v_exp_f32 v122, v122\n\t"
      "v_exp_f32 v123, v123\n\t"
      "v_smfmac_f32_32x32x32_bf16 v[160:175], v[252:255], v[104:111], %[idx]\n\t"
      "v_exp_f32 v124, v124\n\t"
      "v_exp_f32 v125, v125\n\t"
      "v_exp_f32 v126, v126\n\t"
      "v_exp_f32 v127, v127\n\t"
      "v_mfma_f32_32x32x16_bf16 v[144:159], v[232:235], v[56:59], 0\n\t"
      "v_exp_f32 v128, v128\n\t"
      "v_exp_f32 v129, v129\n\t"
      "v_exp_f32 v130, v130\n\t"
      "v_exp_f32 v131, v131\n\t"
      "v_mfma_f32_32x32x16_bf16 v[144:159], v[236:239], v[60:63], v[144:159]\n\t"
      "s_waitcnt vmcnt(0)\n\t"
      "v_exp_f32 v132, v132\n\t"
      "v_exp_f32 v133, v133\n\t"
      "v_exp_f32 v134, v134\n\t"
      "v_exp_f32 v135, v135\n\t"
      "v_smfmac_f32_32x32x32_bf16 v[176:191], v[248:251], v[112:119], %[idx]\n\t"
      "ds_write_b128 %[wr0], v[160:163] offset:0\n\t"
      "v_exp_f32 v136, v136\n\t"
      "v_exp_f32 v137, v137\n\t"
      "v_exp_f32 v138, v138\n\t"
      "v_exp_f32 v139, v139\n\t"
      "v_smfmac_f32_32x32x32_bf16 v[176:191], v[252:255], v[120:127], %[idx]\n\t"
      "ds_write_b128 %[wr0], v[164:167] offset:1024\n\t"
      "v_exp_f32 v140, v140\n\t"
      "v_exp_f32 v141, v141\n\t"
      "v_exp_f32 v142, v142\n\t"
      "v_exp_f32 v143, v143\n\t"
      "ds_write_b128 %[wr0], v[168:171] offset:2048\n\t"
      "ds_write_b128 %[wr0], v[172:175] offset:3072\n\t"
      "v_exp_f32 v144, v144\n\t"
      "v_exp_f32 v145, v145\n\t"
      "v_exp_f32 v146, v146\n\t"
      "v_exp_f32 v147, v147\n\t"
      "ds_write_b128 %[wr1], v[176:179] offset:0\n\t"
      "ds_write_b128 %[wr1], v[180:183] offset:1024\n\t"
      "v_exp_f32 v148, v148\n\t"
      "v_exp_f32 v149, v149\n\t"
      "v_exp_f32 v150, v150\n\t"
      "v_exp_f32 v151, v151\n\t"
      "v_smfmac_f32_32x32x32_bf16 v[192:207], v[248:251], v[128:135], %[idx]\n\t"
      "ds_write_b128 %[wr1], v[184:187] offset:2048\n\t"
      "v_exp_f32 v152, v152\n\t"
      "v_exp_f32 v153, v153\n\t"
      "v_exp_f32 v154, v154\n\t"
      "v_exp_f32 v155, v155\n\t"
      "v_smfmac_f32_32x32x32_bf16 v[192:207], v[252:255], v[136:143], %[idx]\n\t"
      "ds_write_b128 %[wr1], v[188:191] offset:3072\n\t"
      "v_exp_f32 v156, v156\n\t"
      "v_exp_f32 v157, v157\n\t"
      "v_exp_f32 v158, v158\n\t"
      "v_exp_f32 v159, v159\n\t"
      "s_branch 3b\n\t"
      "4:\n\t"
      : "+{v[32:63]}"(Qr), "+{v[64:71]}"(K0), "+{v[80:87]}"(V0), "+{v[160:191]}"(zo0), "+{v[192:223]}"(zo1), [kv] "+s"(kvo), [w1o] "+s"(w1o), [cnt] "+s"(cnt), [w2o] "+s"(w2o), "+{v[224:255]}"(warm)
      : [idx] "v"(idx), [mb] "v"(mb), [l16] "v"(c.lane16), [ws] "s"(c.ws), [m0b] "s"(m0b), [oo] "s"(oo), [pfv] "v"(pfv), [pfo] "s"(pfo), [tl] "s"(tl), [th] "s"(th), [z0] "v"(z0), [co] "s"(co), [w2p] "s"(w2p), [wr0] "v"(wr0), [wr1] "v"(wr1), [wr2] "v"(wr2), [rd] "v"(rd)
      : "v72", "v73", "v74", "v75", "v76", "v77", "v78", "v79", "v88", "v89", "v90", "v91", "v92", "v93", "v94", "v95", "v96", "v97", "v98", "v99", "v100", "v101", "v102", "v103", "v104", "v105", "v106", "v107", "v108", "v109", "v110", "v111", "v112", "v113", "v114", "v115", "v116", "v117", "v118", "v119", "v120", "v121", "v122", "v123", "v124", "v125", "v126", "v127", "v128", "v129", "v130", "v131", "v132", "v133", "v134", "v135", "v136", "v137", "v138", "v139", "v140", "v141", "v142", "v143", "v144", "v145", "v146", "v147", "v148", "v149", "v150", "v151", "v152", "v153", "v154", "v155", "v156", "v157", "v158", "v159", "m0", "memory", "scc", ACC_FILE);
    }
    bar();
    refresh(c);
    if (act) {
      {
        if (c.w >= 2) {
#pragma unroll
          for (int i = 0; i < 4; ++i) dma4(c.lds0 + L_WO + ((c.w - 2) * 16 + 4 * i) * 1024, c.lane16, c.ws, WS_WOF + (c.c * 32 + (c.w - 2) * 16 + 4 * i) * 1024);
        }
        asm volatile("" : "={v[224:255]}"(warm));
        if (layer == 0) {
          asm volatile("buffer_load_dwordx4 a[224:227], %0, %1, %2 offen sc1\n\tbuffer_load_dwordx4 a[228:231], %0, %1, %2 offen offset:1024 sc1\n\tbuffer_load_dwordx4 a[232:235], %0, %1, %2 offen offset:2048 sc1"
                       :: "v"(c.lane16), "s"(c.ws), "s"(U(WS_WEQ + c.c * 3072)) : "memory");
          const unsigned b0 = c.lds0 + L_W1 + c.w * TILEB + c.lane16, bv = c.l31 * 2, bo = U((c.c * 128 + c.w * 32) * 2);
          const unsigned cv = line_of(12, c.half ? c.r : c.lane & 15) + (c.half ? c.lane & 15 : c.r) * FS;
          const rsrc_t rb = __builtin_amdgcn_make_buffer_rsrc((void*)p.b1, 0, DFF * 2, 0x00020000);
          unsigned bb;
          f32x16 a; u32x2 cf;
    asm volatile(
      "buffer_load_dwordx2 v[72:73], %[cv], %[ws], 0 offen sc1\n\t"
      "buffer_load_ushort v74, %[bv], %[rb], %[bo] offen\n\t"
      "v_mov_b32 v116, 0x3f803f80\n\t"
      "v_mov_b32 v117, 0x3f803f80\n\t"
      "v_mov_b32 v118, 0x3f803f80\n\t"
      "v_mov_b32 v119, 0x3f803f80\n\t"
      "ds_read_b128 v[120:123], %[b0] offset:0\n\t"
      "ds_read_b128 v[124:127], %[b0] offset:1024\n\t"
      "ds_read_b128 v[128:131], %[b0] offset:2048\n\t"
      "ds_read_b128 v[132:135], %[b0] offset:3072\n\t"
      "ds_read_b128 v[136:139], %[b0] offset:4096\n\t"
      "ds_read_b128 v[140:143], %[b0] offset:5120\n\t"
      "s_waitcnt lgkmcnt(5)\n\t"
      "v_mfma_f32_32x32x16_bf16 v[56:71], v[116:119], v[120:123], 0\n\t"
      "ds_read_b128 v[120:123], %[b0] offset:6144\n\t"
      "s_waitcnt lgkmcnt(5)\n\t"
      "v_mfma_f32_32x32x16_bf16 v[56:71], v[116:119], v[124:127], v[56:71]\n\t"
      "ds_read_b128 v[124:127], %[b0] offset:7168\n\t"
      "s_waitcnt lgkmcnt(5)\n\t"
      "v_mfma_f32_32x32x16_bf16 v[56:71], v[116:119], v[128:131], v[56:71]\n\t"
      "ds_read_b128 v[128:131], %[b0] offset:8192\n\t"
      "s_waitcnt lgkmcnt(5)\n\t"
      "v_mfma_f32_32x32x16_bf16 v[56:71], v[116:119], v[132:135], v[56:71]\n\t"
      "ds_read_b128 v[132:135], %[b0] offset:9216\n\t"
      "s_waitcnt lgkmcnt(5)\n\t"
      "v_mfma_f32_32x32x16_bf16 v[56:71], v[116:119], v[136:139], v[56:71]\n\t"
      "ds_read_b128 v[136:139], %[b0] offset:10240\n\t"
      "s_waitcnt lgkmcnt(5)\n\t"
      "v_mfma_f32_32x32x16_bf16 v[56:71], v[116:119], v[140:143], v[56:71]\n\t"
      "ds_read_b128 v[140:143], %[b0] offset:11264\n\t"
      "s_waitcnt lgkmcnt(5)\n\t"
      "v_mfma_f32_32x32x16_bf16 v[56:71], v[116:119], v[120:123], v[56:71]\n\t"
      "ds_read_b128 v[120:123], %[b0] offset:12288\n\t"
      "s_waitcnt lgkmcnt(5)\n\t"
      "v_mfma_f32_32x32x16_bf16 v[56:71], v[116:119], v[124:127], v[56:71]\n\t"
      "ds_read_b128 v[124:127], %[b0] offset:13312\n\t"
      "s_waitcnt lgkmcnt(5)\n\t"
      "v_mfma_f32_32x32x16_bf16 v[56:71], v[116:119], v[128:131], v[56:71]\n\t"
      "ds_read_b128 v[128:131], %[b0] offset:14336\n\t"
      "s_waitcnt lgkmcnt(5)\n\t"
      "v_mfma_f32_32x32x16_bf16 v[56:71], v[116:119], v[132:135], v[56:71]\n\t"
      "ds_read_b128 v[132:135], %[b0] offset:15360\n\t"
      "s_waitcnt lgkmcnt(5)\n\t"
      "v_mfma_f32_32x32x16_bf16 v[56:71], v[116:119], v[136:139], v[56:71]\n\t"
      "ds_read_b128 v[136:139], %[b0] offset:16384\n\t"
      "s_waitcnt lgkmcnt(5)\n\t"
      "v_mfma_f32_32x32x16_bf16 v[56:71], v[116:119], v[140:143], v[56:71]\n\t"
      "ds_read_b128 v[140:143], %[b0] offset:17408\n\t"
      "s_waitcnt lgkmcnt(5)\n\t"
      "v_mfma_f32_32x32x16_bf16 v[56:71], v[116:119], v[120:123], v[56:71]\n\t"
      "ds_read_b128 v[120:123], %[b0] offset:18432\n\t"
      "s_waitcnt lgkmcnt(5)\n\t"
      "v_mfma_f32_32x32x16_bf16 v[56:71], v[116:119], v[124:127], v[56:71]\n\t"
      "ds_read_b128 v[124:127], %[b0] offset:19456\n\t"
      "s_waitcnt lgkmcnt(5)\n\t"
      "v_mfma_f32_32x32x16_bf16 v[56:71], v[116:119], v[128:131], v[56:71]\n\t"
      "ds_read_b128 v[128:131], %[b0] offset:20480\n\t"
      "s_waitcnt lgkmcnt(5)\n\t"
      "v_mfma_f32_32x32x16_bf16 v[56:71], v[116:119], v[132:135], v[56:71]\n\t"
      "ds_read_b128 v[132:135], %[b0] offset:21504\n\t"
      "s_waitcnt lgkmcnt(5)\n\t"
      "v_mfma_f32_32x32x16_bf16 v[56:71], v[116:119], v[136:139], v[56:71]\n\t"
      "ds_read_b128 v[136:139], %[b0] offset:22528\n\t"
      "s_waitcnt lgkmcnt(5)\n\t"
      "v_mfma_f32_32x32x16_bf16 v[56:71], v[116:119], v[140:143], v[56:71]\n\t"
      "ds_read_b128 v[140:143], %[b0] offset:23552\n\t"
      "s_waitcnt lgkmcnt(5)\n\t"
      "v_mfma_f32_32x32x16_bf16 v[56:71], v[116:119], v[120:123], v[56:71]\n\t"
      "ds_read_b128 v[120:123], %[b0] offset:24576\n\t"
      "s_waitcnt lgkmcnt(5)\n\t"
      "v_mfma_f32_32x32x16_bf16 v[56:71], v[116:119], v[124:127], v[56:71]\n\t"
      "ds_read_b128 v[124:127], %[b0] offset:25600\n\t"
      "s_waitcnt lgkmcnt(5)\n\t"
      "v_mfma_f32_32x32x16_bf16 v[56:71], v[116:119], v[128:131], v[56:71]\n\t"
      "ds_read_b128 v[128:131], %[b0] offset:26624\n\t"
      "s_waitcnt lgkmcnt(5)\n\t"
      "v_mfma_f32_32x32x16_bf16 v[56:71], v[116:119], v[132:135], v[56:71]\n\t"
      "ds_read_b128 v[132:135], %[b0] offset:27648\n\t"
      "s_waitcnt lgkmcnt(5)\n\t"
      "v_mfma_f32_32x32x16_bf16 v[56:71], v[116:119], v[136:139], v[56:71]\n\t"
      "s_waitcnt lgkmcnt(4)\n\t"
      "v_mfma_f32_32x32x16_bf16 v[56:71], v[116:119], v[140:143], v[56:71]\n\t"
      "s_waitcnt lgkmcnt(3)\n\t"
      "v_mfma_f32_32x32x16_bf16 v[56:71], v[116:119], v[120:123], v[56:71]\n\t"
      "s_waitcnt lgkmcnt(2)\n\t"
      "v_mfma_f32_32x32x16_bf16 v[56:71], v[116:119], v[124:127], v[56:71]\n\t"
      "s_waitcnt lgkmcnt(1)\n\t"
      "v_mfma_f32_32x32x16_bf16 v[56:71], v[116:119], v[128:131], v[56:71]\n\t"
      "s_waitcnt lgkmcnt(0)\n\t"
      "v_mfma_f32_32x32x16_bf16 v[56:71], v[116:119], v[132:135], v[56:71]\n\t"
      "s_waitcnt vmcnt(0)\n\t"
      "s_nop 11\n\t"
      : "=&{v[56:71]}"(a), "=&{v[72:73]}"(cf), "=&{v74}"(bb)
      : [b0] "v"(b0), [ws] "s"(c.ws), [cv] "v"(cv), [bv] "v"(bv), [rb] "s"(rb), [bo] "s"(bo)
      : "v116", "v117", "v118", "v119", "v120", "v121", "v122", "v123", "v124", "v125", "v126", "v127", "v128", "v129", "v130", "v131", "v132", "v133", "v134", "v135", "v136", "v137", "v138", "v139", "v140", "v141", "v142", "v143", "scc", "memory", ACC_FILE);
          write_wext(c, L_EX1 + c.w * 1024, a[0], bb);
          const u64 tg = tag_of(c, 12);
          if (!__all(cf[1] == (unsigned)(tg >> 32) && (cf[0] >> 16) == ((unsigned)tg >> 16))) poll<0>(c, warm, cv, c.lane < 64, tg);
        }
        const unsigned ro = U(WS_L0 + (layer ? 0u : LSTRIDE) + O_XF + ((c.r * 4 + c.w) * 28 + 2 * c.c) * 1024);
        unsigned so, n;
        const unsigned so0 = U(lb + O_OF + (c.r * 4 + c.w) * 32 * 1024), ps = so0, pv = (c.lane & 15) * 2048 + 2032;
        const unsigned b0 = c.lds0 + L_WO + c.lane16, xo = U(lb + O_X2F + ((c.r * 4 + c.w) * 28 + 2 * c.c) * 1024);
        const unsigned stw = c.half ? OOB : c.l31 * 8, sto = U(lb + O_ST2 + ((c.r * 14 + c.c) * 128 + c.w * 32) * 8);
        asm volatile("" :: "s"(so0), "v"(pv), "v"(b0), "s"(xo), "v"(stw), "s"(sto), "v"(c.lane16));
        unsigned f;
    asm volatile(
      "s_mov_b32 %[f], 1\n\t"
      "buffer_load_dwordx4 v[64:67], %[l16], %[ws], %[ro] offen sc1\n\t"
      "buffer_load_dwordx4 v[68:71], %[l16], %[ws], %[ro] offen offset:1024 sc1\n\t"
      "s_mov_b32 %[n], 0x40000\n\t"
      "s_cmp_eq_u32 %[lay], 0\n\t"
      "s_cbranch_scc1 8f\n\t"
      "s_sleep 16\n\t"
      "8:\n\t"
      "s_mov_b32 %[so], %[so0]\n\t"
      "buffer_load_dword v55, %[pv], %[ws], %[ps] offen sc1\n\t"
      "v_mov_b32 v54, 0\n\t"
      "s_sub_u32 %[n], %[n], 1\n\t"
      "s_waitcnt vmcnt(0)\n\t"
      "v_mov_b32 v54, v55\n\t"
      "v_cmp_eq_u32 vcc, -1, v54\n\t"
      "s_cbranch_vccz 7f\n\t"
      "s_cmp_gt_i32 %[n], 0\n\t"
      "s_cbranch_scc1 8b\n\t"
      "7:\n\t"
      "buffer_load_dwordx4 v[128:131], %[l16], %[ws], %[so] offen offset:0 sc1\n\t"
      "buffer_load_dwordx4 v[132:135], %[l16], %[ws], %[so] offen offset:1024 sc1\n\t"
      "buffer_load_dwordx4 v[136:139], %[l16], %[ws], %[so] offen offset:2048 sc1\n\t"
      "buffer_load_dwordx4 v[140:143], %[l16], %[ws], %[so] offen offset:3072 sc1\n\t"
      "s_add_u32 %[so], %[so], 0x1000\n\t"
      "buffer_load_dwordx4 v[144:147], %[l16], %[ws], %[so] offen offset:0 sc1\n\t"
      "buffer_load_dwordx4 v[148:151], %[l16], %[ws], %[so] offen offset:1024 sc1\n\t"
      "buffer_load_dwordx4 v[152:155], %[l16], %[ws], %[so] offen offset:2048 sc1\n\t"
      "buffer_load_dwordx4 v[156:159], %[l16], %[ws], %[so] offen offset:3072 sc1\n\t"
      "s_add_u32 %[so], %[so], 0x1000\n\t"
      "buffer_load_dwordx4 v[160:163], %[l16], %[ws], %[so] offen offset:0 sc1\n\t"
      "buffer_load_dwordx4 v[164:167], %[l16], %[ws], %[so] offen offset:1024 sc1\n\t"
      "buffer_load_dwordx4 v[168:171], %[l16], %[ws], %[so] offen offset:2048 sc1\n\t"
      "buffer_load_dwordx4 v[172:175], %[l16], %[ws], %[so] offen offset:3072 sc1\n\t"
      "s_add_u32 %[so], %[so], 0x1000\n\t"
      "v_lshlrev_b32 v72, 16, v64\n\t"
      "v_and_b32 v73, 0xffff0000, v64\n\t"
      "v_lshlrev_b32 v74, 16, v65\n\t"
      "v_and_b32 v75, 0xffff0000, v65\n\t"
      "v_lshlrev_b32 v76, 16, v66\n\t"
      "v_and_b32 v77, 0xffff0000, v66\n\t"
      "v_lshlrev_b32 v78, 16, v67\n\t"
      "v_and_b32 v79, 0xffff0000, v67\n\t"
      "v_lshlrev_b32 v80, 16, v68\n\t"
      "v_and_b32 v81, 0xffff0000, v68\n\t"
      "v_lshlrev_b32 v82, 16, v69\n\t"
      "v_and_b32 v83, 0xffff0000, v69\n\t"
      "v_lshlrev_b32 v84, 16, v70\n\t"
      "v_and_b32 v85, 0xffff0000, v70\n\t"
      "v_lshlrev_b32 v86, 16, v71\n\t"
      "v_and_b32 v87, 0xffff0000, v71\n\t"
      "s_cmp_eq_u32 %[f], 0\n\t"
      "s_cbranch_scc1 5f\n\t"
      "s_mov_b32 %[f], 0\n\t"
      "s_waitcnt lgkmcnt(0)\n\t"
      "s_barrier\n\t"
      "5:\n\t"
      "ds_read_b128 v[104:107], %[b0] offset:0\n\t"
      "ds_read_b128 v[108:111], %[b0] offset:1024\n\t"
      "ds_read_b128 v[112:115], %[b0] offset:2048\n\t"
      "ds_read_b128 v[116:119], %[b0] offset:3072\n\t"
      "ds_read_b128 v[120:123], %[b0] offset:4096\n\t"
      "ds_read_b128 v[124:127], %[b0] offset:5120\n\t"
      "s_waitcnt vmcnt(11)\n\t"
      "s_waitcnt lgkmcnt(5)\n\t"
      "v_mfma_f32_32x32x16_bf16 v[88:103], v[104:107], v[128:131], v[72:87]\n\t"
      "buffer_load_dwordx4 v[176:179], %[l16], %[ws], %[so] offen offset:0 sc1\n\t"
      "ds_read_b128 v[104:107], %[b0] offset:6144\n\t"
      "v_max3_u32 v54, v128, v129, v54\n\t"
      "v_max3_u32 v54, v130, v131, v54\n\t"
      "s_waitcnt vmcnt(11)\n\t"
      "s_waitcnt lgkmcnt(5)\n\t"
      "v_mfma_f32_32x32x16_bf16 v[88:103], v[108:111], v[132:135], v[88:103]\n\t"
      "buffer_load_dwordx4 v[180:183], %[l16], %[ws], %[so] offen offset:1024 sc1\n\t"
      "ds_read_b128 v[108:111], %[b0] offset:7168\n\t"
      "v_max3_u32 v54, v132, v133, v54\n\t"
      "v_max3_u32 v54, v134, v135, v54\n\t"
      "s_waitcnt vmcnt(11)\n\t"
      "s_waitcnt lgkmcnt(5)\n\t"
      "v_mfma_f32_32x32x16_bf16 v[88:103], v[112:115], v[136:139], v[88:103]\n\t"
      "buffer_load_dwordx4 v[184:187], %[l16], %[ws], %[so] offen offset:2048 sc1\n\t"
      "ds_read_b128 v[112:115], %[b0] offset:8192\n\t"
      "v_max3_u32 v54, v136, v137, v54\n\t"
      "v_max3_u32 v54, v138, v139, v54\n\t"
      "s_waitcnt vmcnt(11)\n\t"
      "s_waitcnt lgkmcnt(5)\n\t"
      "v_mfma_f32_32x32x16_bf16 v[88:103], v[116:119], v[140:143], v[88:103]\n\t"
      "buffer_load_dwordx4 v[188:191], %[l16], %[ws], %[so] offen offset:3072 sc1\n\t"
      "s_add_u32 %[so], %[so], 0x1000\n\t"
      "ds_read_b128 v[116:119], %[b0] offset:9216\n\t"
      "v_max3_u32 v54, v140, v141, v54\n\t"
      "v_max3_u32 v54, v142, v143, v54\n\t"
      "s_waitcnt vmcnt(11)\n\t"
      "s_waitcnt lgkmcnt(5)\n\t"
      "v_mfma_f32_32x32x16_bf16 v[88:103], v[120:123], v[144:147], v[88:103]\n\t"
      "buffer_load_dwordx4 v[192:195], %[l16], %[ws], %[so] offen offset:0 sc1\n\t"
      "ds_read_b128 v[120:123], %[b0] offset:10240\n\t"
      "v_max3_u32 v54, v144, v145, v54\n\t"
      "v_max3_u32 v54, v146, v147, v54\n\t"
      "s_waitcnt vmcnt(11)\n\t"
      "s_waitcnt lgkmcnt(5)\n\t"
      "v_mfma_f32_32x32x16_bf16 v[88:103], v[124:127], v[148:151], v[88:103]\n\t"
      "buffer_load_dwordx4 v[196:199], %[l16], %[ws], %[so] offen offset:1024 sc1\n\t"
      "ds_read_b128 v[124:127], %[b0] offset:11264\n\t"
      "v_max3_u32 v54, v148, v149, v54\n\t"
      "v_max3_u32 v54, v150, v151, v54\n\t"
      "s_waitcnt vmcnt(11)\n\t"
      "s_waitcnt lgkmcnt(5)\n\t"
      "v_mfma_f32_32x32x16_bf16 v[88:103], v[104:107], v[152:155], v[88:103]\n\t"
      "buffer_load_dwordx4 v[200:203], %[l16], %[ws], %[so] offen offset:2048 sc1\n\t"
      "ds_read_b128 v[104:107], %[b0] offset:12288\n\t"
      "v_max3_u32 v54, v152, v153, v54\n\t"
      "v_max3_u32 v54, v154, v155, v54\n\t"
      "s_waitcnt vmcnt(11)\n\t"
      "s_waitcnt lgkmcnt(5)\n\t"
      "v_mfma_f32_32x32x16_bf16 v[88:103], v[108:111], v[156:159], v[88:103]\n\t"
      "buffer_load_dwordx4 v[204:207], %[l16], %[ws], %[so] offen offset:3072 sc1\n\t"
      "s_add_u32 %[so], %[so], 0x1000\n\t"
      "ds_read_b128 v[108:111], %[b0] offset:13312\n\t"
      "v_max3_u32 v54, v156, v157, v54\n\t"
      "v_max3_u32 v54, v158, v159, v54\n\t"
      "s_waitcnt vmcnt(11)\n\t"
      "s_waitcnt lgkmcnt(5)\n\t"
      "v_mfma_f32_32x32x16_bf16 v[88:103], v[112:115], v[160:163], v[88:103]\n\t"
      "buffer_load_dwordx4 v[208:211], %[l16], %[ws], %[so] offen offset:0 sc1\n\t"
      "ds_read_b128 v[112:115], %[b0] offset:14336\n\t"
      "v_max3_u32 v54, v160, v161, v54\n\t"
      "v_max3_u32 v54, v162, v163, v54\n\t"
      "s_waitcnt vmcnt(11)\n\t"
      "s_waitcnt lgkmcnt(5)\n\t"
      "v_mfma_f32_32x32x16_bf16 v[88:103], v[116:119], v[164:167], v[88:103]\n\t"
      "buffer_load_dwordx4 v[212:215], %[l16], %[ws], %[so] offen offset:1024 sc1\n\t"
      "ds_read_b128 v[116:119], %[b0] offset:15360\n\t"
      "v_max3_u32 v54, v164, v165, v54\n\t"
      "v_max3_u32 v54, v166, v167, v54\n\t"
      "s_waitcnt vmcnt(11)\n\t"
      "s_waitcnt lgkmcnt(5)\n\t"
      "v_mfma_f32_32x32x16_bf16 v[88:103], v[120:123], v[168:171], v[88:103]\n\t"
      "buffer_load_dwordx4 v[216:219], %[l16], %[ws], %[so] offen offset:2048 sc1\n\t"
      "ds_read_b128 v[120:123], %[b0] offset:16384\n\t"
      "v_max3_u32 v54, v168, v169, v54\n\t"
      "v_max3_u32 v54, v170, v171, v54\n\t"
      "s_waitcnt vmcnt(11)\n\t"
      "s_waitcnt lgkmcnt(5)\n\t"
      "v_mfma_f32_32x32x16_bf16 v[88:103], v[124:127], v[172:175], v[88:103]\n\t"
      "buffer_load_dwordx4 v[220:223], %[l16], %[ws], %[so] offen offset:3072 sc1\n\t"
      "s_add_u32 %[so], %[so], 0x1000\n\t"
      "ds_read_b128 v[124:127], %[b0] offset:17408\n\t"
      "v_max3_u32 v54, v172, v173, v54\n\t"
      "v_max3_u32 v54, v174, v175, v54\n\t"
      "s_waitcnt vmcnt(11)\n\t"
      "s_waitcnt lgkmcnt(5)\n\t"
      "v_mfma_f32_32x32x16_bf16 v[88:103], v[104:107], v[176:179], v[88:103]\n\t"
      "buffer_load_dwordx4 v[224:227], %[l16], %[ws], %[so] offen offset:0 sc1\n\t"
      "ds_read_b128 v[104:107], %[b0] offset:18432\n\t"
      "v_max3_u32 v54, v176, v177, v54\n\t"
      "v_max3_u32 v54, v178, v179, v54\n\t"
      "s_waitcnt vmcnt(11)\n\t"
      "s_waitcnt lgkmcnt(5)\n\t"
      "v_mfma_f32_32x32x16_bf16 v[88:103], v[108:111], v[180:183], v[88:103]\n\t"
      "buffer_load_dwordx4 v[228:231], %[l16], %[ws], %[so] offen offset:1024 sc1\n\t"
      "ds_read_b128 v[108:111], %[b0] offset:19456\n\t"
      "v_max3_u32 v54, v180, v181, v54\n\t"
      "v_max3_u32 v54, v182, v183, v54\n\t"
      "s_waitcnt vmcnt(11)\n\t"
      "s_waitcnt lgkmcnt(5)\n\t"
      "v_mfma_f32_32x32x16_bf16 v[88:103], v[112:115], v[184:187], v[88:103]\n\t"
      "buffer_load_dwordx4 v[232:235], %[l16], %[ws], %[so] offen offset:2048 sc1\n\t"
      "ds_read_b128 v[112:115], %[b0] offset:20480\n\t"
      "v_max3_u32 v54, v184, v185, v54\n\t"
      "v_max3_u32 v54, v186, v187, v54\n\t"
      "s_waitcnt vmcnt(11)\n\t"
      "s_waitcnt lgkmcnt(5)\n\t"
      "v_mfma_f32_32x32x16_bf16 v[88:103], v[116:119], v[188:191], v[88:103]\n\t"
      "buffer_load_dwordx4 v[236:239], %[l16], %[ws], %[so] offen offset:3072 sc1\n\t"
      "s_add_u32 %[so], %[so], 0x1000\n\t"
      "ds_read_b128 v[116:119], %[b0] offset:21504\n\t"
      "v_max3_u32 v54, v188, v189, v54\n\t"
      "v_max3_u32 v54, v190, v191, v54\n\t"
      "s_waitcnt vmcnt(11)\n\t"
      "s_waitcnt lgkmcnt(5)\n\t"
      "v_mfma_f32_32x32x16_bf16 v[88:103], v[120:123], v[192:195], v[88:103]\n\t"
      "buffer_load_dwordx4 v[240:243], %[l16], %[ws], %[so] offen offset:0 sc1\n\t"
      "ds_read_b128 v[120:123], %[b0] offset:22528\n\t"
      "v_max3_u32 v54, v192, v193, v54\n\t"
      "v_max3_u32 v54, v194, v195, v54\n\t"
      "s_waitcnt vmcnt(11)\n\t"
      "s_waitcnt lgkmcnt(5)\n\t"
      "v_mfma_f32_32x32x16_bf16 v[88:103], v[124:127], v[196:199], v[88:103]\n\t"
      "buffer_load_dwordx4 v[244:247], %[l16], %[ws], %[so] offen offset:1024 sc1\n\t"
      "ds_read_b128 v[124:127], %[b0] offset:23552\n\t"
      "v_max3_u32 v54, v196, v197, v54\n\t"
      "v_max3_u32 v54, v198, v199, v54\n\t"
      "s_waitcnt vmcnt(11)\n\t"
      "s_waitcnt lgkmcnt(5)\n\t"
      "v_mfma_f32_32x32x16_bf16 v[88:103], v[104:107], v[200:203], v[88:103]\n\t"
      "buffer_load_dwordx4 v[248:251], %[l16], %[ws], %[so] offen offset:2048 sc1\n\t"
      "ds_read_b128 v[104:107], %[b0] offset:24576\n\t"
      "v_max3_u32 v54, v200, v201, v54\n\t"
      "v_max3_u32 v54, v202, v203, v54\n\t"
      "s_waitcnt vmcnt(11)\n\t"
      "s_waitcnt lgkmcnt(5)\n\t"
      "v_mfma_f32_32x32x16_bf16 v[88:103], v[108:111], v[204:207], v[88:103]\n\t"
      "buffer_load_dwordx4 v[252:255], %[l16], %[ws], %[so] offen offset:3072 sc1\n\t"
      "s_add_u32 %[so], %[so], 0x1000\n\t"
      "ds_read_b128 v[108:111], %[b0] offset:25600\n\t"
      "v_max3_u32 v54, v204, v205, v54\n\t"
      "v_max3_u32 v54, v206, v207, v54\n\t"
      "s_waitcnt vmcnt(11)\n\t"
      "s_waitcnt lgkmcnt(5)\n\t"
      "v_mfma_f32_32x32x16_bf16 v[88:103], v[112:115], v[208:211], v[88:103]\n\t"
      "ds_read_b128 v[112:115], %[b0] offset:26624\n\t"
      "v_max3_u32 v54, v208, v209, v54\n\t"
      "v_max3_u32 v54, v210, v211, v54\n\t"
      "s_waitcnt vmcnt(10)\n\t"
      "s_waitcnt lgkmcnt(5)\n\t"
      "v_mfma_f32_32x32x16_bf16 v[88:103], v[116:119], v[212:215], v[88:103]\n\t"
      "ds_read_b128 v[116:119], %[b0] offset:27648\n\t"
      "v_max3_u32 v54, v212, v213, v54\n\t"
      "v_max3_u32 v54, v214, v215, v54\n\t"
      "s_waitcnt vmcnt(9)\n\t"
      "s_waitcnt lgkmcnt(5)\n\t"
      "v_mfma_f32_32x32x16_bf16 v[88:103], v[120:123], v[216:219], v[88:103]\n\t"
      "ds_read_b128 v[120:123], %[b0] offset:28672\n\t"
      "v_max3_u32 v54, v216, v217, v54\n\t"
      "v_max3_u32 v54, v218, v219, v54\n\t"
      "s_waitcnt vmcnt(8)\n\t"
      "s_waitcnt lgkmcnt(5)\n\t"
      "v_mfma_f32_32x32x16_bf16 v[88:103], v[124:127], v[220:223], v[88:103]\n\t"
      "ds_read_b128 v[124:127], %[b0] offset:29696\n\t"
      "v_max3_u32 v54, v220, v221, v54\n\t"
      "v_max3_u32 v54, v222, v223, v54\n\t"
      "s_waitcnt vmcnt(7)\n\t"
      "s_waitcnt lgkmcnt(5)\n\t"
      "v_mfma_f32_32x32x16_bf16 v[88:103], v[104:107], v[224:227], v[88:103]\n\t"
      "ds_read_b128 v[104:107], %[b0] offset:30720\n\t"
      "v_max3_u32 v54, v224, v225, v54\n\t"
      "v_max3_u32 v54, v226, v227, v54\n\t"
      "s_waitcnt vmcnt(6)\n\t"
      "s_waitcnt lgkmcnt(5)\n\t"
      "v_mfma_f32_32x32x16_bf16 v[88:103], v[108:111], v[228:231], v[88:103]\n\t"
      "ds_read_b128 v[108:111], %[b0] offset:31744\n\t"
      "v_max3_u32 v54, v228, v229, v54\n\t"
      "v_max3_u32 v54, v230, v231, v54\n\t"
      "s_waitcnt vmcnt(5)\n\t"
      "s_waitcnt lgkmcnt(5)\n\t"
      "v_mfma_f32_32x32x16_bf16 v[88:103], v[112:115], v[232:235], v[88:103]\n\t"
      "v_max3_u32 v54, v232, v233, v54\n\t"
      "v_max3_u32 v54, v234, v235, v54\n\t"
      "s_waitcnt vmcnt(4)\n\t"
      "s_waitcnt lgkmcnt(4)\n\t"
      "v_mfma_f32_32x32x16_bf16 v[88:103], v[116:119], v[236:239], v[88:103]\n\t"
      "v_max3_u32 v54, v236, v237, v54\n\t"
      "v_max3_u32 v54, v238, v239, v54\n\t"
      "s_waitcnt vmcnt(3)\n\t"
      "s_waitcnt lgkmcnt(3)\n\t"
      "v_mfma_f32_32x32x16_bf16 v[88:103], v[120:123], v[240:243], v[88:103]\n\t"
      "v_max3_u32 v54, v240, v241, v54\n\t"
      "v_max3_u32 v54, v242, v243, v54\n\t"
      "s_waitcnt vmcnt(2)\n\t"
      "s_waitcnt lgkmcnt(2)\n\t"
      "v_mfma_f32_32x32x16_bf16 v[88:103], v[124:127], v[244:247], v[88:103]\n\t"
      "v_max3_u32 v54, v244, v245, v54\n\t"
      "v_max3_u32 v54, v246, v247, v54\n\t"
      "s_waitcnt vmcnt(1)\n\t"
      "s_waitcnt lgkmcnt(1)\n\t"
      "v_mfma_f32_32x32x16_bf16 v[88:103], v[104:107], v[248:251], v[88:103]\n\t"
      "v_max3_u32 v54, v248, v249, v54\n\t"
      "v_max3_u32 v54, v250, v251, v54\n\t"
      "s_waitcnt vmcnt(0)\n\t"
      "s_waitcnt lgkmcnt(0)\n\t"
      "v_mfma_f32_32x32x16_bf16 v[88:103], v[108:111], v[252:255], v[88:103]\n\t"
      "v_max3_u32 v54, v252, v253, v54\n\t"
      "v_max3_u32 v54, v254, v255, v54\n\t"
      "v_cmp_eq_u32 vcc, -1, v54\n\t"
      "s_cbranch_vccz 6f\n\t"
      "s_waitcnt vmcnt(0) lgkmcnt(0)\n\t"
      "s_sub_u32 %[n], %[n], 16\n\t"
      "s_cmp_gt_i32 %[n], 0\n\t"
      "s_cbranch_scc1 8b\n\t"
      "6:\n\t"
      "s_nop 9\n\t"
      "v_cvt_pk_bf16_f32 v104, v88, v89\n\t"
      "v_cvt_pk_bf16_f32 v105, v90, v91\n\t"
      "v_cvt_pk_bf16_f32 v106, v92, v93\n\t"
      "v_cvt_pk_bf16_f32 v107, v94, v95\n\t"
      "v_cvt_pk_bf16_f32 v108, v96, v97\n\t"
      "v_cvt_pk_bf16_f32 v109, v98, v99\n\t"
      "v_cvt_pk_bf16_f32 v110, v100, v101\n\t"
      "v_cvt_pk_bf16_f32 v111, v102, v103\n\t"
      "v_mov_b32 v112, 0\n\t"
      "v_mov_b32 v113, 0\n\t"
      "v_mov_b32 v114, 0\n\t"
      "v_mov_b32 v115, 0\n\t"
      "buffer_store_dwordx4 v[104:107], %[l16], %[ws], %[xo] offen sc1\n\t"
      "buffer_store_dwordx4 v[108:111], %[l16], %[ws], %[xo] offen offset:1024 sc1\n\t"
      "v_dot2c_f32_bf16 v112, 0x3f803f80, v104\n\t"
      "v_dot2c_f32_bf16 v113, v104, v104\n\t"
      "v_dot2c_f32_bf16 v114, 0x3f803f80, v105\n\t"
      "v_dot2c_f32_bf16 v115, v105, v105\n\t"
      "v_dot2c_f32_bf16 v112, 0x3f803f80, v106\n\t"
      "v_dot2c_f32_bf16 v113, v106, v106\n\t"
      "v_dot2c_f32_bf16 v114, 0x3f803f80, v107\n\t"
      "v_dot2c_f32_bf16 v115, v107, v107\n\t"
      "v_dot2c_f32_bf16 v112, 0x3f803f80, v108\n\t"
      "v_dot2c_f32_bf16 v113, v108, v108\n\t"
      "v_dot2c_f32_bf16 v114, 0x3f803f80, v109\n\t"
      "v_dot2c_f32_bf16 v115, v109, v109\n\t"
      "v_dot2c_f32_bf16 v112, 0x3f803f80, v110\n\t"
      "v_dot2c_f32_bf16 v113, v110, v110\n\t"
      "v_dot2c_f32_bf16 v114, 0x3f803f80, v111\n\t"
      "v_dot2c_f32_bf16 v115, v111, v111\n\t"
      "s_nop 2\n\t"
      "v_pk_add_f32 v[112:113], v[112:113], v[114:115]\n\t"
      "s_nop 0\n\t"
      "v_mov_b32 v114, v112\n\t"
      "v_mov_b32 v115, v113\n\t"
      "s_nop 0\n\t"
      "v_permlane32_swap_b32 v112, v114\n\t"
      "v_permlane32_swap_b32 v113, v115\n\t"
      "v_pk_add_f32 v[112:113], v[112:113], v[114:115]\n\t"
      "buffer_store_dwordx2 v[112:113], %[stw], %[ws], %[sto] offen sc1\n\t"
      : [so] "=&s"(so), [n] "=&s"(n), [f] "=&s"(f)
      : [l16] "v"(c.lane16), [ws] "s"(c.ws), [b0] "v"(b0), [ro] "s"(ro), [xo] "s"(xo), [stw] "v"(stw), [sto] "s"(sto), [lay] "s"(layer), [so0] "s"(so0), [pv] "v"(pv), [ps] "s"(ps)
      : "v54", "v55", "v64", "v65", "v66", "v67", "v68", "v69", "v70", "v71", "v72", "v73", "v74", "v75", "v76", "v77", "v78", "v79", "v80", "v81", "v82", "v83", "v84", "v85", "v86", "v87", "v88", "v89", "v90", "v91", "v92", "v93", "v94", "v95", "v96", "v97", "v98", "v99", "v100", "v101", "v102", "v103", "v104", "v105", "v106", "v107", "v108", "v109", "v110", "v111", "v112", "v113", "v114", "v115", "v116", "v117", "v118", "v119", "v120", "v121", "v122", "v123", "v124", "v125", "v126", "v127", "v128", "v129", "v130", "v131", "v132", "v133", "v134", "v135", "v136", "v137", "v138", "v139", "v140", "v141", "v142", "v143", "v144", "v145", "v146", "v147", "v148", "v149", "v150", "v151", "v152", "v153", "v154", "v155", "v156", "v157", "v158", "v159", "v160", "v161", "v162", "v163", "v164", "v165", "v166", "v167", "v168", "v169", "v170", "v171", "v172", "v173", "v174", "v175", "v176", "v177", "v178", "v179", "v180", "v181", "v182", "v183", "v184", "v185", "v186", "v187", "v188", "v189", "v190", "v191", "v192", "v193", "v194", "v195", "v196", "v197", "v198", "v199", "v200", "v201", "v202", "v203", "v204", "v205", "v206", "v207", "v208", "v209", "v210", "v211", "v212", "v213", "v214", "v215", "v216", "v217", "v218", "v219", "v220", "v221", "v222", "v223", "v224", "v225", "v226", "v227", "v228", "v229", "v230", "v231", "v232", "v233", "v234", "v235", "v236", "v237", "v238", "v239", "v240", "v241", "v242", "v243", "v244", "v245", "v246", "v247", "v248", "v249", "v250", "v251", "v252", "v253", "v254", "v255", "vcc", "memory", "scc", ACC_FILE);
        if (layer == 0) {
          st_flag(c, c.r + c.c == 0 ? 2 : 4, 0, c.epoch + 16);
          asm volatile("ds_read_b128 a[236:239], %0\n\tds_read_b128 a[240:243], %0 offset:1024\n\tds_read_b128 a[244:247], %0 offset:2048\n\tds_read_b128 a[248:251], %0 offset:3072\n\ts_waitcnt lgkmcnt(0)"
                       :: "v"(c.lds0 + L_EX1 + c.lane16) : "memory");
        }
      }
      refresh(c);
      {
        const unsigned so0 = U(lb + O_X2F + (c.r * 4 + c.w) * 28 * 1024);
        unsigned so, n, ho = U(lb + O_HF + ((c.r * 4 + c.w) * 112 + 4 * c.c) * 1024);
        const unsigned b0 = c.lds0 + L_W1 + c.lane16, b1 = b0 + 2 * TILEB;
        const unsigned stv = (c.half * 7 * 128 + c.l31) * 8, sto = U(lb + O_ST2 + (c.r * 14 * 128 + c.w * 32) * 8);
        const u64 tg = tag_of(c, 10 + layer);
        const unsigned tl = (unsigned)tg, th = (unsigned)(tg >> 32), z0 = c.lane < 8 ? c.lane16 : OOB, fo = U(c.w == 0 ? line_of(10 + layer, c.r) + c.c * FS : OOB);
        const unsigned sto2 = U(sto + 4096);
        const u64 fl = layer == 0;
        unsigned wq = U(WS_WQF + (c.c * 84 + 28 + c.w * 14) * 1024);
        const unsigned m0b = U(c.lds0 + L_WQKV + c.w * 14 * 1024);
    asm volatile(
      "s_mov_b32 %[n], 0x40000\n\t"
      "s_sleep 6\n\t"
      "8:\n\t"
      "s_mov_b32 %[so], %[so0]\n\t"
      "buffer_load_dwordx2 v[32:33], %[stv], %[ws], %[sto] offen offset:0 sc1\n\t"
      "buffer_load_dwordx2 v[34:35], %[stv], %[ws], %[sto] offen offset:1024 sc1\n\t"
      "buffer_load_dwordx2 v[36:37], %[stv], %[ws], %[sto] offen offset:2048 sc1\n\t"
      "buffer_load_dwordx2 v[38:39], %[stv], %[ws], %[sto] offen offset:3072 sc1\n\t"
      "buffer_load_dwordx2 v[40:41], %[stv], %[ws], %[sto2] offen offset:0 sc1\n\t"
      "buffer_load_dwordx2 v[42:43], %[stv], %[ws], %[sto2] offen offset:1024 sc1\n\t"
      "buffer_load_dwordx2 v[44:45], %[stv], %[ws], %[sto2] offen offset:2048 sc1\n\t"
      "buffer_load_dwordx4 v[144:147], %[l16], %[ws], %[so] offen offset:0 sc1\n\t"
      "buffer_load_dwordx4 v[148:151], %[l16], %[ws], %[so] offen offset:1024 sc1\n\t"
      "buffer_load_dwordx4 v[152:155], %[l16], %[ws], %[so] offen offset:2048 sc1\n\t"
      "buffer_load_dwordx4 v[156:159], %[l16], %[ws], %[so] offen offset:3072 sc1\n\t"
      "s_add_u32 %[so], %[so], 0x1000\n\t"
      "buffer_load_dwordx4 v[160:163], %[l16], %[ws], %[so] offen offset:0 sc1\n\t"
      "buffer_load_dwordx4 v[164:167], %[l16], %[ws], %[so] offen offset:1024 sc1\n\t"
      "buffer_load_dwordx4 v[168:171], %[l16], %[ws], %[so] offen offset:2048 sc1\n\t"
      "buffer_load_dwordx4 v[172:175], %[l16], %[ws], %[so] offen offset:3072 sc1\n\t"
      "s_add_u32 %[so], %[so], 0x1000\n\t"
      "v_mov_b32 v54, 0\n\t"
      "s_sub_u32 %[n], %[n], 1\n\t"
      "s_waitcnt vmcnt(8)\n\t"
      "v_max3_u32 v54, v32, v33, v54\n\t"
      "v_max3_u32 v54, v34, v35, v54\n\t"
      "v_max3_u32 v54, v36, v37, v54\n\t"
      "v_max3_u32 v54, v38, v39, v54\n\t"
      "v_max3_u32 v54, v40, v41, v54\n\t"
      "v_max3_u32 v54, v42, v43, v54\n\t"
      "v_max3_u32 v54, v44, v45, v54\n\t"
      "v_cmp_eq_u32 vcc, -1, v54\n\t"
      "s_cbranch_vccz 7f\n\t"
      "s_cmp_gt_i32 %[n], 0\n\t"
      "s_cbranch_scc1 8b\n\t"
      "7:\n\t"
      "buffer_load_dwordx4 v[176:179], %[l16], %[ws], %[so] offen offset:0 sc1\n\t"
      "buffer_load_dwordx4 v[180:183], %[l16], %[ws], %[so] offen offset:1024 sc1\n\t"
      "buffer_load_dwordx4 v[184:187], %[l16], %[ws], %[so] offen offset:2048 sc1\n\t"
      "buffer_load_dwordx4 v[188:191], %[l16], %[ws], %[so] offen offset:3072 sc1\n\t"
      "s_add_u32 %[so], %[so], 0x1000\n\t"
      "ds_read_b128 v[120:123], %[b0] offset:0\n\t"
      "ds_read_b128 v[124:127], %[b0] offset:28672\n\t"
      "ds_read_b128 v[128:131], %[b0] offset:1024\n\t"
      "ds_read_b128 v[132:135], %[b0] offset:29696\n\t"
      "ds_read_b128 v[136:139], %[b0] offset:2048\n\t"
      "ds_read_b128 v[140:143], %[b0] offset:30720\n\t"
      "s_waitcnt vmcnt(11)\n\t"
      "s_waitcnt lgkmcnt(5)\n\t"
      "v_mfma_f32_32x32x16_bf16 v[56:71], v[120:123], v[144:147], 0\n\t"
      "buffer_load_dwordx4 v[192:195], %[l16], %[ws], %[so] offen offset:0 sc1\n\t"
      "ds_read_b128 v[120:123], %[b0] offset:3072\n\t"
      "v_add_f32 v32, v32, v34\n\t"
      "v_add_f32 v33, v33, v35\n\t"
      "s_waitcnt lgkmcnt(5)\n\t"
      "v_mfma_f32_32x32x16_bf16 v[72:87], v[124:127], v[144:147], 0\n\t"
      "ds_read_b128 v[124:127], %[b0] offset:31744\n\t"
      "v_max3_u32 v54, v144, v145, v54\n\t"
      "v_max3_u32 v54, v146, v147, v54\n\t"
      "s_waitcnt vmcnt(11)\n\t"
      "s_waitcnt lgkmcnt(5)\n\t"
      "v_mfma_f32_32x32x16_bf16 v[56:71], v[128:131], v[148:151], v[56:71]\n\t"
      "buffer_load_dwordx4 v[196:199], %[l16], %[ws], %[so] offen offset:1024 sc1\n\t"
      "ds_read_b128 v[128:131], %[b0] offset:4096\n\t"
      "v_add_f32 v36, v36, v38\n\t"
      "v_add_f32 v37, v37, v39\n\t"
      "s_waitcnt lgkmcnt(5)\n\t"
      "v_mfma_f32_32x32x16_bf16 v[72:87], v[132:135], v[148:151], v[72:87]\n\t"
      "ds_read_b128 v[132:135], %[b0] offset:32768\n\t"
      "v_max3_u32 v54, v148, v149, v54\n\t"
      "v_max3_u32 v54, v150, v151, v54\n\t"
      "s_waitcnt vmcnt(11)\n\t"
      "s_waitcnt lgkmcnt(5)\n\t"
      "v_mfma_f32_32x32x16_bf16 v[56:71], v[136:139], v[152:155], v[56:71]\n\t"
      "buffer_load_dwordx4 v[200:203], %[l16], %[ws], %[so] offen offset:2048 sc1\n\t"
      "ds_read_b128 v[136:139], %[b0] offset:5120\n\t"
      "v_add_f32 v40, v40, v42\n\t"
      "v_add_f32 v41, v41, v43\n\t"
      "s_waitcnt lgkmcnt(5)\n\t"
      "v_mfma_f32_32x32x16_bf16 v[72:87], v[140:143], v[152:155], v[72:87]\n\t"
      "ds_read_b128 v[140:143], %[b0] offset:33792\n\t"
      "v_max3_u32 v54, v152, v153, v54\n\t"
      "v_max3_u32 v54, v154, v155, v54\n\t"
      "s_waitcnt vmcnt(11)\n\t"
      "s_waitcnt lgkmcnt(5)\n\t"
      "v_mfma_f32_32x32x16_bf16 v[56:71], v[120:123], v[156:159], v[56:71]\n\t"
      "buffer_load_dwordx4 v[204:207], %[l16], %[ws], %[so] offen offset:3072 sc1\n\t"
      "s_add_u32 %[so], %[so], 0x1000\n\t"
      "ds_read_b128 v[120:123], %[b0] offset:6144\n\t"
      "v_add_f32 v32, v32, v36\n\t"
      "v_add_f32 v33, v33, v37\n\t"
      "s_waitcnt lgkmcnt(5)\n\t"
      "v_mfma_f32_32x32x16_bf16 v[72:87], v[124:127], v[156:159], v[72:87]\n\t"
      "ds_read_b128 v[124:127], %[b0] offset:34816\n\t"
      "v_max3_u32 v54, v156, v157, v54\n\t"
      "v_max3_u32 v54, v158, v159, v54\n\t"
      "s_waitcnt vmcnt(11)\n\t"
      "s_waitcnt lgkmcnt(5)\n\t"
      "v_mfma_f32_32x32x16_bf16 v[56:71], v[128:131], v[160:163], v[56:71]\n\t"
      "buffer_load_dwordx4 v[208:211], %[l16], %[ws], %[so] offen offset:0 sc1\n\t"
      "ds_read_b128 v[128:131], %[b0] offset:7168\n\t"
      "v_add_f32 v40, v40, v44\n\t"
      "v_add_f32 v41, v41, v45\n\t"
      "s_waitcnt lgkmcnt(5)\n\t"
      "v_mfma_f32_32x32x16_bf16 v[72:87], v[132:135], v[160:163], v[72:87]\n\t"
      "ds_read_b128 v[132:135], %[b0] offset:35840\n\t"
      "v_max3_u32 v54, v160, v161, v54\n\t"
      "v_max3_u32 v54, v162, v163, v54\n\t"
      "s_waitcnt vmcnt(11)\n\t"
      "s_waitcnt lgkmcnt(5)\n\t"
      "v_mfma_f32_32x32x16_bf16 v[56:71], v[136:139], v[164:167], v[56:71]\n\t"
      "buffer_load_dwordx4 v[212:215], %[l16], %[ws], %[so] offen offset:1024 sc1\n\t"
      "ds_read_b128 v[136:139], %[b0] offset:8192\n\t"
      "v_add_f32 v32, v32, v40\n\t"
      "v_add_f32 v33, v33, v41\n\t"
      "s_waitcnt lgkmcnt(5)\n\t"
      "v_mfma_f32_32x32x16_bf16 v[72:87], v[140:143], v[164:167], v[72:87]\n\t"
      "ds_read_b128 v[140:143], %[b0] offset:36864\n\t"
      "v_max3_u32 v54, v164, v165, v54\n\t"
      "v_max3_u32 v54, v166, v167, v54\n\t"
      "s_waitcnt vmcnt(11)\n\t"
      "s_waitcnt lgkmcnt(5)\n\t"
      "v_mfma_f32_32x32x16_bf16 v[56:71], v[120:123], v[168:171], v[56:71]\n\t"
      "buffer_load_dwordx4 v[216:219], %[l16], %[ws], %[so] offen offset:2048 sc1\n\t"
      "ds_read_b128 v[120:123], %[b0] offset:9216\n\t"
      "v_mov_b32 v34, v32\n\t"
      "v_mov_b32 v35, v33\n\t"
      "s_waitcnt lgkmcnt(5)\n\t"
      "v_mfma_f32_32x32x16_bf16 v[72:87], v[124:127], v[168:171], v[72:87]\n\t"
      "ds_read_b128 v[124:127], %[b0] offset:37888\n\t"
      "v_max3_u32 v54, v168, v169, v54\n\t"
      "v_max3_u32 v54, v170, v171, v54\n\t"
      "s_waitcnt vmcnt(11)\n\t"
      "s_waitcnt lgkmcnt(5)\n\t"
      "v_mfma_f32_32x32x16_bf16 v[56:71], v[128:131], v[172:175], v[56:71]\n\t"
      "buffer_load_dwordx4 v[220:223], %[l16], %[ws], %[so] offen offset:3072 sc1\n\t"
      "s_add_u32 %[so], %[so], 0x1000\n\t"
      "ds_read_b128 v[128:131], %[b0] offset:10240\n\t"
      "s_nop 0\n\t"
      "v_permlane32_swap_b32 v32, v34\n\t"
      "s_waitcnt lgkmcnt(5)\n\t"
      "v_mfma_f32_32x32x16_bf16 v[72:87], v[132:135], v[172:175], v[72:87]\n\t"
      "ds_read_b128 v[132:135], %[b0] offset:38912\n\t"
      "v_max3_u32 v54, v172, v173, v54\n\t"
      "v_max3_u32 v54, v174, v175, v54\n\t"
      "s_waitcnt vmcnt(11)\n\t"
      "s_waitcnt lgkmcnt(5)\n\t"
      "v_mfma_f32_32x32x16_bf16 v[56:71], v[136:139], v[176:179], v[56:71]\n\t"
      "buffer_load_dwordx4 v[224:227], %[l16], %[ws], %[so] offen offset:0 sc1\n\t"
      "ds_read_b128 v[136:139], %[b0] offset:11264\n\t"
      "v_permlane32_swap_b32 v33, v35\n\t"
      "v_add_f32 v32, v32, v34\n\t"
      "s_waitcnt lgkmcnt(5)\n\t"
      "v_mfma_f32_32x32x16_bf16 v[72:87], v[140:143], v[176:179], v[72:87]\n\t"
      "ds_read_b128 v[140:143], %[b0] offset:39936\n\t"
      "v_max3_u32 v54, v176, v177, v54\n\t"
      "v_max3_u32 v54, v178, v179, v54\n\t"
      "s_waitcnt vmcnt(11)\n\t"
      "s_waitcnt lgkmcnt(5)\n\t"
      "v_mfma_f32_32x32x16_bf16 v[56:71], v[120:123], v[180:183], v[56:71]\n\t"
      "buffer_load_dwordx4 v[228:231], %[l16], %[ws], %[so] offen offset:1024 sc1\n\t"
      "ds_read_b128 v[120:123], %[b0] offset:12288\n\t"
      "v_add_f32 v33, v33, v35\n\t"
      "v_mul_f32 v32, 0xbb124925, v32\n\t"
      "s_waitcnt lgkmcnt(5)\n\t"
      "v_mfma_f32_32x32x16_bf16 v[72:87], v[124:127], v[180:183], v[72:87]\n\t"
      "ds_read_b128 v[124:127], %[b0] offset:40960\n\t"
      "v_max3_u32 v54, v180, v181, v54\n\t"
      "v_max3_u32 v54, v182, v183, v54\n\t"
      "s_waitcnt vmcnt(11)\n\t"
      "s_waitcnt lgkmcnt(5)\n\t"
      "v_mfma_f32_32x32x16_bf16 v[56:71], v[128:131], v[184:187], v[56:71]\n\t"
      "buffer_load_dwordx4 v[232:235], %[l16], %[ws], %[so] offen offset:2048 sc1\n\t"
      "ds_read_b128 v[128:131], %[b0] offset:13312\n\t"
      "v_mul_f32 v34, v32, -v32\n\t"
      "v_fmamk_f32 v33, v33, 0x3b124925, v34\n\t"
      "s_waitcnt lgkmcnt(5)\n\t"
      "v_mfma_f32_32x32x16_bf16 v[72:87], v[132:135], v[184:187], v[72:87]\n\t"
      "ds_read_b128 v[132:135], %[b0] offset:41984\n\t"
      "v_max3_u32 v54, v184, v185, v54\n\t"
      "v_max3_u32 v54, v186, v187, v54\n\t"
      "s_waitcnt vmcnt(11)\n\t"
      "s_waitcnt lgkmcnt(5)\n\t"
      "v_mfma_f32_32x32x16_bf16 v[56:71], v[136:139], v[188:191], v[56:71]\n\t"
      "buffer_load_dwordx4 v[236:239], %[l16], %[ws], %[so] offen offset:3072 sc1\n\t"
      "s_add_u32 %[so], %[so], 0x1000\n\t"
      "ds_read_b128 v[136:139], %[b0] offset:14336\n\t"
      "v_max_f32 v33, 0, v33\n\t"
      "v_add_f32 v33, 0x3727c5ac, v33\n\t"
      "s_waitcnt lgkmcnt(5)\n\t"
      "v_mfma_f32_32x32x16_bf16 v[72:87], v[140:143], v[188:191], v[72:87]\n\t"
      "ds_read_b128 v[140:143], %[b0] offset:43008\n\t"
      "v_max3_u32 v54, v188, v189, v54\n\t"
      "v_max3_u32 v54, v190, v191, v54\n\t"
      "s_waitcnt vmcnt(11)\n\t"
      "s_waitcnt lgkmcnt(5)\n\t"
      "v_mfma_f32_32x32x16_bf16 v[56:71], v[120:123], v[192:195], v[56:71]\n\t"
      "buffer_load_dwordx4 v[240:243], %[l16], %[ws], %[so] offen offset:0 sc1\n\t"
      "ds_read_b128 v[120:123], %[b0] offset:15360\n\t"
      "v_rsq_f32 v50, v33\n\t"
      "s_waitcnt lgkmcnt(5)\n\t"
      "v_mfma_f32_32x32x16_bf16 v[72:87], v[124:127], v[192:195], v[72:87]\n\t"
      "ds_read_b128 v[124:127], %[b0] offset:44032\n\t"
      "v_max3_u32 v54, v192, v193, v54\n\t"
      "v_max3_u32 v54, v194, v195, v54\n\t"
      "s_waitcnt vmcnt(11)\n\t"
      "s_waitcnt lgkmcnt(5)\n\t"
      "v_mfma_f32_32x32x16_bf16 v[56:71], v[128:131], v[196:199], v[56:71]\n\t"
      "buffer_load_dwordx4 v[244:247], %[l16], %[ws], %[so] offen offset:1024 sc1\n\t"
      "ds_read_b128 v[128:131], %[b0] offset:16384\n\t"
      "s_nop 0\n\t"
      "v_mul_f32 v51, v33, v50\n\t"
      "s_waitcnt lgkmcnt(5)\n\t"
      "v_mfma_f32_32x32x16_bf16 v[72:87], v[132:135], v[196:199], v[72:87]\n\t"
      "ds_read_b128 v[132:135], %[b0] offset:45056\n\t"
      "v_max3_u32 v54, v196, v197, v54\n\t"
      "v_max3_u32 v54, v198, v199, v54\n\t"
      "s_waitcnt vmcnt(11)\n\t"
      "s_waitcnt lgkmcnt(5)\n\t"
      "v_mfma_f32_32x32x16_bf16 v[56:71], v[136:139], v[200:203], v[56:71]\n\t"
      "buffer_load_dwordx4 v[248:251], %[l16], %[ws], %[so] offen offset:2048 sc1\n\t"
      "ds_read_b128 v[136:139], %[b0] offset:17408\n\t"
      "v_cvt_pk_bf16_f32 v46, v32, v51\n\t"
      "v_lshlrev_b32 v34, 16, v46\n\t"
      "s_waitcnt lgkmcnt(5)\n\t"
      "v_mfma_f32_32x32x16_bf16 v[72:87], v[140:143], v[200:203], v[72:87]\n\t"
      "ds_read_b128 v[140:143], %[b0] offset:46080\n\t"
      "v_max3_u32 v54, v200, v201, v54\n\t"
      "v_max3_u32 v54, v202, v203, v54\n\t"
      "s_waitcnt vmcnt(11)\n\t"
      "s_waitcnt lgkmcnt(5)\n\t"
      "v_mfma_f32_32x32x16_bf16 v[56:71], v[120:123], v[204:207], v[56:71]\n\t"
      "buffer_load_dwordx4 v[252:255], %[l16], %[ws], %[so] offen offset:3072 sc1\n\t"
      "s_add_u32 %[so], %[so], 0x1000\n\t"
      "ds_read_b128 v[120:123], %[b0] offset:18432\n\t"
      "v_and_b32 v35, 0xffff0000, v46\n\t"
      "v_sub_f32 v34, v32, v34\n\t"
      "s_waitcnt lgkmcnt(5)\n\t"
      "v_mfma_f32_32x32x16_bf16 v[72:87], v[124:127], v[204:207], v[72:87]\n\t"
      "ds_read_b128 v[124:127], %[b0] offset:47104\n\t"
      "v_max3_u32 v54, v204, v205, v54\n\t"
      "v_max3_u32 v54, v206, v207, v54\n\t"
      "s_waitcnt vmcnt(11)\n\t"
      "s_waitcnt lgkmcnt(5)\n\t"
      "v_mfma_f32_32x32x16_bf16 v[56:71], v[128:131], v[208:211], v[56:71]\n\t"
      "ds_read_b128 v[128:131], %[b0] offset:19456\n\t"
      "v_sub_f32 v35, v51, v35\n\t"
      "v_cvt_pk_bf16_f32 v47, v34, v35\n\t"
      "s_waitcnt lgkmcnt(5)\n\t"
      "v_mfma_f32_32x32x16_bf16 v[72:87], v[132:135], v[208:211], v[72:87]\n\t"
      "ds_read_b128 v[132:135], %[b0] offset:48128\n\t"
      "v_max3_u32 v54, v208, v209, v54\n\t"
      "v_max3_u32 v54, v210, v211, v54\n\t"
      "s_waitcnt vmcnt(10)\n\t"
      "s_waitcnt lgkmcnt(5)\n\t"
      "v_mfma_f32_32x32x16_bf16 v[56:71], v[136:139], v[212:215], v[56:71]\n\t"
      "ds_read_b128 v[136:139], %[b0] offset:20480\n\t"
      "v_mov_b32 v48, v46\n\t"
      "v_mov_b32 v49, v46\n\t"
      "s_waitcnt lgkmcnt(5)\n\t"
      "v_mfma_f32_32x32x16_bf16 v[72:87], v[140:143], v[212:215], v[72:87]\n\t"
      "ds_read_b128 v[140:143], %[b0] offset:49152\n\t"
      "v_max3_u32 v54, v212, v213, v54\n\t"
      "v_max3_u32 v54, v214, v215, v54\n\t"
      "s_waitcnt vmcnt(9)\n\t"
      "s_waitcnt lgkmcnt(5)\n\t"
      "v_mfma_f32_32x32x16_bf16 v[56:71], v[120:123], v[216:219], v[56:71]\n\t"
      "ds_read_b128 v[120:123], %[b0] offset:21504\n\t"
      "v_mul_f32 v53, 0xc0135761, v50\n\t"
      "v_mul_f32 v52, v50, v50\n\t"
      "s_waitcnt lgkmcnt(5)\n\t"
      "v_mfma_f32_32x32x16_bf16 v[72:87], v[124:127], v[216:219], v[72:87]\n\t"
      "ds_read_b128 v[124:127], %[b0] offset:50176\n\t"
      "v_max3_u32 v54, v216, v217, v54\n\t"
      "v_max3_u32 v54, v218, v219, v54\n\t"
      "s_waitcnt vmcnt(8)\n\t"
      "s_waitcnt lgkmcnt(5)\n\t"
      "v_mfma_f32_32x32x16_bf16 v[56:71], v[128:131], v[220:223], v[56:71]\n\t"
      "ds_read_b128 v[128:131], %[b0] offset:22528\n\t"
      "v_mul_f32 v52, 0x3d372713, v52\n\t"
      "v_mul_f32 v52, v52, v53\n\t"
      "s_waitcnt lgkmcnt(5)\n\t"
      "v_mfma_f32_32x32x16_bf16 v[72:87], v[132:135], v[220:223], v[72:87]\n\t"
      "ds_read_b128 v[132:135], %[b0] offset:51200\n\t"
      "v_max3_u32 v54, v220, v221, v54\n\t"
      "v_max3_u32 v54, v222, v223, v54\n\t"
      "s_waitcnt vmcnt(7)\n\t"
      "s_waitcnt lgkmcnt(5)\n\t"
      "v_mfma_f32_32x32x16_bf16 v[56:71], v[136:139], v[224:227], v[56:71]\n\t"
      "ds_read_b128 v[136:139], %[b0] offset:23552\n\t"
      "s_waitcnt lgkmcnt(5)\n\t"
      "v_mfma_f32_32x32x16_bf16 v[72:87], v[140:143], v[224:227], v[72:87]\n\t"
      "ds_read_b128 v[140:143], %[b0] offset:52224\n\t"
      "v_max3_u32 v54, v224, v225, v54\n\t"
      "v_max3_u32 v54, v226, v227, v54\n\t"
      "s_waitcnt vmcnt(6)\n\t"
      "s_waitcnt lgkmcnt(5)\n\t"
      "v_mfma_f32_32x32x16_bf16 v[56:71], v[120:123], v[228:231], v[56:71]\n\t"
      "ds_read_b128 v[120:123], %[b0] offset:24576\n\t"
      "s_waitcnt lgkmcnt(5)\n\t"
      "v_mfma_f32_32x32x16_bf16 v[72:87], v[124:127], v[228:231], v[72:87]\n\t"
      "ds_read_b128 v[124:127], %[b0] offset:53248\n\t"
      "v_max3_u32 v54, v228, v229, v54\n\t"
      "v_max3_u32 v54, v230, v231, v54\n\t"
      "s_waitcnt vmcnt(5)\n\t"
      "s_waitcnt lgkmcnt(5)\n\t"
      "v_mfma_f32_32x32x16_bf16 v[56:71], v[128:131], v[232:235], v[56:71]\n\t"
      "ds_read_b128 v[128:131], %[b0] offset:25600\n\t"
      "s_waitcnt lgkmcnt(5)\n\t"
      "v_mfma_f32_32x32x16_bf16 v[72:87], v[132:135], v[232:235], v[72:87]\n\t"
      "ds_read_b128 v[132:135], %[b0] offset:54272\n\t"
      "v_max3_u32 v54, v232, v233, v54\n\t"
      "v_max3_u32 v54, v234, v235, v54\n\t"
      "s_waitcnt vmcnt(4)\n\t"
      "s_waitcnt lgkmcnt(5)\n\t"
      "v_mfma_f32_32x32x16_bf16 v[56:71], v[136:139], v[236:239], v[56:71]\n\t"
      "ds_read_b128 v[136:139], %[b0] offset:26624\n\t"
      "s_waitcnt lgkmcnt(5)\n\t"
      "v_mfma_f32_32x32x16_bf16 v[72:87], v[140:143], v[236:239], v[72:87]\n\t"
      "ds_read_b128 v[140:143], %[b0] offset:55296\n\t"
      "v_max3_u32 v54, v236, v237, v54\n\t"
      "v_max3_u32 v54, v238, v239, v54\n\t"
      "s_waitcnt vmcnt(3)\n\t"
      "s_waitcnt lgkmcnt(5)\n\t"
      "v_mfma_f32_32x32x16_bf16 v[56:71], v[120:123], v[240:243], v[56:71]\n\t"
      "ds_read_b128 v[120:123], %[b0] offset:27648\n\t"
      "s_waitcnt lgkmcnt(5)\n\t"
      "v_mfma_f32_32x32x16_bf16 v[72:87], v[124:127], v[240:243], v[72:87]\n\t"
      "ds_read_b128 v[124:127], %[b0] offset:56320\n\t"
      "v_max3_u32 v54, v240, v241, v54\n\t"
      "v_max3_u32 v54, v242, v243, v54\n\t"
      "s_waitcnt vmcnt(2)\n\t"
      "s_waitcnt lgkmcnt(5)\n\t"
      "v_mfma_f32_32x32x16_bf16 v[56:71], v[128:131], v[244:247], v[56:71]\n\t"
      "ds_read_b128 v[128:131], %[b1] offset:0\n\t"
      "s_waitcnt lgkmcnt(5)\n\t"
      "v_mfma_f32_32x32x16_bf16 v[72:87], v[132:135], v[244:247], v[72:87]\n\t"
      "ds_read_b128 v[132:135], %[b1] offset:1024\n\t"
      "v_max3_u32 v54, v244, v245, v54\n\t"
      "v_max3_u32 v54, v246, v247, v54\n\t"
      "s_waitcnt vmcnt(1)\n\t"
      "s_waitcnt lgkmcnt(5)\n\t"
      "v_mfma_f32_32x32x16_bf16 v[56:71], v[136:139], v[248:251], v[56:71]\n\t"
      "ds_read_b128 v[136:139], %[b1] offset:2048\n\t"
      "s_waitcnt lgkmcnt(5)\n\t"
      "v_mfma_f32_32x32x16_bf16 v[72:87], v[140:143], v[248:251], v[72:87]\n\t"
      "ds_read_b128 v[140:143], %[b1] offset:3072\n\t"
      "v_max3_u32 v54, v248, v249, v54\n\t"
      "v_max3_u32 v54, v250, v251, v54\n\t"
      "s_waitcnt vmcnt(0)\n\t"
      "s_waitcnt lgkmcnt(5)\n\t"
      "v_mfma_f32_32x32x16_bf16 v[56:71], v[120:123], v[252:255], v[56:71]\n\t"
      "ds_read_b128 v[120:123], %[b1] offset:4096\n\t"
      "s_waitcnt lgkmcnt(5)\n\t"
      "v_mfma_f32_32x32x16_bf16 v[72:87], v[124:127], v[252:255], v[72:87]\n\t"
      "ds_read_b128 v[124:127], %[b1] offset:5120\n\t"
      "v_max3_u32 v54, v252, v253, v54\n\t"
      "v_max3_u32 v54, v254, v255, v54\n\t"
      "v_cmp_eq_u32 vcc, -1, v54\n\t"
      "s_cbranch_vccz 6f\n\t"
      "s_waitcnt vmcnt(0) lgkmcnt(0)\n\t"
      "s_sub_u32 %[n], %[n], 16\n\t"
      "s_cmp_gt_i32 %[n], 0\n\t"
      "s_cbranch_scc1 8b\n\t"
      "6:\n\t"
      "s_mov_b32 m0, %[m0b]\n\t"
      "s_mov_b64 vcc, %[fl]\n\t"
      "s_barrier\n\t"
      "v_mfma_f32_32x32x16_bf16 v[56:71], a[236:239], v[46:49], v[56:71]\n\t"
      "v_mfma_f32_32x32x16_bf16 v[72:87], a[240:243], v[46:49], v[72:87]\n\t"
      "s_waitcnt lgkmcnt(4)\n\t"
      "v_mfma_f32_32x32x16_bf16 v[88:103], v[128:131], v[144:147], 0\n\t"
      "ds_read_b128 v[128:131], %[b1] offset:6144\n\t"
      "s_cbranch_vccz 20f\n\t"
      "buffer_load_dwordx4 %[l16], %[ws], %[wq] offen offset:0 sc1 lds\n\t"
      "buffer_load_dwordx4 %[l16], %[ws], %[wq] offen offset:1024 sc1 lds\n\t"
      "buffer_load_dwordx4 %[l16], %[ws], %[wq] offen offset:2048 sc1 lds\n\t"
      "buffer_load_dwordx4 %[l16], %[ws], %[wq] offen offset:3072 sc1 lds\n\t"
      "s_add_u32 m0, m0, 0x1000\n\t"
      "s_add_u32 %[wq], %[wq], 0x1000\n\t"
      "20:\n\t"
      "v_mfma_f32_32x32x16_bf16 v[88:103], v[132:135], v[148:151], v[88:103]\n\t"
      "ds_read_b128 v[132:135], %[b1] offset:7168\n\t"
      "s_waitcnt lgkmcnt(4)\n\t"
      "v_mfma_f32_32x32x16_bf16 v[88:103], v[136:139], v[152:155], v[88:103]\n\t"
      "ds_read_b128 v[136:139], %[b1] offset:8192\n\t"
      "v_mfma_f32_32x32x16_bf16 v[88:103], v[140:143], v[156:159], v[88:103]\n\t"
      "ds_read_b128 v[140:143], %[b1] offset:9216\n\t"
      "s_waitcnt lgkmcnt(4)\n\t"
      "v_mfma_f32_32x32x16_bf16 v[88:103], v[120:123], v[160:163], v[88:103]\n\t"
      "ds_read_b128 v[120:123], %[b1] offset:10240\n\t"
      "v_mul_f32 v32, v56, v56\n\t"
      "v_mul_f32 v33, v57, v57\n\t"
      "v_mul_f32 v34, v58, v58\n\t"
      "v_mul_f32 v35, v59, v59\n\t"
      "v_fma_f32 v32, v32, v52, v53\n\t"
      "v_mfma_f32_32x32x16_bf16 v[88:103], v[124:127], v[164:167], v[88:103]\n\t"
      "ds_read_b128 v[124:127], %[b1] offset:11264\n\t"
      "v_fma_f32 v33, v33, v52, v53\n\t"
      "v_fma_f32 v34, v34, v52, v53\n\t"
      "v_fma_f32 v35, v35, v52, v53\n\t"
      "v_mul_f32 v32, v32, v56\n\t"
      "v_mul_f32 v33, v33, v57\n\t"
      "v_mul_f32 v34, v34, v58\n\t"
      "s_waitcnt lgkmcnt(4)\n\t"
      "v_mfma_f32_32x32x16_bf16 v[88:103], v[128:131], v[168:171], v[88:103]\n\t"
      "ds_read_b128 v[128:131], %[b1] offset:12288\n\t"
      "v_mul_f32 v35, v35, v59\n\t"
      "v_exp_f32 v32, v32\n\t"
      "v_exp_f32 v33, v33\n\t"
      "v_mfma_f32_32x32x16_bf16 v[88:103], v[132:135], v[172:175], v[88:103]\n\t"
      "ds_read_b128 v[132:135], %[b1] offset:13312\n\t"
      "v_exp_f32 v34, v34\n\t"
      "v_exp_f32 v35, v35\n\t"
      "v_fma_f32 v32, v32, v51, v51\n\t"
      "v_fma_f32 v33, v33, v51, v51\n\t"
      "s_waitcnt lgkmcnt(4)\n\t"
      "v_mfma_f32_32x32x16_bf16 v[88:103], v[136:139], v[176:179], v[88:103]\n\t"
      "ds_read_b128 v[136:139], %[b1] offset:14336\n\t"
      "s_cbranch_vccz 24f\n\t"
      "buffer_load_dwordx4 %[l16], %[ws], %[wq] offen offset:0 sc1 lds\n\t"
      "buffer_load_dwordx4 %[l16], %[ws], %[wq] offen offset:1024 sc1 lds\n\t"
      "buffer_load_dwordx4 %[l16], %[ws], %[wq] offen offset:2048 sc1 lds\n\t"
      "buffer_load_dwordx4 %[l16], %[ws], %[wq] offen offset:3072 sc1 lds\n\t"
      "s_add_u32 m0, m0, 0x1000\n\t"
      "s_add_u32 %[wq], %[wq], 0x1000\n\t"
      "24:\n\t"
      "v_mfma_f32_32x32x16_bf16 v[88:103], v[140:143], v[180:183], v[88:103]\n\t"
      "ds_read_b128 v[140:143], %[b1] offset:15360\n\t"
      "v_fma_f32 v34, v34, v51, v51\n\t"
      "v_fma_f32 v35, v35, v51, v51\n\t"
      "v_rcp_f32 v32, v32\n\t"
      "v_rcp_f32 v33, v33\n\t"
      "s_waitcnt lgkmcnt(4)\n\t"
      "v_mfma_f32_32x32x16_bf16 v[88:103], v[120:123], v[184:187], v[88:103]\n\t"
      "ds_read_b128 v[120:123], %[b1] offset:16384\n\t"
      "v_rcp_f32 v34, v34\n\t"
      "v_rcp_f32 v35, v35\n\t"
      "v_mul_f32 v32, v32, v56\n\t"
      "v_mfma_f32_32x32x16_bf16 v[88:103], v[124:127], v[188:191], v[88:103]\n\t"
      "ds_read_b128 v[124:127], %[b1] offset:17408\n\t"
      "v_mul_f32 v33, v33, v57\n\t"
      "v_mul_f32 v34, v34, v58\n\t"
      "v_mul_f32 v35, v35, v59\n\t"
      "v_cvt_pk_bf16_f32 v56, v32, v33\n\t"
      "v_cvt_pk_bf16_f32 v57, v34, v35\n\t"
      "v_mul_f32 v36, v60, v60\n\t"
      "s_waitcnt lgkmcnt(4)\n\t"
      "v_mfma_f32_32x32x16_bf16 v[88:103], v[128:131], v[192:195], v[88:103]\n\t"
      "ds_read_b128 v[128:131], %[b1] offset:18432\n\t"
      "v_mul_f32 v37, v61, v61\n\t"
      "v_mul_f32 v38, v62, v62\n\t"
      "v_mul_f32 v39, v63, v63\n\t"
      "v_fma_f32 v36, v36, v52, v53\n\t"
      "v_fma_f32 v37, v37, v52, v53\n\t"
      "v_mfma_f32_32x32x16_bf16 v[88:103], v[132:135], v[196:199], v[88:103]\n\t"
      "ds_read_b128 v[132:135], %[b1] offset:19456\n\t"
      "v_fma_f32 v38, v38, v52, v53\n\t"
      "v_fma_f32 v39, v39, v52, v53\n\t"
      "v_mul_f32 v36, v36, v60\n\t"
      "v_mul_f32 v37, v37, v61\n\t"
      "v_mul_f32 v38, v38, v62\n\t"
      "v_mul_f32 v39, v39, v63\n\t"
      "s_waitcnt lgkmcnt(4)\n\t"
      "v_mfma_f32_32x32x16_bf16 v[88:103], v[136:139], v[200:203], v[88:103]\n\t"
      "ds_read_b128 v[136:139], %[b1] offset:20480\n\t"
      "v_exp_f32 v36, v36\n\t"
      "v_exp_f32 v37, v37\n\t"
      "v_mfma_f32_32x32x16_bf16 v[88:103], v[140:143], v[204:207], v[88:103]\n\t"
      "ds_read_b128 v[140:143], %[b1] offset:21504\n\t"
      "v_exp_f32 v38, v38\n\t"
      "v_exp_f32 v39, v39\n\t"
      "v_fma_f32 v36, v36, v51, v51\n\t"
      "v_fma_f32 v37, v37, v51, v51\n\t"
      "s_waitcnt lgkmcnt(4)\n\t"
      "v_mfma_f32_32x32x16_bf16 v[88:103], v[120:123], v[208:211], v[88:103]\n\t"
      "ds_read_b128 v[120:123], %[b1] offset:22528\n\t"
      "s_cbranch_vccz 28f\n\t"
      "buffer_load_dwordx4 %[l16], %[ws], %[wq] offen offset:0 sc1 lds\n\t"
      "buffer_load_dwordx4 %[l16], %[ws], %[wq] offen offset:1024 sc1 lds\n\t"
      "buffer_load_dwordx4 %[l16], %[ws], %[wq] offen offset:2048 sc1 lds\n\t"
      "buffer_load_dwordx4 %[l16], %[ws], %[wq] offen offset:3072 sc1 lds\n\t"
      "s_add_u32 m0, m0, 0x1000\n\t"
      "s_add_u32 %[wq], %[wq], 0x1000\n\t"
      "28:\n\t"
      "v_mfma_f32_32x32x16_bf16 v[88:103], v[124:127], v[212:215], v[88:103]\n\t"
      "ds_read_b128 v[124:127], %[b1] offset:23552\n\t"
      "v_fma_f32 v38, v38, v51, v51\n\t"
      "v_fma_f32 v39, v39, v51, v51\n\t"
      "v_rcp_f32 v36, v36\n\t"
      "v_rcp_f32 v37, v37\n\t"
      "s_waitcnt lgkmcnt(4)\n\t"
      "v_mfma_f32_32x32x16_bf16 v[88:103], v[128:131], v[216:219], v[88:103]\n\t"
      "ds_read_b128 v[128:131], %[b1] offset:24576\n\t"
      "v_rcp_f32 v38, v38\n\t"
      "v_rcp_f32 v39, v39\n\t"
      "v_mul_f32 v36, v36, v60\n\t"
      "v_mfma_f32_32x32x16_bf16 v[88:103], v[132:135], v[220:223], v[88:103]\n\t"
      "ds_read_b128 v[132:135], %[b1] offset:25600\n\t"
      "v_mul_f32 v37, v37, v61\n\t"
      "v_mul_f32 v38, v38, v62\n\t"
      "v_mul_f32 v39, v39, v63\n\t"
      "v_cvt_pk_bf16_f32 v58, v36, v37\n\t"
      "v_cvt_pk_bf16_f32 v59, v38, v39\n\t"
      "buffer_store_dwordx4 v[56:59], %[l16], %[ws], %[ho] offen offset:0 sc1\n\t"
      "s_waitcnt lgkmcnt(4)\n\t"
      "v_mfma_f32_32x32x16_bf16 v[88:103], v[136:139], v[224:227], v[88:103]\n\t"
      "ds_read_b128 v[136:139], %[b1] offset:26624\n\t"
      "v_mul_f32 v32, v64, v64\n\t"
      "v_mul_f32 v33, v65, v65\n\t"
      "v_mul_f32 v34, v66, v66\n\t"
      "v_mul_f32 v35, v67, v67\n\t"
      "v_fma_f32 v32, v32, v52, v53\n\t"
      "v_mfma_f32_32x32x16_bf16 v[88:103], v[140:143], v[228:231], v[88:103]\n\t"
      "ds_read_b128 v[140:143], %[b1] offset:27648\n\t"
      "v_fma_f32 v33, v33, v52, v53\n\t"
      "v_fma_f32 v34, v34, v52, v53\n\t"
      "v_fma_f32 v35, v35, v52, v53\n\t"
      "v_mul_f32 v32, v32, v64\n\t"
      "v_mul_f32 v33, v33, v65\n\t"
      "v_mul_f32 v34, v34, v66\n\t"
      "s_waitcnt lgkmcnt(4)\n\t"
      "v_mfma_f32_32x32x16_bf16 v[88:103], v[120:123], v[232:235], v[88:103]\n\t"
      "ds_read_b128 v[120:123], %[b1] offset:28672\n\t"
      "v_mul_f32 v35, v35, v67\n\t"
      "v_exp_f32 v32, v32\n\t"
      "v_exp_f32 v33, v33\n\t"
      "v_mfma_f32_32x32x16_bf16 v[88:103], v[124:127], v[236:239], v[88:103]\n\t"
      "ds_read_b128 v[124:127], %[b1] offset:29696\n\t"
      "v_exp_f32 v34, v34\n\t"
      "v_exp_f32 v35, v35\n\t"
      "v_fma_f32 v32, v32, v51, v51\n\t"
      "v_fma_f32 v33, v33, v51, v51\n\t"
      "s_waitcnt lgkmcnt(4)\n\t"
      "v_mfma_f32_32x32x16_bf16 v[88:103], v[128:131], v[240:243], v[88:103]\n\t"
      "ds_read_b128 v[128:131], %[b1] offset:30720\n\t"
      "s_cbranch_vccz 212f\n\t"
      "buffer_load_dwordx4 %[l16], %[ws], %[wq] offen offset:0 sc1 lds\n\t"
      "buffer_load_dwordx4 %[l16], %[ws], %[wq] offen offset:1024 sc1 lds\n\t"
      "212:\n\t"
      "v_fma_f32 v34, v34, v51, v51\n\t"
      "v_fma_f32 v35, v35, v51, v51\n\t"
      "v_mfma_f32_32x32x16_bf16 v[88:103], v[132:135], v[244:247], v[88:103]\n\t"
      "ds_read_b128 v[132:135], %[b1] offset:31744\n\t"
      "v_rcp_f32 v32, v32\n\t"
      "v_rcp_f32 v33, v33\n\t"
      "v_rcp_f32 v34, v34\n\t"
      "s_waitcnt lgkmcnt(4)\n\t"
      "v_mfma_f32_32x32x16_bf16 v[88:103], v[136:139], v[248:251], v[88:103]\n\t"
      "ds_read_b128 v[136:139], %[b1] offset:32768\n\t"
      "v_rcp_f32 v35, v35\n\t"
      "v_mul_f32 v32, v32, v64\n\t"
      "v_mul_f32 v33, v33, v65\n\t"
      "v_mul_f32 v34, v34, v66\n\t"
      "v_mfma_f32_32x32x16_bf16 v[88:103], v[140:143], v[252:255], v[88:103]\n\t"
      "ds_read_b128 v[140:143], %[b1] offset:33792\n\t"
      "v_mfma_f32_32x32x16_bf16 v[88:103], a[244:247], v[46:49], v[88:103]\n\t"
      "v_mul_f32 v35, v35, v67\n\t"
      "v_cvt_pk_bf16_f32 v60, v32, v33\n\t"
      "v_cvt_pk_bf16_f32 v61, v34, v35\n\t"
      "v_mul_f32 v36, v68, v68\n\t"
      "v_mul_f32 v37, v69, v69\n\t"
      "v_mul_f32 v38, v70, v70\n\t"
      "s_waitcnt lgkmcnt(4)\n\t"
      "v_mfma_f32_32x32x16_bf16 v[104:119], v[120:123], v[144:147], 0\n\t"
      "ds_read_b128 v[120:123], %[b1] offset:34816\n\t"
      "v_mul_f32 v39, v71, v71\n\t"
      "v_fma_f32 v36, v36, v52, v53\n\t"
      "v_fma_f32 v37, v37, v52, v53\n\t"
      "v_fma_f32 v38, v38, v52, v53\n\t"
      "v_fma_f32 v39, v39, v52, v53\n\t"
      "v_mfma_f32_32x32x16_bf16 v[104:119], v[124:127], v[148:151], v[104:119]\n\t"
      "ds_read_b128 v[124:127], %[b1] offset:35840\n\t"
      "v_mul_f32 v36, v36, v68\n\t"
      "v_mul_f32 v37, v37, v69\n\t"
      "v_mul_f32 v38, v38, v70\n\t"
      "v_mul_f32 v39, v39, v71\n\t"
      "v_exp_f32 v36, v36\n\t"
      "s_waitcnt lgkmcnt(4)\n\t"
      "v_mfma_f32_32x32x16_bf16 v[104:119], v[128:131], v[152:155], v[104:119]\n\t"
      "ds_read_b128 v[128:131], %[b1] offset:36864\n\t"
      "v_exp_f32 v37, v37\n\t"
      "v_exp_f32 v38, v38\n\t"
      "v_mfma_f32_32x32x16_bf16 v[104:119], v[132:135], v[156:159], v[104:119]\n\t"
      "ds_read_b128 v[132:135], %[b1] offset:37888\n\t"
      "v_exp_f32 v39, v39\n\t"
      "v_fma_f32 v36, v36, v51, v51\n\t"
      "v_fma_f32 v37, v37, v51, v51\n\t"
      "v_fma_f32 v38, v38, v51, v51\n\t"
      "v_fma_f32 v39, v39, v51, v51\n\t"
      "s_waitcnt lgkmcnt(4)\n\t"
      "v_mfma_f32_32x32x16_bf16 v[104:119], v[136:139], v[160:163], v[104:119]\n\t"
      "ds_read_b128 v[136:139], %[b1] offset:38912\n\t"
      "v_rcp_f32 v36, v36\n\t"
      "v_rcp_f32 v37, v37\n\t"
      "v_mfma_f32_32x32x16_bf16 v[104:119], v[140:143], v[164:167], v[104:119]\n\t"
      "ds_read_b128 v[140:143], %[b1] offset:39936\n\t"
      "v_rcp_f32 v38, v38\n\t"
      "v_rcp_f32 v39, v39\n\t"
      "v_mul_f32 v36, v36, v68\n\t"
      "v_mul_f32 v37, v37, v69\n\t"
      "s_waitcnt lgkmcnt(4)\n\t"
      "v_mfma_f32_32x32x16_bf16 v[104:119], v[120:123], v[168:171], v[104:119]\n\t"
      "ds_read_b128 v[120:123], %[b1] offset:40960\n\t"
      "v_mul_f32 v38, v38, v70\n\t"
      "v_mul_f32 v39, v39, v71\n\t"
      "v_cvt_pk_bf16_f32 v62, v36, v37\n\t"
      "v_cvt_pk_bf16_f32 v63, v38, v39\n\t"
      "buffer_store_dwordx4 v[60:63], %[l16], %[ws], %[ho] offen offset:1024 sc1\n\t"
      "v_mfma_f32_32x32x16_bf16 v[104:119], v[124:127], v[172:175], v[104:119]\n\t"
      "ds_read_b128 v[124:127], %[b1] offset:41984\n\t"
      "v_mul_f32 v32, v72, v72\n\t"
      "v_mul_f32 v33, v73, v73\n\t"
      "v_mul_f32 v34, v74, v74\n\t"
      "v_mul_f32 v35, v75, v75\n\t"
      "v_fma_f32 v32, v32, v52, v53\n\t"
      "v_fma_f32 v33, v33, v52, v53\n\t"
      "s_waitcnt lgkmcnt(4)\n\t"
      "v_mfma_f32_32x32x16_bf16 v[104:119], v[128:131], v[176:179], v[104:119]\n\t"
      "ds_read_b128 v[128:131], %[b1] offset:43008\n\t"
      "v_fma_f32 v34, v34, v52, v53\n\t"
      "v_fma_f32 v35, v35, v52, v53\n\t"
      "v_mul_f32 v32, v32, v72\n\t"
      "v_mul_f32 v33, v33, v73\n\t"
      "v_mul_f32 v34, v34, v74\n\t"
      "v_mfma_f32_32x32x16_bf16 v[104:119], v[132:135], v[180:183], v[104:119]\n\t"
      "ds_read_b128 v[132:135], %[b1] offset:44032\n\t"
      "v_mul_f32 v35, v35, v75\n\t"
      "v_exp_f32 v32, v32\n\t"
      "v_exp_f32 v33, v33\n\t"
      "s_waitcnt lgkmcnt(4)\n\t"
      "v_mfma_f32_32x32x16_bf16 v[104:119], v[136:139], v[184:187], v[104:119]\n\t"
      "ds_read_b128 v[136:139], %[b1] offset:45056\n\t"
      "v_exp_f32 v34, v34\n\t"
      "v_exp_f32 v35, v35\n\t"
      "v_fma_f32 v32, v32, v51, v51\n\t"
      "v_mfma_f32_32x32x16_bf16 v[104:119], v[140:143], v[188:191], v[104:119]\n\t"
      "ds_read_b128 v[140:143], %[b1] offset:46080\n\t"
      "v_fma_f32 v33, v33, v51, v51\n\t"
      "v_fma_f32 v34, v34, v51, v51\n\t"
      "v_fma_f32 v35, v35, v51, v51\n\t"
      "v_rcp_f32 v32, v32\n\t"
      "s_waitcnt lgkmcnt(4)\n\t"
      "v_mfma_f32_32x32x16_bf16 v[104:119], v[120:123], v[192:195], v[104:119]\n\t"
      "ds_read_b128 v[120:123], %[b1] offset:47104\n\t"
      "v_rcp_f32 v33, v33\n\t"
      "v_rcp_f32 v34, v34\n\t"
      "v_mfma_f32_32x32x16_bf16 v[104:119], v[124:127], v[196:199], v[104:119]\n\t"
      "ds_read_b128 v[124:127], %[b1] offset:48128\n\t"
      "v_rcp_f32 v35, v35\n\t"
      "v_mul_f32 v32, v32, v72\n\t"
      "v_mul_f32 v33, v33, v73\n\t"
      "v_mul_f32 v34, v34, v74\n\t"
      "v_mul_f32 v35, v35, v75\n\t"
      "s_waitcnt lgkmcnt(4)\n\t"
      "v_mfma_f32_32x32x16_bf16 v[104:119], v[128:131], v[200:203], v[104:119]\n\t"
      "ds_read_b128 v[128:131], %[b1] offset:49152\n\t"
      "v_cvt_pk_bf16_f32 v72, v32, v33\n\t"
      "v_cvt_pk_bf16_f32 v73, v34, v35\n\t"
      "v_mul_f32 v36, v76, v76\n\t"
      "v_mul_f32 v37, v77, v77\n\t"
      "v_mul_f32 v38, v78, v78\n\t"
      "v_mfma_f32_32x32x16_bf16 v[104:119], v[132:135], v[204:207], v[104:119]\n\t"
      "ds_read_b128 v[132:135], %[b1] offset:50176\n\t"
      "v_mul_f32 v39, v79, v79\n\t"
      "v_fma_f32 v36, v36, v52, v53\n\t"
      "v_fma_f32 v37, v37, v52, v53\n\t"
      "v_fma_f32 v38, v38, v52, v53\n\t"
      "v_fma_f32 v39, v39, v52, v53\n\t"
      "v_mul_f32 v36, v36, v76\n\t"
      "s_waitcnt lgkmcnt(4)\n\t"
      "v_mfma_f32_32x32x16_bf16 v[104:119], v[136:139], v[208:211], v[104:119]\n\t"
      "ds_read_b128 v[136:139], %[b1] offset:51200\n\t"
      "v_mul_f32 v37, v37, v77\n\t"
      "v_mul_f32 v38, v38, v78\n\t"
      "v_mul_f32 v39, v39, v79\n\t"
      "v_exp_f32 v36, v36\n\t"
      "v_mfma_f32_32x32x16_bf16 v[104:119], v[140:143], v[212:215], v[104:119]\n\t"
      "ds_read_b128 v[140:143], %[b1] offset:52224\n\t"
      "v_exp_f32 v37, v37\n\t"
      "v_exp_f32 v38, v38\n\t"
      "v_exp_f32 v39, v39\n\t"
      "s_waitcnt lgkmcnt(4)\n\t"
      "v_mfma_f32_32x32x16_bf16 v[104:119], v[120:123], v[216:219], v[104:119]\n\t"
      "ds_read_b128 v[120:123], %[b1] offset:53248\n\t"
      "v_fma_f32 v36, v36, v51, v51\n\t"
      "v_fma_f32 v37, v37, v51, v51\n\t"
      "v_fma_f32 v38, v38, v51, v51\n\t"
      "v_fma_f32 v39, v39, v51, v51\n\t"
      "v_mfma_f32_32x32x16_bf16 v[104:119], v[124:127], v[220:223], v[104:119]\n\t"
      "ds_read_b128 v[124:127], %[b1] offset:54272\n\t"
      "v_rcp_f32 v36, v36\n\t"
      "v_rcp_f32 v37, v37\n\t"
      "v_rcp_f32 v38, v38\n\t"
      "s_waitcnt lgkmcnt(4)\n\t"
      "v_mfma_f32_32x32x16_bf16 v[104:119], v[128:131], v[224:227], v[104:119]\n\t"
      "ds_read_b128 v[128:131], %[b1] offset:55296\n\t"
      "v_rcp_f32 v39, v39\n\t"
      "v_mul_f32 v36, v36, v76\n\t"
      "v_mul_f32 v37, v37, v77\n\t"
      "v_mul_f32 v38, v38, v78\n\t"
      "v_mfma_f32_32x32x16_bf16 v[104:119], v[132:135], v[228:231], v[104:119]\n\t"
      "ds_read_b128 v[132:135], %[b1] offset:56320\n\t"
      "v_mul_f32 v39, v39, v79\n\t"
      "v_cvt_pk_bf16_f32 v74, v36, v37\n\t"
      "v_cvt_pk_bf16_f32 v75, v38, v39\n\t"
      "buffer_store_dwordx4 v[72:75], %[l16], %[ws], %[ho] offen offset:2048 sc1\n\t"
      "v_mul_f32 v32, v80, v80\n\t"
      "v_mul_f32 v33, v81, v81\n\t"
      "s_waitcnt lgkmcnt(4)\n\t"
      "v_mfma_f32_32x32x16_bf16 v[104:119], v[136:139], v[232:235], v[104:119]\n\t"
      "v_mul_f32 v34, v82, v82\n\t"
      "v_mul_f32 v35, v83, v83\n\t"
      "v_fma_f32 v32, v32, v52, v53\n\t"
      "v_fma_f32 v33, v33, v52, v53\n\t"
      "v_fma_f32 v34, v34, v52, v53\n\t"
      "v_mfma_f32_32x32x16_bf16 v[104:119], v[140:143], v[236:239], v[104:119]\n\t"
      "v_fma_f32 v35, v35, v52, v53\n\t"
      "v_mul_f32 v32, v32, v80\n\t"
      "v_mul_f32 v33, v33, v81\n\t"
      "v_mul_f32 v34, v34, v82\n\t"
      "v_mul_f32 v35, v35, v83\n\t"
      "s_waitcnt lgkmcnt(2)\n\t"
      "v_mfma_f32_32x32x16_bf16 v[104:119], v[120:123], v[240:243], v[104:119]\n\t"
      "v_exp_f32 v32, v32\n\t"
      "v_exp_f32 v33, v33\n\t"
      "v_mfma_f32_32x32x16_bf16 v[104:119], v[124:127], v[244:247], v[104:119]\n\t"
      "v_exp_f32 v34, v34\n\t"
      "v_exp_f32 v35, v35\n\t"
      "v_fma_f32 v32, v32, v51, v51\n\t"
      "v_fma_f32 v33, v33, v51, v51\n\t"
      "s_waitcnt lgkmcnt(0)\n\t"
      "v_mfma_f32_32x32x16_bf16 v[104:119], v[128:131], v[248:251], v[104:119]\n\t"
      "v_fma_f32 v34, v34, v51, v51\n\t"
      "v_fma_f32 v35, v35, v51, v51\n\t"
      "v_rcp_f32 v32, v32\n\t"
      "v_mfma_f32_32x32x16_bf16 v[104:119], v[132:135], v[252:255], v[104:119]\n\t"
      "v_mfma_f32_32x32x16_bf16 v[104:119], a[248:251], v[46:49], v[104:119]\n\t"
      "v_rcp_f32 v33, v33\n\t"
      "v_rcp_f32 v34, v34\n\t"
      "v_rcp_f32 v35, v35\n\t"
      "v_mul_f32 v32, v32, v80\n\t"
      "v_mul_f32 v33, v33, v81\n\t"
      "v_mul_f32 v34, v34, v82\n\t"
      "v_mul_f32 v35, v35, v83\n\t"
      "v_cvt_pk_bf16_f32 v76, v32, v33\n\t"
      "v_cvt_pk_bf16_f32 v77, v34, v35\n\t"
      "v_mul_f32 v36, v84, v84\n\t"
      "v_mul_f32 v37, v85, v85\n\t"
      "v_mul_f32 v38, v86, v86\n\t"
      "v_mul_f32 v39, v87, v87\n\t"
      "v_fma_f32 v36, v36, v52, v53\n\t"
      "v_fma_f32 v37, v37, v52, v53\n\t"
      "v_fma_f32 v38, v38, v52, v53\n\t"
      "v_fma_f32 v39, v39, v52, v53\n\t"
      "v_mul_f32 v36, v36, v84\n\t"
      "v_mul_f32 v37, v37, v85\n\t"
      "v_mul_f32 v38, v38, v86\n\t"
      "v_mul_f32 v39, v39, v87\n\t"
      "v_exp_f32 v36, v36\n\t"
      "v_exp_f32 v37, v37\n\t"
      "v_exp_f32 v38, v38\n\t"
      "v_exp_f32 v39, v39\n\t"
      "v_fma_f32 v36, v36, v51, v51\n\t"
      "v_fma_f32 v37, v37, v51, v51\n\t"
      "v_fma_f32 v38, v38, v51, v51\n\t"
      "v_fma_f32 v39, v39, v51, v51\n\t"
      "v_rcp_f32 v36, v36\n\t"
      "v_rcp_f32 v37, v37\n\t"
      "v_rcp_f32 v38, v38\n\t"
      "v_rcp_f32 v39, v39\n\t"
      "v_mul_f32 v36, v36, v84\n\t"
      "v_mul_f32 v37, v37, v85\n\t"
      "v_mul_f32 v38, v38, v86\n\t"
      "v_mul_f32 v39, v39, v87\n\t"
      "v_cvt_pk_bf16_f32 v78, v36, v37\n\t"
      "v_cvt_pk_bf16_f32 v79, v38, v39\n\t"
      "buffer_store_dwordx4 v[76:79], %[l16], %[ws], %[ho] offen offset:3072 sc1\n\t"
      "s_add_u32 %[ho], %[ho], 0xe000\n\t"
      "v_mov_b32 v40, %[tl]\n\t"
      "v_mov_b32 v41, %[th]\n\t"
      "v_mov_b32 v42, %[tl]\n\t"
      "v_mov_b32 v43, %[th]\n\t"
      "v_pk_mul_f32 v[32:33], v[88:89], v[88:89]\n\t"
      "v_pk_mul_f32 v[34:35], v[90:91], v[90:91]\n\t"
      "v_pk_mul_f32 v[36:37], v[92:93], v[92:93]\n\t"
      "v_pk_mul_f32 v[38:39], v[94:95], v[94:95]\n\t"
      "v_pk_fma_f32 v[32:33], v[32:33], v[52:53], v[52:53] op_sel:[0,0,1] op_sel_hi:[1,0,1]\n\t"
      "v_pk_fma_f32 v[34:35], v[34:35], v[52:53], v[52:53] op_sel:[0,0,1] op_sel_hi:[1,0,1]\n\t"
      "v_pk_fma_f32 v[36:37], v[36:37], v[52:53], v[52:53] op_sel:[0,0,1] op_sel_hi:[1,0,1]\n\t"
      "v_pk_fma_f32 v[38:39], v[38:39], v[52:53], v[52:53] op_sel:[0,0,1] op_sel_hi:[1,0,1]\n\t"
      "v_pk_mul_f32 v[32:33], v[32:33], v[88:89]\n\t"
      "v_pk_mul_f32 v[34:35], v[34:35], v[90:91]\n\t"
      "v_pk_mul_f32 v[36:37], v[36:37], v[92:93]\n\t"
      "v_pk_mul_f32 v[38:39], v[38:39], v[94:95]\n\t"
      "v_exp_f32 v32, v32\n\t"
      "v_exp_f32 v33, v33\n\t"
      "v_exp_f32 v34, v34\n\t"
      "v_exp_f32 v35, v35\n\t"
      "v_exp_f32 v36, v36\n\t"
      "v_exp_f32 v37, v37\n\t"
      "v_exp_f32 v38, v38\n\t"
      "v_exp_f32 v39, v39\n\t"
      "v_pk_fma_f32 v[32:33], v[32:33], v[50:51], v[50:51] op_sel:[0,1,1] op_sel_hi:[1,1,1]\n\t"
      "v_pk_fma_f32 v[34:35], v[34:35], v[50:51], v[50:51] op_sel:[0,1,1] op_sel_hi:[1,1,1]\n\t"
      "v_pk_fma_f32 v[36:37], v[36:37], v[50:51], v[50:51] op_sel:[0,1,1] op_sel_hi:[1,1,1]\n\t"
      "v_pk_fma_f32 v[38:39], v[38:39], v[50:51], v[50:51] op_sel:[0,1,1] op_sel_hi:[1,1,1]\n\t"
      "v_rcp_f32 v32, v32\n\t"
      "v_rcp_f32 v33, v33\n\t"
      "v_rcp_f32 v34, v34\n\t"
      "v_rcp_f32 v35, v35\n\t"
      "v_rcp_f32 v36, v36\n\t"
      "v_rcp_f32 v37, v37\n\t"
      "v_rcp_f32 v38, v38\n\t"
      "v_rcp_f32 v39, v39\n\t"
      "v_pk_mul_f32 v[32:33], v[32:33], v[88:89]\n\t"
      "v_pk_mul_f32 v[34:35], v[34:35], v[90:91]\n\t"
      "v_pk_mul_f32 v[36:37], v[36:37], v[92:93]\n\t"
      "v_pk_mul_f32 v[38:39], v[38:39], v[94:95]\n\t"
      "v_cvt_pk_bf16_f32 v88, v32, v33\n\t"
      "v_cvt_pk_bf16_f32 v89, v34, v35\n\t"
      "v_cvt_pk_bf16_f32 v90, v36, v37\n\t"
      "v_cvt_pk_bf16_f32 v91, v38, v39\n\t"
      "buffer_store_dwordx4 v[88:91], %[l16], %[ws], %[ho] offen offset:0 sc1\n\t"
      "v_pk_mul_f32 v[32:33], v[96:97], v[96:97]\n\t"
      "v_pk_mul_f32 v[34:35], v[98:99], v[98:99]\n\t"
      "v_pk_mul_f32 v[36:37], v[100:101], v[100:101]\n\t"
      "v_pk_mul_f32 v[38:39], v[102:103], v[102:103]\n\t"
      "v_pk_fma_f32 v[32:33], v[32:33], v[52:53], v[52:53] op_sel:[0,0,1] op_sel_hi:[1,0,1]\n\t"
      "v_pk_fma_f32 v[34:35], v[34:35], v[52:53], v[52:53] op_sel:[0,0,1] op_sel_hi:[1,0,1]\n\t"
      "v_pk_fma_f32 v[36:37], v[36:37], v[52:53], v[52:53] op_sel:[0,0,1] op_sel_hi:[1,0,1]\n\t"
      "v_pk_fma_f32 v[38:39], v[38:39], v[52:53], v[52:53] op_sel:[0,0,1] op_sel_hi:[1,0,1]\n\t"
      "v_pk_mul_f32 v[32:33], v[32:33], v[96:97]\n\t"
      "v_pk_mul_f32 v[34:35], v[34:35], v[98:99]\n\t"
      "v_pk_mul_f32 v[36:37], v[36:37], v[100:101]\n\t"
      "v_pk_mul_f32 v[38:39], v[38:39], v[102:103]\n\t"
      "v_exp_f32 v32, v32\n\t"
      "v_exp_f32 v33, v33\n\t"
      "v_exp_f32 v34, v34\n\t"
      "v_exp_f32 v35, v35\n\t"
      "v_exp_f32 v36, v36\n\t"
      "v_exp_f32 v37, v37\n\t"
      "v_exp_f32 v38, v38\n\t"
      "v_exp_f32 v39, v39\n\t"
      "v_pk_fma_f32 v[32:33], v[32:33], v[50:51], v[50:51] op_sel:[0,1,1] op_sel_hi:[1,1,1]\n\t"
      "v_pk_fma_f32 v[34:35], v[34:35], v[50:51], v[50:51] op_sel:[0,1,1] op_sel_hi:[1,1,1]\n\t"
      "v_pk_fma_f32 v[36:37], v[36:37], v[50:51], v[50:51] op_sel:[0,1,1] op_sel_hi:[1,1,1]\n\t"
      "v_pk_fma_f32 v[38:39], v[38:39], v[50:51], v[50:51] op_sel:[0,1,1] op_sel_hi:[1,1,1]\n\t"
      "v_rcp_f32 v32, v32\n\t"
      "v_rcp_f32 v33, v33\n\t"
      "v_rcp_f32 v34, v34\n\t"
      "v_rcp_f32 v35, v35\n\t"
      "v_rcp_f32 v36, v36\n\t"
      "v_rcp_f32 v37, v37\n\t"
      "v_rcp_f32 v38, v38\n\t"
      "v_rcp_f32 v39, v39\n\t"
      "v_pk_mul_f32 v[32:33], v[32:33], v[96:97]\n\t"
      "v_pk_mul_f32 v[34:35], v[34:35], v[98:99]\n\t"
      "v_pk_mul_f32 v[36:37], v[36:37], v[100:101]\n\t"
      "v_pk_mul_f32 v[38:39], v[38:39], v[102:103]\n\t"
      "v_cvt_pk_bf16_f32 v92, v32, v33\n\t"
      "v_cvt_pk_bf16_f32 v93, v34, v35\n\t"
      "v_cvt_pk_bf16_f32 v94, v36, v37\n\t"
      "v_cvt_pk_bf16_f32 v95, v38, v39\n\t"
      "buffer_store_dwordx4 v[92:95], %[l16], %[ws], %[ho] offen offset:1024 sc1\n\t"
      "v_pk_mul_f32 v[32:33], v[104:105], v[104:105]\n\t"
      "v_pk_mul_f32 v[34:35], v[106:107], v[106:107]\n\t"
      "v_pk_mul_f32 v[36:37], v[108:109], v[108:109]\n\t"
      "v_pk_mul_f32 v[38:39], v[110:111], v[110:111]\n\t"
      "v_pk_fma_f32 v[32:33], v[32:33], v[52:53], v[52:53] op_sel:[0,0,1] op_sel_hi:[1,0,1]\n\t"
      "v_pk_fma_f32 v[34:35], v[34:35], v[52:53], v[52:53] op_sel:[0,0,1] op_sel_hi:[1,0,1]\n\t"
      "v_pk_fma_f32 v[36:37], v[36:37], v[52:53], v[52:53] op_sel:[0,0,1] op_sel_hi:[1,0,1]\n\t"
      "v_pk_fma_f32 v[38:39], v[38:39], v[52:53], v[52:53] op_sel:[0,0,1] op_sel_hi:[1,0,1]\n\t"
      "v_pk_mul_f32 v[32:33], v[32:33], v[104:105]\n\t"
      "v_pk_mul_f32 v[34:35], v[34:35], v[106:107]\n\t"
      "v_pk_mul_f32 v[36:37], v[36:37], v[108:109]\n\t"
      "v_pk_mul_f32 v[38:39], v[38:39], v[110:111]\n\t"
      "v_exp_f32 v32, v32\n\t"
      "v_exp_f32 v33, v33\n\t"
      "v_exp_f32 v34, v34\n\t"
      "v_exp_f32 v35, v35\n\t"
      "v_exp_f32 v36, v36\n\t"
      "v_exp_f32 v37, v37\n\t"
      "v_exp_f32 v38, v38\n\t"
      "v_exp_f32 v39, v39\n\t"
      "v_pk_fma_f32 v[32:33], v[32:33], v[50:51], v[50:51] op_sel:[0,1,1] op_sel_hi:[1,1,1]\n\t"
      "v_pk_fma_f32 v[34:35], v[34:35], v[50:51], v[50:51] op_sel:[0,1,1] op_sel_hi:[1,1,1]\n\t"
      "v_pk_fma_f32 v[36:37], v[36:37], v[50:51], v[50:51] op_sel:[0,1,1] op_sel_hi:[1,1,1]\n\t"
      "v_pk_fma_f32 v[38:39], v[38:39], v[50:51], v[50:51] op_sel:[0,1,1] op_sel_hi:[1,1,1]\n\t"
      "v_rcp_f32 v32, v32\n\t"
      "v_rcp_f32 v33, v33\n\t"
      "v_rcp_f32 v34, v34\n\t"
      "v_rcp_f32 v35, v35\n\t"
      "v_rcp_f32 v36, v36\n\t"
      "v_rcp_f32 v37, v37\n\t"
      "v_rcp_f32 v38, v38\n\t"
      "v_rcp_f32 v39, v39\n\t"
      "v_pk_mul_f32 v[32:33], v[32:33], v[104:105]\n\t"
      "v_pk_mul_f32 v[34:35], v[34:35], v[106:107]\n\t"
      "v_pk_mul_f32 v[36:37], v[36:37], v[108:109]\n\t"
      "v_pk_mul_f32 v[38:39], v[38:39], v[110:111]\n\t"
      "v_cvt_pk_bf16_f32 v104, v32, v33\n\t"
      "v_cvt_pk_bf16_f32 v105, v34, v35\n\t"
      "v_cvt_pk_bf16_f32 v106, v36, v37\n\t"
      "v_cvt_pk_bf16_f32 v107, v38, v39\n\t"
      "buffer_store_dwordx4 v[104:107], %[l16], %[ws], %[ho] offen offset:2048 sc1\n\t"
      "s_waitcnt vmcnt(3)\n\t"
      "s_barrier\n\t"
      "buffer_store_dwordx4 v[40:43], %[z0], %[ws], %[fo] offen sc1\n\t"
      "v_pk_mul_f32 v[32:33], v[112:113], v[112:113]\n\t"
      "v_pk_mul_f32 v[34:35], v[114:115], v[114:115]\n\t"
      "v_pk_mul_f32 v[36:37], v[116:117], v[116:117]\n\t"
      "v_pk_mul_f32 v[38:39], v[118:119], v[118:119]\n\t"
      "v_pk_fma_f32 v[32:33], v[32:33], v[52:53], v[52:53] op_sel:[0,0,1] op_sel_hi:[1,0,1]\n\t"
      "v_pk_fma_f32 v[34:35], v[34:35], v[52:53], v[52:53] op_sel:[0,0,1] op_sel_hi:[1,0,1]\n\t"
      "v_pk_fma_f32 v[36:37], v[36:37], v[52:53], v[52:53] op_sel:[0,0,1] op_sel_hi:[1,0,1]\n\t"
      "v_pk_fma_f32 v[38:39], v[38:39], v[52:53], v[52:53] op_sel:[0,0,1] op_sel_hi:[1,0,1]\n\t"
      "v_pk_mul_f32 v[32:33], v[32:33], v[112:113]\n\t"
      "v_pk_mul_f32 v[34:35], v[34:35], v[114:115]\n\t"
      "v_pk_mul_f32 v[36:37], v[36:37], v[116:117]\n\t"
      "v_pk_mul_f32 v[38:39], v[38:39], v[118:119]\n\t"
      "v_exp_f32 v32, v32\n\t"
      "v_exp_f32 v33, v33\n\t"
      "v_exp_f32 v34, v34\n\t"
      "v_exp_f32 v35, v35\n\t"
      "v_exp_f32 v36, v36\n\t"
      "v_exp_f32 v37, v37\n\t"
      "v_exp_f32 v38, v38\n\t"
      "v_exp_f32 v39, v39\n\t"
      "v_pk_fma_f32 v[32:33], v[32:33], v[50:51], v[50:51] op_sel:[0,1,1] op_sel_hi:[1,1,1]\n\t"
      "v_pk_fma_f32 v[34:35], v[34:35], v[50:51], v[50:51] op_sel:[0,1,1] op_sel_hi:[1,1,1]\n\t"
      "v_pk_fma_f32 v[36:37], v[36:37], v[50:51], v[50:51] op_sel:[0,1,1] op_sel_hi:[1,1,1]\n\t"
      "v_pk_fma_f32 v[38:39], v[38:39], v[50:51], v[50:51] op_sel:[0,1,1] op_sel_hi:[1,1,1]\n\t"
      "v_rcp_f32 v32, v32\n\t"
      "v_rcp_f32 v33, v33\n\t"
      "v_rcp_f32 v34, v34\n\t"
      "v_rcp_f32 v35, v35\n\t"
      "v_rcp_f32 v36, v36\n\t"
      "v_rcp_f32 v37, v37\n\t"
      "v_rcp_f32 v38, v38\n\t"
      "v_rcp_f32 v39, v39\n\t"
      "v_pk_mul_f32 v[32:33], v[32:33], v[112:113]\n\t"
      "v_pk_mul_f32 v[34:35], v[34:35], v[114:115]\n\t"
      "v_pk_mul_f32 v[36:37], v[36:37], v[116:117]\n\t"
      "v_pk_mul_f32 v[38:39], v[38:39], v[118:119]\n\t"
      "v_cvt_pk_bf16_f32 v108, v32, v33\n\t"
      "v_cvt_pk_bf16_f32 v109, v34, v35\n\t"
      "v_cvt_pk_bf16_f32 v110, v36, v37\n\t"
      "v_cvt_pk_bf16_f32 v111, v38, v39\n\t"
      "buffer_store_dwordx4 v[108:111], %[l16], %[ws], %[ho] offen offset:3072 sc1\n\t"
      : [so] "=&s"(so), [n] "=&s"(n), [ho] "+s"(ho), [wq] "+s"(wq)
      : [l16] "v"(c.lane16), [ws] "s"(c.ws), [b0] "v"(b0), [b1] "v"(b1), [fl] "s"(fl), [m0b] "s"(m0b), [tl] "s"(tl), [th] "s"(th), [z0] "v"(z0), [fo] "s"(fo), [stv] "v"(stv), [sto] "s"(sto), [sto2] "s"(sto2), [so0] "s"(so0)
      : "v32", "v33", "v34", "v35", "v36", "v37", "v38", "v39", "v40", "v41", "v42", "v43", "v44", "v45", "v46", "v47", "v48", "v49", "v50", "v51", "v52", "v53", "v54", "v55", "v56", "v57", "v58", "v59", "v60", "v61", "v62", "v63", "v64", "v65", "v66", "v67", "v68", "v69", "v70", "v71", "v72", "v73", "v74", "v75", "v76", "v77", "v78", "v79", "v80", "v81", "v82", "v83", "v84", "v85", "v86", "v87", "v88", "v89", "v90", "v91", "v92", "v93", "v94", "v95", "v96", "v97", "v98", "v99", "v100", "v101", "v102", "v103", "v104", "v105", "v106", "v107", "v108", "v109", "v110", "v111", "v112", "v113", "v114", "v115", "v116", "v117", "v118", "v119", "v120", "v121", "v122", "v123", "v124", "v125", "v126", "v127", "v128", "v129", "v130", "v131", "v132", "v133", "v134", "v135", "v136", "v137", "v138", "v139", "v140", "v141", "v142", "v143", "v144", "v145", "v146", "v147", "v148", "v149", "v150", "v151", "v152", "v153", "v154", "v155", "v156", "v157", "v158", "v159", "v160", "v161", "v162", "v163", "v164", "v165", "v166", "v167", "v168", "v169", "v170", "v171", "v172", "v173", "v174", "v175", "v176", "v177", "v178", "v179", "v180", "v181", "v182", "v183", "v184", "v185", "v186", "v187", "v188", "v189", "v190", "v191", "v192", "v193", "v194", "v195", "v196", "v197", "v198", "v199", "v200", "v201", "v202", "v203", "v204", "v205", "v206", "v207", "v208", "v209", "v210", "v211", "v212", "v213", "v214", "v215", "v216", "v217", "v218", "v219", "v220", "v221", "v222", "v223", "v224", "v225", "v226", "v227", "v228", "v229", "v230", "v231", "v232", "v233", "v234", "v235", "v236", "v237", "v238", "v239", "v240", "v241", "v242", "v243", "v244", "v245", "v246", "v247", "v248", "v249", "v250", "v251", "v252", "v253", "v254", "v255", "m0", "vcc", "memory", "scc", ACC_FILE);
      }
      refresh(c);
      {
        const unsigned rh = c.c >= 7 ? 1 : 0, c7 = c.c - 7 * rh;
        const unsigned m5 = 2 * rh + (c.w >> 1), n5 = 2 * c7 + (c.w & 1);
        const unsigned ro = U(lb + O_X2F + ((c.r * 4 + m5) * 28 + 2 * n5) * 1024), bv = c.half * 8, bo = U(n5 * 64);
        const rsrc_t rb = __builtin_amdgcn_make_buffer_rsrc((void*)p.b2, 0, DM * 2, 0x00020000);
        const u64 tg = tag_of(c, ph + 3);
        const unsigned tl = (unsigned)tg, th = (unsigned)(tg >> 32), z0 = c.lane < 8 ? c.lane16 : OOB, fo = U(c.w == 0 ? line_of(ph + 3, c.r) + c.c * FS : OOB);
        const unsigned fv = line_of(ph + 3, c.r) + min(c.lane & 15, 13u) * FS;
        unsigned n, s0 = U(lb + O_HF + ((c.r * 4 + (m5 ^ 1)) * 112 + 14 * c.w) * 1024), s1 = U(lb + O_HF + ((c.r * 4 + m5) * 112 + 14 * c.w) * 1024);
        RED_ARGS
        u32x16 rsd;
    asm volatile(
      "buffer_load_dwordx4 v[56:59], %[l16], %[ws], %[ro] offen sc1\n\t"
      "buffer_load_dwordx4 v[60:63], %[l16], %[ws], %[ro] offen offset:1024 sc1\n\t"
      "buffer_load_dwordx2 v[64:65], %[bv], %[rb], %[bo] offen offset:0\n\t"
      "buffer_load_dwordx2 v[66:67], %[bv], %[rb], %[bo] offen offset:16\n\t"
      "buffer_load_dwordx2 v[68:69], %[bv], %[rb], %[bo] offen offset:32\n\t"
      "buffer_load_dwordx2 v[70:71], %[bv], %[rb], %[bo] offen offset:48\n\t"
      : "=&{v[56:71]}"(rsd)
      : [l16] "v"(c.lane16), [ws] "s"(c.ws), [ro] "s"(ro), [bv] "v"(bv), [rb] "s"(rb), [bo] "s"(bo)
      : "memory", ACC_FILE);
        asm volatile("" :: "s"(tl), "s"(th), "v"(z0), "s"(fo), "v"(fv), "s"(s0), "s"(s1), "v"(wr0), "v"(wr1), "v"(wr2), "v"(rd), "v"(c.lane16));
        asm volatile("" : "={v[224:255]}"(warm));
        if (c.w != 1) asm volatile("s_waitcnt vmcnt(%0)" :: "i"(6) : "memory");
        if (c.w == 1) poll<0>(c, warm, line_of(10 + layer, c.r) + (c.lane & 15) * FS, c.lane < 14, tag_of(c, 10 + layer));
        if (layer == 0 && c.w >= 2) {
#pragma unroll
          for (int i = 0; i < 4; ++i) { const unsigned f = (c.w - 2) * 12 + 4 * i; dma4(c.lds0 + L_WQKV + (56 + f) * 1024, c.lane16, c.ws, WS_WQF + (c.c * 84 + f) * 1024); }
        }
        bar();
        u32x8 xp;
    asm volatile(
      "v_mov_b32 v80, %[tl]\n\t"
      "v_mov_b32 v81, %[th]\n\t"
      "v_mov_b32 v82, %[tl]\n\t"
      "v_mov_b32 v83, %[th]\n\t"
      "buffer_store_dwordx4 v[80:83], %[z0], %[ws], %[fo] offen sc1\n\t"
      "buffer_load_dwordx4 v[160:163], %[l16], %[ws], %[s0] offen offset:0 sc1\n\t"
      "buffer_load_dwordx4 v[164:167], %[l16], %[ws], %[s0] offen offset:1024 sc1\n\t"
      "buffer_load_dwordx4 v[168:171], %[l16], %[ws], %[s0] offen offset:2048 sc1\n\t"
      "buffer_load_dwordx4 v[172:175], %[l16], %[ws], %[s0] offen offset:3072 sc1\n\t"
      "s_add_u32 %[s0], %[s0], 0x1000\n\t"
      "buffer_load_dwordx4 v[176:179], %[l16], %[ws], %[s0] offen offset:0 sc1\n\t"
      "buffer_load_dwordx4 v[180:183], %[l16], %[ws], %[s0] offen offset:1024 sc1\n\t"
      "buffer_load_dwordx4 v[184:187], %[l16], %[ws], %[s0] offen offset:2048 sc1\n\t"
      "buffer_load_dwordx4 v[188:191], %[l16], %[ws], %[s0] offen offset:3072 sc1\n\t"
      "s_add_u32 %[s0], %[s0], 0x1000\n\t"
      "buffer_load_dwordx4 v[192:195], %[l16], %[ws], %[s0] offen offset:0 sc1\n\t"
      "buffer_load_dwordx4 v[196:199], %[l16], %[ws], %[s0] offen offset:1024 sc1\n\t"
      "buffer_load_dwordx4 v[200:203], %[l16], %[ws], %[s0] offen offset:2048 sc1\n\t"
      "buffer_load_dwordx4 v[204:207], %[l16], %[ws], %[s0] offen offset:3072 sc1\n\t"
      "s_add_u32 %[s0], %[s0], 0x1000\n\t"
      "s_waitcnt vmcnt(11)\n\t"
      "v_mfma_f32_32x32x16_bf16 v[144:159], a[112:115], v[160:163], 0\n\t"
      "buffer_load_dwordx4 v[208:211], %[l16], %[ws], %[s0] offen offset:0 sc1\n\t"
      "v_mfma_f32_32x32x16_bf16 v[128:143], a[0:3], v[160:163], 0\n\t"
      "buffer_load_dwordx4 v[160:163], %[l16], %[ws], %[s0] offen offset:1024 sc1\n\t"
      "s_add_u32 %[s0], %[s0], 0xb000\n\t"
      "s_waitcnt vmcnt(12)\n\t"
      "v_mfma_f32_32x32x16_bf16 v[144:159], a[116:119], v[164:167], v[144:159]\n\t"
      "v_mfma_f32_32x32x16_bf16 v[128:143], a[4:7], v[164:167], v[128:143]\n\t"
      "buffer_load_dwordx4 v[164:167], %[l16], %[ws], %[s1] offen offset:0 sc1\n\t"
      "s_waitcnt vmcnt(12)\n\t"
      "v_mfma_f32_32x32x16_bf16 v[144:159], a[120:123], v[168:171], v[144:159]\n\t"
      "v_lshlrev_b32 v96, 16, v56\n\t"
      "v_and_b32 v97, 0xffff0000, v56\n\t"
      "v_lshlrev_b32 v74, 16, v64\n\t"
      "v_mfma_f32_32x32x16_bf16 v[128:143], a[8:11], v[168:171], v[128:143]\n\t"
      "buffer_load_dwordx4 v[168:171], %[l16], %[ws], %[s1] offen offset:1024 sc1\n\t"
      "v_and_b32 v75, 0xffff0000, v64\n\t"
      "v_add_f32 v96, v96, v74\n\t"
      "v_add_f32 v97, v97, v75\n\t"
      "s_waitcnt vmcnt(12)\n\t"
      "v_mfma_f32_32x32x16_bf16 v[144:159], a[124:127], v[172:175], v[144:159]\n\t"
      "v_lshlrev_b32 v98, 16, v57\n\t"
      "v_and_b32 v99, 0xffff0000, v57\n\t"
      "v_lshlrev_b32 v74, 16, v65\n\t"
      "v_mfma_f32_32x32x16_bf16 v[128:143], a[12:15], v[172:175], v[128:143]\n\t"
      "buffer_load_dwordx4 v[172:175], %[l16], %[ws], %[s1] offen offset:2048 sc1\n\t"
      "v_and_b32 v75, 0xffff0000, v65\n\t"
      "v_add_f32 v98, v98, v74\n\t"
      "v_add_f32 v99, v99, v75\n\t"
      "s_waitcnt vmcnt(12)\n\t"
      "v_mfma_f32_32x32x16_bf16 v[144:159], a[128:131], v[176:179], v[144:159]\n\t"
      "v_lshlrev_b32 v100, 16, v58\n\t"
      "v_and_b32 v101, 0xffff0000, v58\n\t"
      "v_lshlrev_b32 v74, 16, v66\n\t"
      "v_mfma_f32_32x32x16_bf16 v[128:143], a[16:19], v[176:179], v[128:143]\n\t"
      "buffer_load_dwordx4 v[176:179], %[l16], %[ws], %[s1] offen offset:3072 sc1\n\t"
      "s_add_u32 %[s1], %[s1], 0x1000\n\t"
      "v_and_b32 v75, 0xffff0000, v66\n\t"
      "v_add_f32 v100, v100, v74\n\t"
      "v_add_f32 v101, v101, v75\n\t"
      "s_waitcnt vmcnt(12)\n\t"
      "v_mfma_f32_32x32x16_bf16 v[144:159], a[132:135], v[180:183], v[144:159]\n\t"
      "v_lshlrev_b32 v102, 16, v59\n\t"
      "v_and_b32 v103, 0xffff0000, v59\n\t"
      "v_lshlrev_b32 v74, 16, v67\n\t"
      "v_mfma_f32_32x32x16_bf16 v[128:143], a[20:23], v[180:183], v[128:143]\n\t"
      "buffer_load_dwordx4 v[180:183], %[l16], %[ws], %[s1] offen offset:0 sc1\n\t"
      "v_and_b32 v75, 0xffff0000, v67\n\t"
      "v_add_f32 v102, v102, v74\n\t"
      "v_add_f32 v103, v103, v75\n\t"
      "s_waitcnt vmcnt(12)\n\t"
      "buffer_load_dwordx2 v[76:77], %[fv], %[ws], 0 offen sc1\n\t"
      "v_mfma_f32_32x32x16_bf16 v[144:159], a[136:139], v[184:187], v[144:159]\n\t"
      "v_lshlrev_b32 v104, 16, v60\n\t"
      "v_and_b32 v105, 0xffff0000, v60\n\t"
      "v_lshlrev_b32 v74, 16, v68\n\t"
      "v_mfma_f32_32x32x16_bf16 v[128:143], a[24:27], v[184:187], v[128:143]\n\t"
      "buffer_load_dwordx4 v[184:187], %[l16], %[ws], %[s1] offen offset:1024 sc1\n\t"
      "v_and_b32 v75, 0xffff0000, v68\n\t"
      "v_add_f32 v104, v104, v74\n\t"
      "v_add_f32 v105, v105, v75\n\t"
      "s_waitcnt vmcnt(13)\n\t"
      "v_mfma_f32_32x32x16_bf16 v[144:159], a[140:143], v[188:191], v[144:159]\n\t"
      "v_lshlrev_b32 v106, 16, v61\n\t"
      "v_and_b32 v107, 0xffff0000, v61\n\t"
      "v_lshlrev_b32 v74, 16, v69\n\t"
      "v_mfma_f32_32x32x16_bf16 v[128:143], a[28:31], v[188:191], v[128:143]\n\t"
      "buffer_load_dwordx4 v[188:191], %[l16], %[ws], %[s1] offen offset:2048 sc1\n\t"
      "v_and_b32 v75, 0xffff0000, v69\n\t"
      "v_add_f32 v106, v106, v74\n\t"
      "v_add_f32 v107, v107, v75\n\t"
      "s_waitcnt vmcnt(13)\n\t"
      "v_mfma_f32_32x32x16_bf16 v[144:159], a[144:147], v[192:195], v[144:159]\n\t"
      "v_lshlrev_b32 v108, 16, v62\n\t"
      "v_and_b32 v109, 0xffff0000, v62\n\t"
      "v_lshlrev_b32 v74, 16, v70\n\t"
      "v_mfma_f32_32x32x16_bf16 v[128:143], a[32:35], v[192:195], v[128:143]\n\t"
      "buffer_load_dwordx4 v[192:195], %[l16], %[ws], %[s1] offen offset:3072 sc1\n\t"
      "s_add_u32 %[s1], %[s1], 0x1000\n\t"
      "v_and_b32 v75, 0xffff0000, v70\n\t"
      "v_add_f32 v108, v108, v74\n\t"
      "v_add_f32 v109, v109, v75\n\t"
      "s_waitcnt vmcnt(13)\n\t"
      "v_mfma_f32_32x32x16_bf16 v[144:159], a[148:151], v[196:199], v[144:159]\n\t"
      "v_lshlrev_b32 v110, 16, v63\n\t"
      "v_and_b32 v111, 0xffff0000, v63\n\t"
      "v_lshlrev_b32 v74, 16, v71\n\t"
      "v_mfma_f32_32x32x16_bf16 v[128:143], a[36:39], v[196:199], v[128:143]\n\t"
      "buffer_load_dwordx4 v[196:199], %[l16], %[ws], %[s1] offen offset:0 sc1\n\t"
      "v_and_b32 v75, 0xffff0000, v71\n\t"
      "v_add_f32 v110, v110, v74\n\t"
      "v_add_f32 v111, v111, v75\n\t"
      "s_waitcnt vmcnt(13)\n\t"
      "v_mfma_f32_32x32x16_bf16 v[144:159], a[152:155], v[200:203], v[144:159]\n\t"
      "v_mfma_f32_32x32x16_bf16 v[128:143], a[40:43], v[200:203], v[128:143]\n\t"
      "buffer_load_dwordx4 v[200:203], %[l16], %[ws], %[s1] offen offset:1024 sc1\n\t"
      "s_waitcnt vmcnt(13)\n\t"
      "v_mfma_f32_32x32x16_bf16 v[144:159], a[156:159], v[204:207], v[144:159]\n\t"
      "v_mfma_f32_32x32x16_bf16 v[128:143], a[44:47], v[204:207], v[128:143]\n\t"
      "buffer_load_dwordx4 v[204:207], %[l16], %[ws], %[s1] offen offset:2048 sc1\n\t"
      "s_waitcnt vmcnt(13)\n\t"
      "v_mfma_f32_32x32x16_bf16 v[144:159], a[160:163], v[208:211], v[144:159]\n\t"
      "v_mfma_f32_32x32x16_bf16 v[128:143], a[48:51], v[208:211], v[128:143]\n\t"
      "buffer_load_dwordx4 v[208:211], %[l16], %[ws], %[s1] offen offset:3072 sc1\n\t"
      "s_add_u32 %[s1], %[s1], 0x1000\n\t"
      "s_waitcnt vmcnt(13)\n\t"
      "v_mfma_f32_32x32x16_bf16 v[144:159], a[164:167], v[160:163], v[144:159]\n\t"
      "v_mfma_f32_32x32x16_bf16 v[128:143], a[52:55], v[160:163], v[128:143]\n\t"
      "buffer_load_dwordx4 v[160:163], %[l16], %[ws], %[s1] offen offset:0 sc1\n\t"
      "s_waitcnt vmcnt(13)\n\t"
      "v_mfma_f32_32x32x16_bf16 v[112:127], a[112:115], v[164:167], 0\n\t"
      "v_mfma_f32_32x32x16_bf16 v[96:111], a[0:3], v[164:167], v[96:111]\n\t"
      "buffer_load_dwordx4 v[164:167], %[l16], %[ws], %[s1] offen offset:1024 sc1\n\t"
      "s_add_u32 %[s1], %[s1], 0xb000\n\t"
      "s_waitcnt vmcnt(13)\n\t"
      "v_mfma_f32_32x32x16_bf16 v[112:127], a[116:119], v[168:171], v[112:127]\n\t"
      "v_mfma_f32_32x32x16_bf16 v[96:111], a[4:7], v[168:171], v[96:111]\n\t"
      "s_waitcnt vmcnt(12)\n\t"
      "s_waitcnt vmcnt(9)\n\t"
      "v_xor_b32 v76, %[tl], v76\n\t"
      "v_xor_b32 v77, %[th], v77\n\t"
      "v_lshrrev_b32 v76, 16, v76\n\t"
      "v_or_b32 v76, v76, v77\n\t"
      "v_cmp_ne_u32 vcc, 0, v76\n\t"
      "s_cbranch_vccz 2f\n\t"
      "s_mov_b32 %[n], 0x100000\n\t"
      "1:\n\t"
      "buffer_load_dwordx2 v[76:77], %[fv], %[ws], 0 offen sc1\n\t"
      "s_waitcnt vmcnt(0)\n\t"
      "v_xor_b32 v76, %[tl], v76\n\t"
      "v_xor_b32 v77, %[th], v77\n\t"
      "v_lshrrev_b32 v76, 16, v76\n\t"
      "v_or_b32 v76, v76, v77\n\t"
      "v_cmp_ne_u32 vcc, 0, v76\n\t"
      "s_cbranch_vccz 2f\n\t"
      "s_sleep 1\n\t"
      "s_sub_u32 %[n], %[n], 1\n\t"
      "s_cmp_lg_u32 %[n], 0\n\t"
      "s_cbranch_scc1 1b\n\t"
      "2:\n\t"
      "v_mfma_f32_32x32x16_bf16 v[112:127], a[120:123], v[172:175], v[112:127]\n\t"
      "buffer_load_dwordx4 v[168:171], %[l16], %[ws], %[s0] offen offset:0 sc1\n\t"
      "v_mfma_f32_32x32x16_bf16 v[96:111], a[8:11], v[172:175], v[96:111]\n\t"
      "buffer_load_dwordx4 v[172:175], %[l16], %[ws], %[s0] offen offset:1024 sc1\n\t"
      "v_mfma_f32_32x32x16_bf16 v[112:127], a[124:127], v[176:179], v[112:127]\n\t"
      "v_mfma_f32_32x32x16_bf16 v[96:111], a[12:15], v[176:179], v[96:111]\n\t"
      "buffer_load_dwordx4 v[176:179], %[l16], %[ws], %[s0] offen offset:2048 sc1\n\t"
      "v_mfma_f32_32x32x16_bf16 v[112:127], a[128:131], v[180:183], v[112:127]\n\t"
      "v_mfma_f32_32x32x16_bf16 v[96:111], a[16:19], v[180:183], v[96:111]\n\t"
      "buffer_load_dwordx4 v[180:183], %[l16], %[ws], %[s0] offen offset:3072 sc1\n\t"
      "s_add_u32 %[s0], %[s0], 0x1000\n\t"
      "s_waitcnt vmcnt(12)\n\t"
      "v_mfma_f32_32x32x16_bf16 v[112:127], a[132:135], v[184:187], v[112:127]\n\t"
      "v_mfma_f32_32x32x16_bf16 v[96:111], a[20:23], v[184:187], v[96:111]\n\t"
      "buffer_load_dwordx4 v[184:187], %[l16], %[ws], %[s0] offen offset:0 sc1\n\t"
      "s_waitcnt vmcnt(12)\n\t"
      "v_mfma_f32_32x32x16_bf16 v[112:127], a[136:139], v[188:191], v[112:127]\n\t"
      "v_mfma_f32_32x32x16_bf16 v[96:111], a[24:27], v[188:191], v[96:111]\n\t"
      "buffer_load_dwordx4 v[188:191], %[l16], %[ws], %[s0] offen offset:1024 sc1\n\t"
      "s_waitcnt vmcnt(12)\n\t"
      "v_mfma_f32_32x32x16_bf16 v[112:127], a[140:143], v[192:195], v[112:127]\n\t"
      "v_mfma_f32_32x32x16_bf16 v[96:111], a[28:31], v[192:195], v[96:111]\n\t"
      "buffer_load_dwordx4 v[192:195], %[l16], %[ws], %[s0] offen offset:2048 sc1\n\t"
      "s_waitcnt vmcnt(12)\n\t"
      "v_mfma_f32_32x32x16_bf16 v[112:127], a[144:147], v[196:199], v[112:127]\n\t"
      "v_mfma_f32_32x32x16_bf16 v[96:111], a[32:35], v[196:199], v[96:111]\n\t"
      "buffer_load_dwordx4 v[196:199], %[l16], %[ws], %[s0] offen offset:3072 sc1\n\t"
      "s_add_u32 %[s0], %[s0], 0x1000\n\t"
      "s_waitcnt vmcnt(12)\n\t"
      "v_mfma_f32_32x32x16_bf16 v[112:127], a[148:151], v[200:203], v[112:127]\n\t"
      "v_mfma_f32_32x32x16_bf16 v[96:111], a[36:39], v[200:203], v[96:111]\n\t"
      "buffer_load_dwordx4 v[200:203], %[l16], %[ws], %[s0] offen offset:0 sc1\n\t"
      "s_waitcnt vmcnt(12)\n\t"
      "v_mfma_f32_32x32x16_bf16 v[112:127], a[152:155], v[204:207], v[112:127]\n\t"
      "v_mfma_f32_32x32x16_bf16 v[96:111], a[40:43], v[204:207], v[96:111]\n\t"
      "buffer_load_dwordx4 v[204:207], %[l16], %[ws], %[s0] offen offset:1024 sc1\n\t"
      "s_waitcnt vmcnt(12)\n\t"
      "v_mfma_f32_32x32x16_bf16 v[112:127], a[156:159], v[208:211], v[112:127]\n\t"
      "v_mfma_f32_32x32x16_bf16 v[96:111], a[44:47], v[208:211], v[96:111]\n\t"
      "buffer_load_dwordx4 v[208:211], %[l16], %[ws], %[s0] offen offset:2048 sc1\n\t"
      "s_waitcnt vmcnt(12)\n\t"
      "v_mfma_f32_32x32x16_bf16 v[112:127], a[160:163], v[160:163], v[112:127]\n\t"
      "v_mfma_f32_32x32x16_bf16 v[96:111], a[48:51], v[160:163], v[96:111]\n\t"
      "buffer_load_dwordx4 v[160:163], %[l16], %[ws], %[s0] offen offset:3072 sc1\n\t"
      "s_add_u32 %[s0], %[s0], 0x1000\n\t"
      "s_waitcnt vmcnt(12)\n\t"
      "v_mfma_f32_32x32x16_bf16 v[112:127], a[164:167], v[164:167], v[112:127]\n\t"
      "v_mfma_f32_32x32x16_bf16 v[96:111], a[52:55], v[164:167], v[96:111]\n\t"
      "buffer_load_dwordx4 v[164:167], %[l16], %[ws], %[s0] offen offset:0 sc1\n\t"
      "s_waitcnt vmcnt(12)\n\t"
      "v_mfma_f32_32x32x16_bf16 v[144:159], a[168:171], v[168:171], v[144:159]\n\t"
      "v_mfma_f32_32x32x16_bf16 v[128:143], a[56:59], v[168:171], v[128:143]\n\t"
      "buffer_load_dwordx4 v[168:171], %[l16], %[ws], %[s0] offen offset:1024 sc1\n\t"
      "s_add_u32 %[s0], %[s0], 0xb000\n\t"
      "s_waitcnt vmcnt(12)\n\t"
      "v_mfma_f32_32x32x16_bf16 v[144:159], a[172:175], v[172:175], v[144:159]\n\t"
      "v_mfma_f32_32x32x16_bf16 v[128:143], a[60:63], v[172:175], v[128:143]\n\t"
      "buffer_load_dwordx4 v[172:175], %[l16], %[ws], %[s1] offen offset:0 sc1\n\t"
      "s_waitcnt vmcnt(12)\n\t"
      "v_mfma_f32_32x32x16_bf16 v[144:159], a[176:179], v[176:179], v[144:159]\n\t"
      "v_mfma_f32_32x32x16_bf16 v[128:143], a[64:67], v[176:179], v[128:143]\n\t"
      "buffer_load_dwordx4 v[176:179], %[l16], %[ws], %[s1] offen offset:1024 sc1\n\t"
      "s_waitcnt vmcnt(12)\n\t"
      "v_mfma_f32_32x32x16_bf16 v[144:159], a[180:183], v[180:183], v[144:159]\n\t"
      "v_mfma_f32_32x32x16_bf16 v[128:143], a[68:71], v[180:183], v[128:143]\n\t"
      "buffer_load_dwordx4 v[180:183], %[l16], %[ws], %[s1] offen offset:2048 sc1\n\t"
      "s_waitcnt vmcnt(12)\n\t"
      "v_mfma_f32_32x32x16_bf16 v[144:159], a[184:187], v[184:187], v[144:159]\n\t"
      "v_mfma_f32_32x32x16_bf16 v[128:143], a[72:75], v[184:187], v[128:143]\n\t"
      "buffer_load_dwordx4 v[184:187], %[l16], %[ws], %[s1] offen offset:3072 sc1\n\t"
      "s_add_u32 %[s1], %[s1], 0x1000\n\t"
      "s_waitcnt vmcnt(12)\n\t"
      "v_mfma_f32_32x32x16_bf16 v[144:159], a[188:191], v[188:191], v[144:159]\n\t"
      "v_mfma_f32_32x32x16_bf16 v[128:143], a[76:79], v[188:191], v[128:143]\n\t"
      "buffer_load_dwordx4 v[188:191], %[l16], %[ws], %[s1] offen offset:0 sc1\n\t"
      "s_waitcnt vmcnt(12)\n\t"
      "v_mfma_f32_32x32x16_bf16 v[144:159], a[192:195], v[192:195], v[144:159]\n\t"
      "v_mfma_f32_32x32x16_bf16 v[128:143], a[80:83], v[192:195], v[128:143]\n\t"
      "buffer_load_dwordx4 v[192:195], %[l16], %[ws], %[s1] offen offset:1024 sc1\n\t"
      "s_waitcnt vmcnt(12)\n\t"
      "v_mfma_f32_32x32x16_bf16 v[144:159], a[196:199], v[196:199], v[144:159]\n\t"
      "v_mfma_f32_32x32x16_bf16 v[128:143], a[84:87], v[196:199], v[128:143]\n\t"
      "buffer_load_dwordx4 v[196:199], %[l16], %[ws], %[s1] offen offset:2048 sc1\n\t"
      "s_waitcnt vmcnt(12)\n\t"
      "v_mfma_f32_32x32x16_bf16 v[144:159], a[200:203], v[200:203], v[144:159]\n\t"
      "v_mfma_f32_32x32x16_bf16 v[128:143], a[88:91], v[200:203], v[128:143]\n\t"
      "buffer_load_dwordx4 v[200:203], %[l16], %[ws], %[s1] offen offset:3072 sc1\n\t"
      "s_add_u32 %[s1], %[s1], 0x1000\n\t"
      "s_waitcnt vmcnt(12)\n\t"
      "v_mfma_f32_32x32x16_bf16 v[144:159], a[204:207], v[204:207], v[144:159]\n\t"
      "v_mfma_f32_32x32x16_bf16 v[128:143], a[92:95], v[204:207], v[128:143]\n\t"
      "buffer_load_dwordx4 v[204:207], %[l16], %[ws], %[s1] offen offset:0 sc1\n\t"
      "s_waitcnt vmcnt(12)\n\t"
      "v_mfma_f32_32x32x16_bf16 v[144:159], a[208:211], v[208:211], v[144:159]\n\t"
      "v_mfma_f32_32x32x16_bf16 v[128:143], a[96:99], v[208:211], v[128:143]\n\t"
      "buffer_load_dwordx4 v[208:211], %[l16], %[ws], %[s1] offen offset:1024 sc1\n\t"
      "s_waitcnt vmcnt(12)\n\t"
      "v_mfma_f32_32x32x16_bf16 v[144:159], a[212:215], v[160:163], v[144:159]\n\t"
      "v_mfma_f32_32x32x16_bf16 v[128:143], a[100:103], v[160:163], v[128:143]\n\t"
      "buffer_load_dwordx4 v[160:163], %[l16], %[ws], %[s1] offen offset:2048 sc1\n\t"
      "s_waitcnt vmcnt(12)\n\t"
      "v_mfma_f32_32x32x16_bf16 v[144:159], a[216:219], v[164:167], v[144:159]\n\t"
      "v_mfma_f32_32x32x16_bf16 v[128:143], a[104:107], v[164:167], v[128:143]\n\t"
      "buffer_load_dwordx4 v[164:167], %[l16], %[ws], %[s1] offen offset:3072 sc1\n\t"
      "s_add_u32 %[s1], %[s1], 0x1000\n\t"
      "s_waitcnt vmcnt(12)\n\t"
      "v_mfma_f32_32x32x16_bf16 v[144:159], a[220:223], v[168:171], v[144:159]\n\t"
      "v_mfma_f32_32x32x16_bf16 v[128:143], a[108:111], v[168:171], v[128:143]\n\t"
      "buffer_load_dwordx4 v[168:171], %[l16], %[ws], %[s1] offen offset:0 sc1\n\t"
      "s_waitcnt vmcnt(12)\n\t"
      "v_mfma_f32_32x32x16_bf16 v[112:127], a[168:171], v[172:175], v[112:127]\n\t"
      "v_mfma_f32_32x32x16_bf16 v[96:111], a[56:59], v[172:175], v[96:111]\n\t"
      "buffer_load_dwordx4 v[172:175], %[l16], %[ws], %[s1] offen offset:1024 sc1\n\t"
      "s_add_u32 %[s1], %[s1], 0xb000\n\t"
      "s_waitcnt vmcnt(12)\n\t"
      "v_mfma_f32_32x32x16_bf16 v[112:127], a[172:175], v[176:179], v[112:127]\n\t"
      "v_mfma_f32_32x32x16_bf16 v[96:111], a[60:63], v[176:179], v[96:111]\n\t"
      "s_waitcnt vmcnt(11)\n\t"
      "v_mfma_f32_32x32x16_bf16 v[112:127], a[176:179], v[180:183], v[112:127]\n\t"
      "ds_write_b128 %[wr1], v[128:131] offset:0\n\t"
      "v_mfma_f32_32x32x16_bf16 v[96:111], a[64:67], v[180:183], v[96:111]\n\t"
      "s_waitcnt vmcnt(10)\n\t"
      "v_mfma_f32_32x32x16_bf16 v[112:127], a[180:183], v[184:187], v[112:127]\n\t"
      "ds_write_b128 %[wr1], v[132:135] offset:1024\n\t"
      "v_mfma_f32_32x32x16_bf16 v[96:111], a[68:71], v[184:187], v[96:111]\n\t"
      "s_waitcnt vmcnt(9)\n\t"
      "v_mfma_f32_32x32x16_bf16 v[112:127], a[184:187], v[188:191], v[112:127]\n\t"
      "ds_write_b128 %[wr1], v[136:139] offset:2048\n\t"
      "v_mfma_f32_32x32x16_bf16 v[96:111], a[72:75], v[188:191], v[96:111]\n\t"
      "s_waitcnt vmcnt(8)\n\t"
      "v_mfma_f32_32x32x16_bf16 v[112:127], a[188:191], v[192:195], v[112:127]\n\t"
      "ds_write_b128 %[wr1], v[140:143] offset:3072\n\t"
      "v_mfma_f32_32x32x16_bf16 v[96:111], a[76:79], v[192:195], v[96:111]\n\t"
      "s_waitcnt vmcnt(7)\n\t"
      "v_mfma_f32_32x32x16_bf16 v[112:127], a[192:195], v[196:199], v[112:127]\n\t"
      "ds_write_b128 %[wr2], v[144:147] offset:0\n\t"
      "v_mfma_f32_32x32x16_bf16 v[96:111], a[80:83], v[196:199], v[96:111]\n\t"
      "s_waitcnt vmcnt(6)\n\t"
      "v_mfma_f32_32x32x16_bf16 v[112:127], a[196:199], v[200:203], v[112:127]\n\t"
      "ds_write_b128 %[wr2], v[148:151] offset:1024\n\t"
      "v_mfma_f32_32x32x16_bf16 v[96:111], a[84:87], v[200:203], v[96:111]\n\t"
      "s_waitcnt vmcnt(5)\n\t"
      "v_mfma_f32_32x32x16_bf16 v[112:127], a[200:203], v[204:207], v[112:127]\n\t"
      "ds_write_b128 %[wr2], v[152:155] offset:2048\n\t"
      "v_mfma_f32_32x32x16_bf16 v[96:111], a[88:91], v[204:207], v[96:111]\n\t"
      "s_waitcnt vmcnt(4)\n\t"
      "v_mfma_f32_32x32x16_bf16 v[112:127], a[204:207], v[208:211], v[112:127]\n\t"
      "ds_write_b128 %[wr2], v[156:159] offset:3072\n\t"
      "v_mfma_f32_32x32x16_bf16 v[96:111], a[92:95], v[208:211], v[96:111]\n\t"
      "s_waitcnt vmcnt(3)\n\t"
      "v_mfma_f32_32x32x16_bf16 v[112:127], a[208:211], v[160:163], v[112:127]\n\t"
      "v_mfma_f32_32x32x16_bf16 v[96:111], a[96:99], v[160:163], v[96:111]\n\t"
      "s_waitcnt vmcnt(2)\n\t"
      "v_mfma_f32_32x32x16_bf16 v[112:127], a[212:215], v[164:167], v[112:127]\n\t"
      "v_mfma_f32_32x32x16_bf16 v[96:111], a[100:103], v[164:167], v[96:111]\n\t"
      "s_waitcnt vmcnt(1)\n\t"
      "v_mfma_f32_32x32x16_bf16 v[112:127], a[216:219], v[168:171], v[112:127]\n\t"
      "v_mfma_f32_32x32x16_bf16 v[96:111], a[104:107], v[168:171], v[96:111]\n\t"
      "s_waitcnt vmcnt(0)\n\t"
      "v_mfma_f32_32x32x16_bf16 v[112:127], a[220:223], v[172:175], v[112:127]\n\t"
      "v_mfma_f32_32x32x16_bf16 v[96:111], a[108:111], v[172:175], v[96:111]\n\t"
      "s_nop 10\n\t"
      "ds_write_b128 %[wr0], v[112:115] offset:0\n\t"
      "ds_write_b128 %[wr0], v[116:119] offset:1024\n\t"
      "ds_write_b128 %[wr0], v[120:123] offset:2048\n\t"
      "ds_write_b128 %[wr0], v[124:127] offset:3072\n\t"
      "s_waitcnt lgkmcnt(0)\n\t"
      "s_barrier\n\t"
      "ds_read_b128 v[160:163], %[rd] offset:0\n\t"
      "ds_read_b128 v[164:167], %[rd] offset:1024\n\t"
      "ds_read_b128 v[168:171], %[rd] offset:2048\n\t"
      "ds_read_b128 v[172:175], %[rd] offset:3072\n\t"
      "ds_read_b128 v[176:179], %[rd] offset:4096\n\t"
      "ds_read_b128 v[180:183], %[rd] offset:5120\n\t"
      "ds_read_b128 v[184:187], %[rd] offset:6144\n\t"
      "ds_read_b128 v[188:191], %[rd] offset:7168\n\t"
      "ds_read_b128 v[192:195], %[rd] offset:8192\n\t"
      "ds_read_b128 v[196:199], %[rd] offset:9216\n\t"
      "ds_read_b128 v[200:203], %[rd] offset:10240\n\t"
      "ds_read_b128 v[204:207], %[rd] offset:11264\n\t"
      "s_waitcnt lgkmcnt(11)\n\t"
      "v_pk_add_f32 v[96:97], v[96:97], v[160:161]\n\t"
      "v_pk_add_f32 v[98:99], v[98:99], v[162:163]\n\t"
      "s_waitcnt lgkmcnt(10)\n\t"
      "v_pk_add_f32 v[100:101], v[100:101], v[164:165]\n\t"
      "v_pk_add_f32 v[102:103], v[102:103], v[166:167]\n\t"
      "s_waitcnt lgkmcnt(9)\n\t"
      "v_pk_add_f32 v[104:105], v[104:105], v[168:169]\n\t"
      "v_pk_add_f32 v[106:107], v[106:107], v[170:171]\n\t"
      "s_waitcnt lgkmcnt(8)\n\t"
      "v_pk_add_f32 v[108:109], v[108:109], v[172:173]\n\t"
      "v_pk_add_f32 v[110:111], v[110:111], v[174:175]\n\t"
      "s_waitcnt lgkmcnt(7)\n\t"
      "v_pk_add_f32 v[96:97], v[96:97], v[176:177]\n\t"
      "v_pk_add_f32 v[98:99], v[98:99], v[178:179]\n\t"
      "s_waitcnt lgkmcnt(6)\n\t"
      "v_pk_add_f32 v[100:101], v[100:101], v[180:181]\n\t"
      "v_pk_add_f32 v[102:103], v[102:103], v[182:183]\n\t"
      "s_waitcnt lgkmcnt(5)\n\t"
      "v_pk_add_f32 v[104:105], v[104:105], v[184:185]\n\t"
      "v_pk_add_f32 v[106:107], v[106:107], v[186:187]\n\t"
      "s_waitcnt lgkmcnt(4)\n\t"
      "v_pk_add_f32 v[108:109], v[108:109], v[188:189]\n\t"
      "v_pk_add_f32 v[110:111], v[110:111], v[190:191]\n\t"
      "s_waitcnt lgkmcnt(3)\n\t"
      "v_pk_add_f32 v[96:97], v[96:97], v[192:193]\n\t"
      "v_pk_add_f32 v[98:99], v[98:99], v[194:195]\n\t"
      "s_waitcnt lgkmcnt(2)\n\t"
      "v_pk_add_f32 v[100:101], v[100:101], v[196:197]\n\t"
      "v_pk_add_f32 v[102:103], v[102:103], v[198:199]\n\t"
      "s_waitcnt lgkmcnt(1)\n\t"
      "v_pk_add_f32 v[104:105], v[104:105], v[200:201]\n\t"
      "v_pk_add_f32 v[106:107], v[106:107], v[202:203]\n\t"
      "s_waitcnt lgkmcnt(0)\n\t"
      "v_pk_add_f32 v[108:109], v[108:109], v[204:205]\n\t"
      "v_pk_add_f32 v[110:111], v[110:111], v[206:207]\n\t"
      "v_cvt_pk_bf16_f32 v112, v96, v97\n\t"
      "v_cvt_pk_bf16_f32 v113, v98, v99\n\t"
      "v_cvt_pk_bf16_f32 v114, v100, v101\n\t"
      "v_cvt_pk_bf16_f32 v115, v102, v103\n\t"
      "v_cvt_pk_bf16_f32 v116, v104, v105\n\t"
      "v_cvt_pk_bf16_f32 v117, v106, v107\n\t"
      "v_cvt_pk_bf16_f32 v118, v108, v109\n\t"
      "v_cvt_pk_bf16_f32 v119, v110, v111\n\t"
      : "=&{v[112:119]}"(xp), [s0] "+s"(s0), [s1] "+s"(s1), [n] "=&s"(n)
      : [l16] "v"(c.lane16), [ws] "s"(c.ws), "{v[56:71]}"(rsd), [tl] "s"(tl), [th] "s"(th), [z0] "v"(z0), [fo] "s"(fo), [fv] "v"(fv), [wr0] "v"(wr0), [wr1] "v"(wr1), [wr2] "v"(wr2), [rd] "v"(rd)
      : "v72", "v73", "v74", "v75", "v76", "v77", "v78", "v79", "v80", "v81", "v82", "v83", "v84", "v85", "v86", "v87", "v88", "v89", "v90", "v91", "v92", "v93", "v94", "v95", "v96", "v97", "v98", "v99", "v100", "v101", "v102", "v103", "v104", "v105", "v106", "v107", "v108", "v109", "v110", "v111", "v120", "v121", "v122", "v123", "v124", "v125", "v126", "v127", "v128", "v129", "v130", "v131", "v132", "v133", "v134", "v135", "v136", "v137", "v138", "v139", "v140", "v141", "v142", "v143", "v144", "v145", "v146", "v147", "v148", "v149", "v150", "v151", "v152", "v153", "v154", "v155", "v156", "v157", "v158", "v159", "v160", "v161", "v162", "v163", "v164", "v165", "v166", "v167", "v168", "v169", "v170", "v171", "v172", "v173", "v174", "v175", "v176", "v177", "v178", "v179", "v180", "v181", "v182", "v183", "v184", "v185", "v186", "v187", "v188", "v189", "v190", "v191", "v192", "v193", "v194", "v195", "v196", "v197", "v198", "v199", "v200", "v201", "v202", "v203", "v204", "v205", "v206", "v207", "v208", "v209", "v210", "v211", "v212", "v213", "v214", "v215", "v216", "v217", "v218", "v219", "v220", "v221", "v222", "v223", "v224", "v225", "v226", "v227", "v228", "v229", "v230", "v231", "v232", "v233", "v234", "v235", "v236", "v237", "v238", "v239", "v240", "v241", "v242", "v243", "v244", "v245", "v246", "v247", "v248", "v249", "v250", "v251", "v252", "v253", "v254", "v255", "m0", "vcc", "memory", "scc", ACC_FILE);
        if (layer == 0) {
          const unsigned xo = U(lb + O_XF + ((c.r * 4 + m5) * 28 + 2 * n5) * 1024);
          const unsigned stw = c.half ? OOB : c.l31 * 8, sto = U(lb + O_ST + ((c.r * 14 + n5) * 128 + m5 * 32) * 8);
    asm volatile(
      "v_mov_b32 v120, 0\n\t"
      "v_mov_b32 v121, 0\n\t"
      "v_mov_b32 v122, 0\n\t"
      "v_mov_b32 v123, 0\n\t"
      "buffer_store_dwordx4 v[112:115], %[l16], %[ws], %[xo] offen sc1\n\t"
      "buffer_store_dwordx4 v[116:119], %[l16], %[ws], %[xo] offen offset:1024 sc1\n\t"
      "v_dot2c_f32_bf16 v120, 0x3f803f80, v112\n\t"
      "v_dot2c_f32_bf16 v121, v112, v112\n\t"
      "v_dot2c_f32_bf16 v122, 0x3f803f80, v113\n\t"
      "v_dot2c_f32_bf16 v123, v113, v113\n\t"
      "v_dot2c_f32_bf16 v120, 0x3f803f80, v114\n\t"
      "v_dot2c_f32_bf16 v121, v114, v114\n\t"
      "v_dot2c_f32_bf16 v122, 0x3f803f80, v115\n\t"
      "v_dot2c_f32_bf16 v123, v115, v115\n\t"
      "v_dot2c_f32_bf16 v120, 0x3f803f80, v116\n\t"
      "v_dot2c_f32_bf16 v121, v116, v116\n\t"
      "v_dot2c_f32_bf16 v122, 0x3f803f80, v117\n\t"
      "v_dot2c_f32_bf16 v123, v117, v117\n\t"
      "v_dot2c_f32_bf16 v120, 0x3f803f80, v118\n\t"
      "v_dot2c_f32_bf16 v121, v118, v118\n\t"
      "v_dot2c_f32_bf16 v122, 0x3f803f80, v119\n\t"
      "v_dot2c_f32_bf16 v123, v119, v119\n\t"
      "s_nop 2\n\t"
      "v_pk_add_f32 v[120:121], v[120:121], v[122:123]\n\t"
      "s_nop 0\n\t"
      "v_mov_b32 v122, v120\n\t"
      "v_mov_b32 v123, v121\n\t"
      "s_nop 0\n\t"
      "v_permlane32_swap_b32 v120, v122\n\t"
      "v_permlane32_swap_b32 v121, v123\n\t"
      "v_pk_add_f32 v[120:121], v[120:121], v[122:123]\n\t"
      "buffer_store_dwordx2 v[120:121], %[stw], %[ws], %[sto] offen sc1\n\t"
      :
      : "{v[112:119]}"(xp), [l16] "v"(c.lane16), [ws] "s"(c.ws), [xo] "s"(xo), [stw] "v"(stw), [sto] "s"(sto)
      : "v120", "v121", "v122", "v123", "memory", ACC_FILE);
        } else {
          const unsigned tb = c.lds0 + 90112 + (c.w >> 1) * 32 * 144;
#pragma unroll
          for (int g4 = 0; g4 < 4; ++g4) *(LAS u32x2*)(tb + c.l31 * 144 + (c.w & 1) * 64 + g4 * 16 + c.half * 8) = u32x2{xp[2 * g4], xp[2 * g4 + 1]};
          bar();
          const rsrc_t ro = __builtin_amdgcn_make_buffer_rsrc(p.out, 0, SEQ * DM * 2, 0x00020000);
#pragma unroll
          for (int s2 = 0; s2 < 2; ++s2) {
            const unsigned row = (c.w & 1) * 16 + s2 * 8 + (c.lane >> 3);
            const u32x4 v = *(LAS u32x4*)(tb + row * 144 + (c.lane & 7) * 16);
            __builtin_amdgcn_raw_buffer_store_b128(v, ro, row * (DM * 2) + (c.lane & 7) * 16, U((c.r * 128 + m5 * 32) * (DM * 2) + c7 * 128), 0);
          }
        }
      }
    } else if (layer == 0) {
      if (c.w != 1) {
        const unsigned f0 = ((c.w + 1) >> 1) * 28;
#pragma unroll
        for (int i = 0; i < 7; ++i) dma4(c.lds0 + L_WQKV + ((f0 + 56) % 84 + 4 * i) * 1024, c.lane16, c.ws, WS_WQF + (c.c * 84 + f0 + 4 * i) * 1024);
      }
      asm volatile("buffer_load_dwordx4 a[224:227], %0, %1, %2 offen sc1\n\tbuffer_load_dwordx4 a[228:231], %0, %1, %2 offen offset:1024 sc1\n\tbuffer_load_dwordx4 a[232:235], %0, %1, %2 offen offset:2048 sc1"
                   :: "v"(c.lane16), "s"(c.ws), "s"(U(WS_WEQ + c.c * 3072)) : "memory");
      drain();
      if (c.w == 1) {
        for (unsigned spins = 0; spins < (1u << 20); ++spins) {
          u32x2 v;
          asm volatile("s_sleep 16\n\tbuffer_load_dwordx2 %0, %1, %2, 0 offen sc1\n\ts_waitcnt vmcnt(0)" : "=&v"(v) : "v"(line_of(3, c.r)), "s"(c.ws) : "memory");
          if (__all(v[1] == (unsigned)(tag_of(c, 3) >> 32) && (v[0] >> 16) == ((unsigned)tag_of(c, 3) >> 16))) break;
        }
        __builtin_amdgcn_s_sleep(64);
        asm volatile("" : "={v[224:255]}"(warm));
        poll<0>(c, warm, line_of(12, c.r) + (c.lane & 15) * FS, c.lane < 16, tag_of(c, 12));
      }
      bar();
    }
    if (layer == 0) {
      refresh(c);
      {
        const unsigned so0 = U(lb + O_XF + (c.r * 4 + c.w) * 28 * 1024);
        unsigned so, n, f;
        const unsigned b0 = c.lds0 + L_WQKV + c.lane16, b1 = b0 + 2 * TILEB;
        const unsigned stv = (c.half * 7 * 128 + c.l31) * 8, sto = U(lb + O_ST + (c.r * 14 * 128 + c.w * 32) * 8);
        QKV_ARGS(WS_L0 + LSTRIDE, c.c, c.r, L_MISC)
        const unsigned sto2 = U(sto + 4096);
    asm volatile(
      "s_mov_b32 %[f], 1\n\t"
      "s_mov_b32 %[n], 0x40000\n\t"
      "s_sleep 10\n\t"
      "8:\n\t"
      "s_mov_b32 %[so], %[so0]\n\t"
      "buffer_load_dwordx2 v[32:33], %[stv], %[ws], %[sto] offen offset:0 sc1\n\t"
      "buffer_load_dwordx2 v[34:35], %[stv], %[ws], %[sto] offen offset:1024 sc1\n\t"
      "buffer_load_dwordx2 v[36:37], %[stv], %[ws], %[sto] offen offset:2048 sc1\n\t"
      "buffer_load_dwordx2 v[38:39], %[stv], %[ws], %[sto] offen offset:3072 sc1\n\t"
      "buffer_load_dwordx2 v[40:41], %[stv], %[ws], %[sto2] offen offset:0 sc1\n\t"
      "buffer_load_dwordx2 v[42:43], %[stv], %[ws], %[sto2] offen offset:1024 sc1\n\t"
      "buffer_load_dwordx2 v[44:45], %[stv], %[ws], %[sto2] offen offset:2048 sc1\n\t"
      "buffer_load_dwordx4 v[144:147], %[l16], %[ws], %[so] offen offset:0 sc1\n\t"
      "buffer_load_dwordx4 v[148:151], %[l16], %[ws], %[so] offen offset:1024 sc1\n\t"
      "buffer_load_dwordx4 v[152:155], %[l16], %[ws], %[so] offen offset:2048 sc1\n\t"
      "buffer_load_dwordx4 v[156:159], %[l16], %[ws], %[so] offen offset:3072 sc1\n\t"
      "s_add_u32 %[so], %[so], 0x1000\n\t"
      "buffer_load_dwordx4 v[160:163], %[l16], %[ws], %[so] offen offset:0 sc1\n\t"
      "buffer_load_dwordx4 v[164:167], %[l16], %[ws], %[so] offen offset:1024 sc1\n\t"
      "buffer_load_dwordx4 v[168:171], %[l16], %[ws], %[so] offen offset:2048 sc1\n\t"
      "buffer_load_dwordx4 v[172:175], %[l16], %[ws], %[so] offen offset:3072 sc1\n\t"
      "s_add_u32 %[so], %[so], 0x1000\n\t"
      "v_mov_b32 v54, 0\n\t"
      "s_sub_u32 %[n], %[n], 1\n\t"
      "s_waitcnt vmcnt(8)\n\t"
      "v_max3_u32 v54, v32, v33, v54\n\t"
      "v_max3_u32 v54, v34, v35, v54\n\t"
      "v_max3_u32 v54, v36, v37, v54\n\t"
      "v_max3_u32 v54, v38, v39, v54\n\t"
      "v_max3_u32 v54, v40, v41, v54\n\t"
      "v_max3_u32 v54, v42, v43, v54\n\t"
      "v_max3_u32 v54, v44, v45, v54\n\t"
      "v_cmp_eq_u32 vcc, -1, v54\n\t"
      "s_cbranch_vccz 7f\n\t"
      "s_cmp_gt_i32 %[n], 0\n\t"
      "s_cbranch_scc1 8b\n\t"
      "7:\n\t"
      "buffer_load_dwordx4 v[176:179], %[l16], %[ws], %[so] offen offset:0 sc1\n\t"
      "buffer_load_dwordx4 v[180:183], %[l16], %[ws], %[so] offen offset:1024 sc1\n\t"
      "buffer_load_dwordx4 v[184:187], %[l16], %[ws], %[so] offen offset:2048 sc1\n\t"
      "buffer_load_dwordx4 v[188:191], %[l16], %[ws], %[so] offen offset:3072 sc1\n\t"
      "s_add_u32 %[so], %[so], 0x1000\n\t"
      "s_cmp_eq_u32 %[f], 0\n\t"
      "s_cbranch_scc1 5f\n\t"
      "s_mov_b32 %[f], 0\n\t"
      "s_waitcnt lgkmcnt(0)\n\t"
      "s_barrier\n\t"
      "5:\n\t"
      "ds_read_b128 v[120:123], %[b0] offset:0\n\t"
      "ds_read_b128 v[124:127], %[b0] offset:28672\n\t"
      "ds_read_b128 v[128:131], %[b0] offset:1024\n\t"
      "ds_read_b128 v[132:135], %[b0] offset:29696\n\t"
      "ds_read_b128 v[136:139], %[b0] offset:2048\n\t"
      "ds_read_b128 v[140:143], %[b0] offset:30720\n\t"
      "s_waitcnt vmcnt(11)\n\t"
      "s_waitcnt lgkmcnt(5)\n\t"
      "v_mfma_f32_32x32x16_bf16 v[72:87], v[120:123], v[144:147], 0\n\t"
      "buffer_load_dwordx4 v[192:195], %[l16], %[ws], %[so] offen offset:0 sc1\n\t"
      "ds_read_b128 v[120:123], %[b0] offset:3072\n\t"
      "v_add_f32 v32, v32, v34\n\t"
      "v_add_f32 v33, v33, v35\n\t"
      "s_waitcnt lgkmcnt(5)\n\t"
      "v_mfma_f32_32x32x16_bf16 v[88:103], v[144:147], v[124:127], 0\n\t"
      "ds_read_b128 v[124:127], %[b0] offset:31744\n\t"
      "v_max3_u32 v54, v144, v145, v54\n\t"
      "v_max3_u32 v54, v146, v147, v54\n\t"
      "s_waitcnt vmcnt(11)\n\t"
      "s_waitcnt lgkmcnt(5)\n\t"
      "v_mfma_f32_32x32x16_bf16 v[72:87], v[128:131], v[148:151], v[72:87]\n\t"
      "buffer_load_dwordx4 v[196:199], %[l16], %[ws], %[so] offen offset:1024 sc1\n\t"
      "ds_read_b128 v[128:131], %[b0] offset:4096\n\t"
      "v_add_f32 v36, v36, v38\n\t"
      "v_add_f32 v37, v37, v39\n\t"
      "s_waitcnt lgkmcnt(5)\n\t"
      "v_mfma_f32_32x32x16_bf16 v[88:103], v[148:151], v[132:135], v[88:103]\n\t"
      "ds_read_b128 v[132:135], %[b0] offset:32768\n\t"
      "v_max3_u32 v54, v148, v149, v54\n\t"
      "v_max3_u32 v54, v150, v151, v54\n\t"
      "s_waitcnt vmcnt(11)\n\t"
      "s_waitcnt lgkmcnt(5)\n\t"
      "v_mfma_f32_32x32x16_bf16 v[72:87], v[136:139], v[152:155], v[72:87]\n\t"
      "buffer_load_dwordx4 v[200:203], %[l16], %[ws], %[so] offen offset:2048 sc1\n\t"
      "ds_read_b128 v[136:139], %[b0] offset:5120\n\t"
      "v_add_f32 v40, v40, v42\n\t"
      "v_add_f32 v41, v41, v43\n\t"
      "s_waitcnt lgkmcnt(5)\n\t"
      "v_mfma_f32_32x32x16_bf16 v[88:103], v[152:155], v[140:143], v[88:103]\n\t"
      "ds_read_b128 v[140:143], %[b0] offset:33792\n\t"
      "v_max3_u32 v54, v152, v153, v54\n\t"
      "v_max3_u32 v54, v154, v155, v54\n\t"
      "s_waitcnt vmcnt(11)\n\t"
      "s_waitcnt lgkmcnt(5)\n\t"
      "v_mfma_f32_32x32x16_bf16 v[72:87], v[120:123], v[156:159], v[72:87]\n\t"
      "buffer_load_dwordx4 v[204:207], %[l16], %[ws], %[so] offen offset:3072 sc1\n\t"
      "s_add_u32 %[so], %[so], 0x1000\n\t"
      "ds_read_b128 v[120:123], %[b0] offset:6144\n\t"
      "v_add_f32 v32, v32, v36\n\t"
      "v_add_f32 v33, v33, v37\n\t"
      "s_waitcnt lgkmcnt(5)\n\t"
      "v_mfma_f32_32x32x16_bf16 v[88:103], v[156:159], v[124:127], v[88:103]\n\t"
      "ds_read_b128 v[124:127], %[b0] offset:34816\n\t"
      "v_max3_u32 v54, v156, v157, v54\n\t"
      "v_max3_u32 v54, v158, v159, v54\n\t"
      "s_waitcnt vmcnt(11)\n\t"
      "s_waitcnt lgkmcnt(5)\n\t"
      "v_mfma_f32_32x32x16_bf16 v[72:87], v[128:131], v[160:163], v[72:87]\n\t"
      "buffer_load_dwordx4 v[208:211], %[l16], %[ws], %[so] offen offset:0 sc1\n\t"
      "ds_read_b128 v[128:131], %[b0] offset:7168\n\t"
      "v_add_f32 v40, v40, v44\n\t"
      "v_add_f32 v41, v41, v45\n\t"
      "s_waitcnt lgkmcnt(5)\n\t"
      "v_mfma_f32_32x32x16_bf16 v[88:103], v[160:163], v[132:135], v[88:103]\n\t"
      "ds_read_b128 v[132:135], %[b0] offset:35840\n\t"
      "v_max3_u32 v54, v160, v161, v54\n\t"
      "v_max3_u32 v54, v162, v163, v54\n\t"
      "s_waitcnt vmcnt(11)\n\t"
      "s_waitcnt lgkmcnt(5)\n\t"
      "v_mfma_f32_32x32x16_bf16 v[72:87], v[136:139], v[164:167], v[72:87]\n\t"
      "buffer_load_dwordx4 v[212:215], %[l16], %[ws], %[so] offen offset:1024 sc1\n\t"
      "ds_read_b128 v[136:139], %[b0] offset:8192\n\t"
      "v_add_f32 v32, v32, v40\n\t"
      "v_add_f32 v33, v33, v41\n\t"
      "s_waitcnt lgkmcnt(5)\n\t"
      "v_mfma_f32_32x32x16_bf16 v[88:103], v[164:167], v[140:143], v[88:103]\n\t"
      "ds_read_b128 v[140:143], %[b0] offset:36864\n\t"
      "v_max3_u32 v54, v164, v165, v54\n\t"
      "v_max3_u32 v54, v166, v167, v54\n\t"
      "s_waitcnt vmcnt(11)\n\t"
      "s_waitcnt lgkmcnt(5)\n\t"
      "v_mfma_f32_32x32x16_bf16 v[72:87], v[120:123], v[168:171], v[72:87]\n\t"
      "buffer_load_dwordx4 v[216:219], %[l16], %[ws], %[so] offen offset:2048 sc1\n\t"
      "ds_read_b128 v[120:123], %[b0] offset:9216\n\t"
      "v_mov_b32 v34, v32\n\t"
      "v_mov_b32 v35, v33\n\t"
      "s_waitcnt lgkmcnt(5)\n\t"
      "v_mfma_f32_32x32x16_bf16 v[88:103], v[168:171], v[124:127], v[88:103]\n\t"
      "ds_read_b128 v[124:127], %[b0] offset:37888\n\t"
      "v_max3_u32 v54, v168, v169, v54\n\t"
      "v_max3_u32 v54, v170, v171, v54\n\t"
      "s_waitcnt vmcnt(11)\n\t"
      "s_waitcnt lgkmcnt(5)\n\t"
      "v_mfma_f32_32x32x16_bf16 v[72:87], v[128:131], v[172:175], v[72:87]\n\t"
      "buffer_load_dwordx4 v[220:223], %[l16], %[ws], %[so] offen offset:3072 sc1\n\t"
      "s_add_u32 %[so], %[so], 0x1000\n\t"
      "ds_read_b128 v[128:131], %[b0] offset:10240\n\t"
      "s_nop 0\n\t"
      "v_permlane32_swap_b32 v32, v34\n\t"
      "s_waitcnt lgkmcnt(5)\n\t"
      "v_mfma_f32_32x32x16_bf16 v[88:103], v[172:175], v[132:135], v[88:103]\n\t"
      "ds_read_b128 v[132:135], %[b0] offset:38912\n\t"
      "v_max3_u32 v54, v172, v173, v54\n\t"
      "v_max3_u32 v54, v174, v175, v54\n\t"
      "s_waitcnt vmcnt(11)\n\t"
      "s_waitcnt lgkmcnt(5)\n\t"
      "v_mfma_f32_32x32x16_bf16 v[72:87], v[136:139], v[176:179], v[72:87]\n\t"
      "buffer_load_dwordx4 v[224:227], %[l16], %[ws], %[so] offen offset:0 sc1\n\t"
      "ds_read_b128 v[136:139], %[b0] offset:11264\n\t"
      "v_permlane32_swap_b32 v33, v35\n\t"
      "v_add_f32 v32, v32, v34\n\t"
      "s_waitcnt lgkmcnt(5)\n\t"
      "v_mfma_f32_32x32x16_bf16 v[88:103], v[176:179], v[140:143], v[88:103]\n\t"
      "ds_read_b128 v[140:143], %[b0] offset:39936\n\t"
      "v_max3_u32 v54, v176, v177, v54\n\t"
      "v_max3_u32 v54, v178, v179, v54\n\t"
      "s_waitcnt vmcnt(11)\n\t"
      "s_waitcnt lgkmcnt(5)\n\t"
      "v_mfma_f32_32x32x16_bf16 v[72:87], v[120:123], v[180:183], v[72:87]\n\t"
      "buffer_load_dwordx4 v[228:231], %[l16], %[ws], %[so] offen offset:1024 sc1\n\t"
      "ds_read_b128 v[120:123], %[b0] offset:12288\n\t"
      "v_add_f32 v33, v33, v35\n\t"
      "v_mul_f32 v32, 0xbb124925, v32\n\t"
      "s_waitcnt lgkmcnt(5)\n\t"
      "v_mfma_f32_32x32x16_bf16 v[88:103], v[180:183], v[124:127], v[88:103]\n\t"
      "ds_read_b128 v[124:127], %[b0] offset:40960\n\t"
      "v_max3_u32 v54, v180, v181, v54\n\t"
      "v_max3_u32 v54, v182, v183, v54\n\t"
      "s_waitcnt vmcnt(11)\n\t"
      "s_waitcnt lgkmcnt(5)\n\t"
      "v_mfma_f32_32x32x16_bf16 v[72:87], v[128:131], v[184:187], v[72:87]\n\t"
      "buffer_load_dwordx4 v[232:235], %[l16], %[ws], %[so] offen offset:2048 sc1\n\t"
      "ds_read_b128 v[128:131], %[b0] offset:13312\n\t"
      "v_mul_f32 v34, v32, -v32\n\t"
      "v_fmamk_f32 v33, v33, 0x3b124925, v34\n\t"
      "s_waitcnt lgkmcnt(5)\n\t"
      "v_mfma_f32_32x32x16_bf16 v[88:103], v[184:187], v[132:135], v[88:103]\n\t"
      "ds_read_b128 v[132:135], %[b0] offset:41984\n\t"
      "v_max3_u32 v54, v184, v185, v54\n\t"
      "v_max3_u32 v54, v186, v187, v54\n\t"
      "s_waitcnt vmcnt(11)\n\t"
      "s_waitcnt lgkmcnt(5)\n\t"
      "v_mfma_f32_32x32x16_bf16 v[72:87], v[136:139], v[188:191], v[72:87]\n\t"
      "buffer_load_dwordx4 v[236:239], %[l16], %[ws], %[so] offen offset:3072 sc1\n\t"
      "s_add_u32 %[so], %[so], 0x1000\n\t"
      "ds_read_b128 v[136:139], %[b0] offset:14336\n\t"
      "v_max_f32 v33, 0, v33\n\t"
      "v_add_f32 v33, 0x3727c5ac, v33\n\t"
      "s_waitcnt lgkmcnt(5)\n\t"
      "v_mfma_f32_32x32x16_bf16 v[88:103], v[188:191], v[140:143], v[88:103]\n\t"
      "ds_read_b128 v[140:143], %[b0] offset:43008\n\t"
      "v_max3_u32 v54, v188, v189, v54\n\t"
      "v_max3_u32 v54, v190, v191, v54\n\t"
      "s_waitcnt vmcnt(11)\n\t"
      "s_waitcnt lgkmcnt(5)\n\t"
      "v_mfma_f32_32x32x16_bf16 v[72:87], v[120:123], v[192:195], v[72:87]\n\t"
      "buffer_load_dwordx4 v[240:243], %[l16], %[ws], %[so] offen offset:0 sc1\n\t"
      "ds_read_b128 v[120:123], %[b0] offset:15360\n\t"
      "v_rsq_f32 v50, v33\n\t"
      "s_waitcnt lgkmcnt(5)\n\t"
      "v_mfma_f32_32x32x16_bf16 v[88:103], v[192:195], v[124:127], v[88:103]\n\t"
      "ds_read_b128 v[124:127], %[b0] offset:44032\n\t"
      "v_max3_u32 v54, v192, v193, v54\n\t"
      "v_max3_u32 v54, v194, v195, v54\n\t"
      "s_waitcnt vmcnt(11)\n\t"
      "s_waitcnt lgkmcnt(5)\n\t"
      "v_mfma_f32_32x32x16_bf16 v[72:87], v[128:131], v[196:199], v[72:87]\n\t"
      "buffer_load_dwordx4 v[244:247], %[l16], %[ws], %[so] offen offset:1024 sc1\n\t"
      "ds_read_b128 v[128:131], %[b0] offset:16384\n\t"
      "s_nop 0\n\t"
      "v_mul_f32 v51, v33, v50\n\t"
      "s_waitcnt lgkmcnt(5)\n\t"
      "v_mfma_f32_32x32x16_bf16 v[88:103], v[196:199], v[132:135], v[88:103]\n\t"
      "ds_read_b128 v[132:135], %[b0] offset:45056\n\t"
      "v_max3_u32 v54, v196, v197, v54\n\t"
      "v_max3_u32 v54, v198, v199, v54\n\t"
      "s_waitcnt vmcnt(11)\n\t"
      "s_waitcnt lgkmcnt(5)\n\t"
      "v_mfma_f32_32x32x16_bf16 v[72:87], v[136:139], v[200:203], v[72:87]\n\t"
      "buffer_load_dwordx4 v[248:251], %[l16], %[ws], %[so] offen offset:2048 sc1\n\t"
      "ds_read_b128 v[136:139], %[b0] offset:17408\n\t"
      "v_cvt_pk_bf16_f32 v46, v32, v51\n\t"
      "v_lshlrev_b32 v34, 16, v46\n\t"
      "s_waitcnt lgkmcnt(5)\n\t"
      "v_mfma_f32_32x32x16_bf16 v[88:103], v[200:203], v[140:143], v[88:103]\n\t"
      "ds_read_b128 v[140:143], %[b0] offset:46080\n\t"
      "v_max3_u32 v54, v200, v201, v54\n\t"
      "v_max3_u32 v54, v202, v203, v54\n\t"
      "s_waitcnt vmcnt(11)\n\t"
      "s_waitcnt lgkmcnt(5)\n\t"
      "v_mfma_f32_32x32x16_bf16 v[72:87], v[120:123], v[204:207], v[72:87]\n\t"
      "buffer_load_dwordx4 v[252:255], %[l16], %[ws], %[so] offen offset:3072 sc1\n\t"
      "s_add_u32 %[so], %[so], 0x1000\n\t"
      "ds_read_b128 v[120:123], %[b0] offset:18432\n\t"
      "v_and_b32 v35, 0xffff0000, v46\n\t"
      "v_sub_f32 v34, v32, v34\n\t"
      "s_waitcnt lgkmcnt(5)\n\t"
      "v_mfma_f32_32x32x16_bf16 v[88:103], v[204:207], v[124:127], v[88:103]\n\t"
      "ds_read_b128 v[124:127], %[b0] offset:47104\n\t"
      "v_max3_u32 v54, v204, v205, v54\n\t"
      "v_max3_u32 v54, v206, v207, v54\n\t"
      "s_waitcnt vmcnt(11)\n\t"
      "s_waitcnt lgkmcnt(5)\n\t"
      "v_mfma_f32_32x32x16_bf16 v[72:87], v[128:131], v[208:211], v[72:87]\n\t"
      "ds_read_b128 v[128:131], %[b0] offset:19456\n\t"
      "v_sub_f32 v35, v51, v35\n\t"
      "v_cvt_pk_bf16_f32 v47, v34, v35\n\t"
      "s_waitcnt lgkmcnt(5)\n\t"
      "v_mfma_f32_32x32x16_bf16 v[88:103], v[208:211], v[132:135], v[88:103]\n\t"
      "ds_read_b128 v[132:135], %[b0] offset:48128\n\t"
      "v_max3_u32 v54, v208, v209, v54\n\t"
      "v_max3_u32 v54, v210, v211, v54\n\t"
      "s_waitcnt vmcnt(10)\n\t"
      "s_waitcnt lgkmcnt(5)\n\t"
      "v_mfma_f32_32x32x16_bf16 v[72:87], v[136:139], v[212:215], v[72:87]\n\t"
      "ds_read_b128 v[136:139], %[b0] offset:20480\n\t"
      "v_mov_b32 v48, v46\n\t"
      "v_mov_b32 v49, v46\n\t"
      "s_waitcnt lgkmcnt(5)\n\t"
      "v_mfma_f32_32x32x16_bf16 v[88:103], v[212:215], v[140:143], v[88:103]\n\t"
      "ds_read_b128 v[140:143], %[b0] offset:49152\n\t"
      "v_max3_u32 v54, v212, v213, v54\n\t"
      "v_max3_u32 v54, v214, v215, v54\n\t"
      "s_waitcnt vmcnt(9)\n\t"
      "s_waitcnt lgkmcnt(5)\n\t"
      "v_mfma_f32_32x32x16_bf16 v[72:87], v[120:123], v[216:219], v[72:87]\n\t"
      "ds_read_b128 v[120:123], %[b0] offset:21504\n\t"
      "s_waitcnt lgkmcnt(5)\n\t"
      "v_mfma_f32_32x32x16_bf16 v[88:103], v[216:219], v[124:127], v[88:103]\n\t"
      "ds_read_b128 v[124:127], %[b0] offset:50176\n\t"
      "v_max3_u32 v54, v216, v217, v54\n\t"
      "v_max3_u32 v54, v218, v219, v54\n\t"
      "s_waitcnt vmcnt(8)\n\t"
      "s_waitcnt lgkmcnt(5)\n\t"
      "v_mfma_f32_32x32x16_bf16 v[72:87], v[128:131], v[220:223], v[72:87]\n\t"
      "ds_read_b128 v[128:131], %[b0] offset:22528\n\t"
      "s_waitcnt lgkmcnt(5)\n\t"
      "v_mfma_f32_32x32x16_bf16 v[88:103], v[220:223], v[132:135], v[88:103]\n\t"
      "ds_read_b128 v[132:135], %[b0] offset:51200\n\t"
      "v_max3_u32 v54, v220, v221, v54\n\t"
      "v_max3_u32 v54, v222, v223, v54\n\t"
      "s_waitcnt vmcnt(7)\n\t"
      "s_waitcnt lgkmcnt(5)\n\t"
      "v_mfma_f32_32x32x16_bf16 v[72:87], v[136:139], v[224:227], v[72:87]\n\t"
      "ds_read_b128 v[136:139], %[b0] offset:23552\n\t"
      "s_waitcnt lgkmcnt(5)\n\t"
      "v_mfma_f32_32x32x16_bf16 v[88:103], v[224:227], v[140:143], v[88:103]\n\t"
      "ds_read_b128 v[140:143], %[b0] offset:52224\n\t"
      "v_max3_u32 v54, v224, v225, v54\n\t"
      "v_max3_u32 v54, v226, v227, v54\n\t"
      "s_waitcnt vmcnt(6)\n\t"
      "s_waitcnt lgkmcnt(5)\n\t"
      "v_mfma_f32_32x32x16_bf16 v[72:87], v[120:123], v[228:231], v[72:87]\n\t"
      "ds_read_b128 v[120:123], %[b0] offset:24576\n\t"
      "s_waitcnt lgkmcnt(5)\n\t"
      "v_mfma_f32_32x32x16_bf16 v[88:103], v[228:231], v[124:127], v[88:103]\n\t"
      "ds_read_b128 v[124:127], %[b0] offset:53248\n\t"
      "v_max3_u32 v54, v228, v229, v54\n\t"
      "v_max3_u32 v54, v230, v231, v54\n\t"
      "s_waitcnt vmcnt(5)\n\t"
      "s_waitcnt lgkmcnt(5)\n\t"
      "v_mfma_f32_32x32x16_bf16 v[72:87], v[128:131], v[232:235], v[72:87]\n\t"
      "ds_read_b128 v[128:131], %[b0] offset:25600\n\t"
      "ds_write_b32 %[rtw], v50\n\t"
      "v_mul_f32 v52, 0x3e8b97f5, v50\n\t"
      "ds_read_b128 v[104:107], %[rtr] offset:0\n\t"
      "ds_read_b128 v[108:111], %[rtr] offset:32\n\t"
      "ds_read_b128 v[112:115], %[rtr] offset:64\n\t"
      "ds_read_b128 v[116:119], %[rtr] offset:96\n\t"
      "s_waitcnt lgkmcnt(10)\n\t"
      "v_mfma_f32_32x32x16_bf16 v[88:103], v[232:235], v[132:135], v[88:103]\n\t"
      "ds_read_b128 v[132:135], %[b0] offset:54272\n\t"
      "v_max3_u32 v54, v232, v233, v54\n\t"
      "v_max3_u32 v54, v234, v235, v54\n\t"
      "s_waitcnt vmcnt(4)\n\t"
      "s_waitcnt lgkmcnt(10)\n\t"
      "v_mfma_f32_32x32x16_bf16 v[72:87], v[136:139], v[236:239], v[72:87]\n\t"
      "ds_read_b128 v[136:139], %[b0] offset:26624\n\t"
      "s_waitcnt lgkmcnt(10)\n\t"
      "v_mfma_f32_32x32x16_bf16 v[88:103], v[236:239], v[140:143], v[88:103]\n\t"
      "ds_read_b128 v[140:143], %[b0] offset:55296\n\t"
      "v_max3_u32 v54, v236, v237, v54\n\t"
      "v_max3_u32 v54, v238, v239, v54\n\t"
      "s_waitcnt vmcnt(3)\n\t"
      "s_waitcnt lgkmcnt(10)\n\t"
      "v_mfma_f32_32x32x16_bf16 v[72:87], v[120:123], v[240:243], v[72:87]\n\t"
      "ds_read_b128 v[120:123], %[b0] offset:27648\n\t"
      "s_waitcnt lgkmcnt(10)\n\t"
      "v_mfma_f32_32x32x16_bf16 v[88:103], v[240:243], v[124:127], v[88:103]\n\t"
      "ds_read_b128 v[124:127], %[b0] offset:56320\n\t"
      "v_max3_u32 v54, v240, v241, v54\n\t"
      "v_max3_u32 v54, v242, v243, v54\n\t"
      "s_waitcnt vmcnt(2)\n\t"
      "s_waitcnt lgkmcnt(10)\n\t"
      "v_mfma_f32_32x32x16_bf16 v[72:87], v[128:131], v[244:247], v[72:87]\n\t"
      "ds_read_b128 v[128:131], %[b1] offset:0\n\t"
      "s_waitcnt lgkmcnt(5)\n\t"
      "v_mfma_f32_32x32x16_bf16 v[88:103], v[244:247], v[132:135], v[88:103]\n\t"
      "ds_read_b128 v[132:135], %[b1] offset:1024\n\t"
      "v_max3_u32 v54, v244, v245, v54\n\t"
      "v_max3_u32 v54, v246, v247, v54\n\t"
      "s_waitcnt vmcnt(1)\n\t"
      "s_waitcnt lgkmcnt(5)\n\t"
      "v_mfma_f32_32x32x16_bf16 v[72:87], v[136:139], v[248:251], v[72:87]\n\t"
      "ds_read_b128 v[136:139], %[b1] offset:2048\n\t"
      "s_waitcnt lgkmcnt(5)\n\t"
      "v_mfma_f32_32x32x16_bf16 v[88:103], v[248:251], v[140:143], v[88:103]\n\t"
      "ds_read_b128 v[140:143], %[b1] offset:3072\n\t"
      "v_max3_u32 v54, v248, v249, v54\n\t"
      "v_max3_u32 v54, v250, v251, v54\n\t"
      "s_waitcnt vmcnt(0)\n\t"
      "s_waitcnt lgkmcnt(5)\n\t"
      "v_mfma_f32_32x32x16_bf16 v[72:87], v[120:123], v[252:255], v[72:87]\n\t"
      "ds_read_b128 v[120:123], %[b1] offset:4096\n\t"
      "s_waitcnt lgkmcnt(5)\n\t"
      "v_mfma_f32_32x32x16_bf16 v[88:103], v[252:255], v[124:127], v[88:103]\n\t"
      "ds_read_b128 v[124:127], %[b1] offset:5120\n\t"
      "v_max3_u32 v54, v252, v253, v54\n\t"
      "v_max3_u32 v54, v254, v255, v54\n\t"
      "v_cmp_eq_u32 vcc, -1, v54\n\t"
      "s_cbranch_vccz 6f\n\t"
      "s_waitcnt vmcnt(0) lgkmcnt(0)\n\t"
      "s_sub_u32 %[n], %[n], 16\n\t"
      "s_cmp_gt_i32 %[n], 0\n\t"
      "s_cbranch_scc1 8b\n\t"
      "6:\n\t"
      "v_mfma_f32_32x32x16_bf16 v[72:87], a[228:231], v[46:49], v[72:87]\n\t"
      "v_mfma_f32_32x32x16_bf16 v[88:103], v[46:49], a[232:235], v[88:103]\n\t"
      "s_waitcnt lgkmcnt(4)\n\t"
      "v_mfma_f32_32x32x16_bf16 v[56:71], v[128:131], v[144:147], 0\n\t"
      "ds_read_b128 v[128:131], %[b1] offset:6144\n\t"
      "v_mfma_f32_32x32x16_bf16 v[56:71], v[132:135], v[148:151], v[56:71]\n\t"
      "ds_read_b128 v[132:135], %[b1] offset:7168\n\t"
      "s_waitcnt lgkmcnt(4)\n\t"
      "v_mfma_f32_32x32x16_bf16 v[56:71], v[136:139], v[152:155], v[56:71]\n\t"
      "ds_read_b128 v[136:139], %[b1] offset:8192\n\t"
      "v_mfma_f32_32x32x16_bf16 v[56:71], v[140:143], v[156:159], v[56:71]\n\t"
      "ds_read_b128 v[140:143], %[b1] offset:9216\n\t"
      "s_waitcnt lgkmcnt(4)\n\t"
      "v_mfma_f32_32x32x16_bf16 v[56:71], v[120:123], v[160:163], v[56:71]\n\t"
      "ds_read_b128 v[120:123], %[b1] offset:10240\n\t"
      "v_mul_f32 v72, v50, v72\n\t"
      "v_mul_f32 v73, v50, v73\n\t"
      "v_mul_f32 v74, v50, v74\n\t"
      "v_mul_f32 v75, v50, v75\n\t"
      "v_mul_f32 v76, v50, v76\n\t"
      "v_mfma_f32_32x32x16_bf16 v[56:71], v[124:127], v[164:167], v[56:71]\n\t"
      "ds_read_b128 v[124:127], %[b1] offset:11264\n\t"
      "v_mul_f32 v77, v50, v77\n\t"
      "v_mul_f32 v78, v50, v78\n\t"
      "v_mul_f32 v79, v50, v79\n\t"
      "v_mul_f32 v80, v50, v80\n\t"
      "v_mul_f32 v81, v50, v81\n\t"
      "v_mul_f32 v82, v50, v82\n\t"
      "s_waitcnt lgkmcnt(4)\n\t"
      "v_mfma_f32_32x32x16_bf16 v[56:71], v[128:131], v[168:171], v[56:71]\n\t"
      "ds_read_b128 v[128:131], %[b1] offset:12288\n\t"
      "v_mul_f32 v83, v50, v83\n\t"
      "v_mul_f32 v84, v50, v84\n\t"
      "v_mul_f32 v85, v50, v85\n\t"
      "v_mul_f32 v86, v50, v86\n\t"
      "v_mul_f32 v87, v50, v87\n\t"
      "v_mfma_f32_32x32x16_bf16 v[56:71], v[132:135], v[172:175], v[56:71]\n\t"
      "ds_read_b128 v[132:135], %[b1] offset:13312\n\t"
      "v_mul_f32 v36, v72, v72\n\t"
      "v_mul_f32 v37, v73, v73\n\t"
      "v_fmac_f32 v36, v74, v74\n\t"
      "v_fmac_f32 v37, v75, v75\n\t"
      "v_fmac_f32 v36, v76, v76\n\t"
      "v_fmac_f32 v37, v77, v77\n\t"
      "s_waitcnt lgkmcnt(4)\n\t"
      "v_mfma_f32_32x32x16_bf16 v[56:71], v[136:139], v[176:179], v[56:71]\n\t"
      "ds_read_b128 v[136:139], %[b1] offset:14336\n\t"
      "v_fmac_f32 v36, v78, v78\n\t"
      "v_fmac_f32 v37, v79, v79\n\t"
      "v_fmac_f32 v36, v80, v80\n\t"
      "v_fmac_f32 v37, v81, v81\n\t"
      "v_fmac_f32 v36, v82, v82\n\t"
      "v_mfma_f32_32x32x16_bf16 v[56:71], v[140:143], v[180:183], v[56:71]\n\t"
      "ds_read_b128 v[140:143], %[b1] offset:15360\n\t"
      "v_fmac_f32 v37, v83, v83\n\t"
      "v_fmac_f32 v36, v84, v84\n\t"
      "v_fmac_f32 v37, v85, v85\n\t"
      "v_fmac_f32 v36, v86, v86\n\t"
      "v_fmac_f32 v37, v87, v87\n\t"
      "v_add_f32 v36, v36, v37\n\t"
      "s_waitcnt lgkmcnt(4)\n\t"
      "v_mfma_f32_32x32x16_bf16 v[56:71], v[120:123], v[184:187], v[56:71]\n\t"
      "ds_read_b128 v[120:123], %[b1] offset:16384\n\t"
      "v_cvt_pk_bf16_f32 v72, v72, v73\n\t"
      "v_cvt_pk_bf16_f32 v73, v74, v75\n\t"
      "v_cvt_pk_bf16_f32 v74, v76, v77\n\t"
      "v_cvt_pk_bf16_f32 v75, v78, v79\n\t"
      "v_cvt_pk_bf16_f32 v76, v80, v81\n\t"
      "v_mfma_f32_32x32x16_bf16 v[56:71], v[124:127], v[188:191], v[56:71]\n\t"
      "ds_read_b128 v[124:127], %[b1] offset:17408\n\t"
      "v_cvt_pk_bf16_f32 v77, v82, v83\n\t"
      "v_cvt_pk_bf16_f32 v78, v84, v85\n\t"
      "v_cvt_pk_bf16_f32 v79, v86, v87\n\t"
      "buffer_store_dwordx4 v[72:75], %[kl], %[ws], %[ko] offen sc1\n\t"
      "buffer_store_dwordx4 v[76:79], %[kl], %[ws], %[ko] offen offset:1024 sc1\n\t"
      "v_mul_f32 v88, v88, v104\n\t"
      "s_waitcnt lgkmcnt(4)\n\t"
      "v_mfma_f32_32x32x16_bf16 v[56:71], v[128:131], v[192:195], v[56:71]\n\t"
      "ds_read_b128 v[128:131], %[b1] offset:18432\n\t"
      "v_mul_f32 v89, v89, v105\n\t"
      "v_mul_f32 v90, v90, v106\n\t"
      "v_mul_f32 v91, v91, v107\n\t"
      "v_mul_f32 v92, v92, v108\n\t"
      "v_mul_f32 v93, v93, v109\n\t"
      "v_mfma_f32_32x32x16_bf16 v[56:71], v[132:135], v[196:199], v[56:71]\n\t"
      "ds_read_b128 v[132:135], %[b1] offset:19456\n\t"
      "v_mul_f32 v94, v94, v110\n\t"
      "v_mul_f32 v95, v95, v111\n\t"
      "v_mul_f32 v96, v96, v112\n\t"
      "v_mul_f32 v97, v97, v113\n\t"
      "v_mul_f32 v98, v98, v114\n\t"
      "v_mul_f32 v99, v99, v115\n\t"
      "s_waitcnt lgkmcnt(4)\n\t"
      "v_mfma_f32_32x32x16_bf16 v[56:71], v[136:139], v[200:203], v[56:71]\n\t"
      "ds_read_b128 v[136:139], %[b1] offset:20480\n\t"
      "v_mul_f32 v100, v100, v116\n\t"
      "v_mul_f32 v101, v101, v117\n\t"
      "v_mul_f32 v102, v102, v118\n\t"
      "v_mul_f32 v103, v103, v119\n\t"
      "v_cvt_pk_bf16_f32 v88, v88, v89\n\t"
      "v_mfma_f32_32x32x16_bf16 v[56:71], v[140:143], v[204:207], v[56:71]\n\t"
      "ds_read_b128 v[140:143], %[b1] offset:21504\n\t"
      "v_cvt_pk_bf16_f32 v89, v90, v91\n\t"
      "v_cvt_pk_bf16_f32 v90, v92, v93\n\t"
      "v_cvt_pk_bf16_f32 v91, v94, v95\n\t"
      "v_cvt_pk_bf16_f32 v92, v96, v97\n\t"
      "v_cvt_pk_bf16_f32 v93, v98, v99\n\t"
      "v_cvt_pk_bf16_f32 v94, v100, v101\n\t"
      "s_waitcnt lgkmcnt(4)\n\t"
      "v_mfma_f32_32x32x16_bf16 v[56:71], v[120:123], v[208:211], v[56:71]\n\t"
      "ds_read_b128 v[120:123], %[b1] offset:22528\n\t"
      "v_cvt_pk_bf16_f32 v95, v102, v103\n\t"
      "buffer_store_dwordx4 v[88:91], %[l16], %[ws], %[ko] offen offset:2048 sc1\n\t"
      "buffer_store_dwordx4 v[92:95], %[l16], %[ws], %[ko] offen offset:3072 sc1\n\t"
      "v_mfma_f32_32x32x16_bf16 v[56:71], v[124:127], v[212:215], v[56:71]\n\t"
      "ds_read_b128 v[124:127], %[b1] offset:23552\n\t"
      "s_waitcnt lgkmcnt(4)\n\t"
      "v_mfma_f32_32x32x16_bf16 v[56:71], v[128:131], v[216:219], v[56:71]\n\t"
      "ds_read_b128 v[128:131], %[b1] offset:24576\n\t"
      "v_mfma_f32_32x32x16_bf16 v[56:71], v[132:135], v[220:223], v[56:71]\n\t"
      "ds_read_b128 v[132:135], %[b1] offset:25600\n\t"
      "s_waitcnt lgkmcnt(4)\n\t"
      "v_mfma_f32_32x32x16_bf16 v[56:71], v[136:139], v[224:227], v[56:71]\n\t"
      "ds_read_b128 v[136:139], %[b1] offset:26624\n\t"
      "v_mfma_f32_32x32x16_bf16 v[56:71], v[140:143], v[228:231], v[56:71]\n\t"
      "ds_read_b128 v[140:143], %[b1] offset:27648\n\t"
      "s_waitcnt lgkmcnt(4)\n\t"
      "v_mfma_f32_32x32x16_bf16 v[56:71], v[120:123], v[232:235], v[56:71]\n\t"
      "v_mfma_f32_32x32x16_bf16 v[56:71], v[124:127], v[236:239], v[56:71]\n\t"
      "s_waitcnt lgkmcnt(2)\n\t"
      "v_mfma_f32_32x32x16_bf16 v[56:71], v[128:131], v[240:243], v[56:71]\n\t"
      "v_mfma_f32_32x32x16_bf16 v[56:71], v[132:135], v[244:247], v[56:71]\n\t"
      "s_waitcnt lgkmcnt(0)\n\t"
      "v_mfma_f32_32x32x16_bf16 v[56:71], v[136:139], v[248:251], v[56:71]\n\t"
      "v_mfma_f32_32x32x16_bf16 v[56:71], v[140:143], v[252:255], v[56:71]\n\t"
      "v_mfma_f32_32x32x16_bf16 v[56:71], a[224:227], v[46:49], v[56:71]\n\t"
      "s_nop 11\n\t"
      "v_pk_mul_f32 v[56:57], v[56:57], v[52:53] op_sel_hi:[1,0]\n\t"
      "v_pk_mul_f32 v[58:59], v[58:59], v[52:53] op_sel_hi:[1,0]\n\t"
      "v_pk_mul_f32 v[60:61], v[60:61], v[52:53] op_sel_hi:[1,0]\n\t"
      "v_pk_mul_f32 v[62:63], v[62:63], v[52:53] op_sel_hi:[1,0]\n\t"
      "v_pk_mul_f32 v[64:65], v[64:65], v[52:53] op_sel_hi:[1,0]\n\t"
      "v_pk_mul_f32 v[66:67], v[66:67], v[52:53] op_sel_hi:[1,0]\n\t"
      "v_pk_mul_f32 v[68:69], v[68:69], v[52:53] op_sel_hi:[1,0]\n\t"
      "v_pk_mul_f32 v[70:71], v[70:71], v[52:53] op_sel_hi:[1,0]\n\t"
      "v_pk_mul_f32 v[32:33], v[56:57], v[56:57]\n\t"
      "v_pk_mul_f32 v[34:35], v[58:59], v[58:59]\n\t"
      "v_pk_fma_f32 v[32:33], v[60:61], v[60:61], v[32:33]\n\t"
      "v_pk_fma_f32 v[34:35], v[62:63], v[62:63], v[34:35]\n\t"
      "v_pk_fma_f32 v[32:33], v[64:65], v[64:65], v[32:33]\n\t"
      "v_pk_fma_f32 v[34:35], v[66:67], v[66:67], v[34:35]\n\t"
      "v_pk_fma_f32 v[32:33], v[68:69], v[68:69], v[32:33]\n\t"
      "v_pk_fma_f32 v[34:35], v[70:71], v[70:71], v[34:35]\n\t"
      "v_pk_add_f32 v[32:33], v[32:33], v[34:35]\n\t"
      "v_add_f32 v32, v32, v33\n\t"
      "v_cvt_pk_bf16_f32 v56, v56, v57\n\t"
      "v_cvt_pk_bf16_f32 v57, v58, v59\n\t"
      "v_cvt_pk_bf16_f32 v58, v60, v61\n\t"
      "v_cvt_pk_bf16_f32 v59, v62, v63\n\t"
      "v_cvt_pk_bf16_f32 v60, v64, v65\n\t"
      "v_cvt_pk_bf16_f32 v61, v66, v67\n\t"
      "v_cvt_pk_bf16_f32 v62, v68, v69\n\t"
      "v_cvt_pk_bf16_f32 v63, v70, v71\n\t"
      "buffer_store_dwordx4 v[56:59], %[l16], %[ws], %[qo] offen sc1\n\t"
      "buffer_store_dwordx4 v[60:63], %[l16], %[ws], %[qo] offen offset:1024 sc1\n\t"
      "v_mov_b32 v37, v32\n\t"
      "v_mov_b32 v38, v36\n\t"
      "v_mov_b32 v39, v37\n\t"
      "s_nop 0\n\t"
      "v_permlane32_swap_b32 v36, v38\n\t"
      "v_permlane32_swap_b32 v37, v39\n\t"
      "v_add_f32 v36, v36, v38\n\t"
      "v_add_f32 v37, v37, v39\n\t"
      "s_nop 0\n\t"
      "v_max_f32_dpp v36, v36, v36 quad_perm:[1,0,3,2] row_mask:0xf bank_mask:0xf\n\t"
      "v_max_f32_dpp v37, v37, v37 quad_perm:[1,0,3,2] row_mask:0xf bank_mask:0xf\n\t"
      "s_nop 0\n\t"
      "v_max_f32_dpp v36, v36, v36 quad_perm:[2,3,0,1] row_mask:0xf bank_mask:0xf\n\t"
      "v_max_f32_dpp v37, v37, v37 quad_perm:[2,3,0,1] row_mask:0xf bank_mask:0xf\n\t"
      "s_nop 0\n\t"
      "v_max_f32_dpp v36, v36, v36 row_half_mirror row_mask:0xf bank_mask:0xf\n\t"
      "v_max_f32_dpp v37, v37, v37 row_half_mirror row_mask:0xf bank_mask:0xf\n\t"
      "s_nop 0\n\t"
      "v_max_f32_dpp v36, v36, v36 row_mirror row_mask:0xf bank_mask:0xf\n\t"
      "v_max_f32_dpp v37, v37, v37 row_mirror row_mask:0xf bank_mask:0xf\n\t"
      "v_mov_b32 v38, v36\n\t"
      "v_mov_b32 v39, v37\n\t"
      "s_nop 0\n\t"
      "v_permlane16_swap_b32 v36, v38\n\t"
      "v_permlane16_swap_b32 v37, v39\n\t"
      "v_max_f32 v36, v36, v38\n\t"
      "v_max_f32 v37, v37, v39\n\t"
      "ds_write_b64 %[kmw], v[36:37]\n\t"
      : [so] "=&s"(so), [n] "=&s"(n), [f] "=&s"(f)
      : [l16] "v"(c.lane16), [ws] "s"(c.ws), [b0] "v"(b0), [b1] "v"(b1), [rtw] "v"(rtw), [rtr] "v"(rtr), [kl] "v"(kl), [kmw] "v"(kmw), [ko] "s"(ko), [qo] "s"(qo), [stv] "v"(stv), [sto] "s"(sto), [sto2] "s"(sto2), [so0] "s"(so0)
      : "v32", "v33", "v34", "v35", "v36", "v37", "v38", "v39", "v40", "v41", "v42", "v43", "v44", "v45", "v46", "v47", "v48", "v49", "v50", "v51", "v52", "v53", "v54", "v55", "v56", "v57", "v58", "v59", "v60", "v61", "v62", "v63", "v64", "v65", "v66", "v67", "v68", "v69", "v70", "v71", "v72", "v73", "v74", "v75", "v76", "v77", "v78", "v79", "v80", "v81", "v82", "v83", "v84", "v85", "v86", "v87", "v88", "v89", "v90", "v91", "v92", "v93", "v94", "v95", "v96", "v97", "v98", "v99", "v100", "v101", "v102", "v103", "v104", "v105", "v106", "v107", "v108", "v109", "v110", "v111", "v112", "v113", "v114", "v115", "v116", "v117", "v118", "v119", "v120", "v121", "v122", "v123", "v124", "v125", "v126", "v127", "v128", "v129", "v130", "v131", "v132", "v133", "v134", "v135", "v136", "v137", "v138", "v139", "v140", "v141", "v142", "v143", "v144", "v145", "v146", "v147", "v148", "v149", "v150", "v151", "v152", "v153", "v154", "v155", "v156", "v157", "v158", "v159", "v160", "v161", "v162", "v163", "v164", "v165", "v166", "v167", "v168", "v169", "v170", "v171", "v172", "v173", "v174", "v175", "v176", "v177", "v178", "v179", "v180", "v181", "v182", "v183", "v184", "v185", "v186", "v187", "v188", "v189", "v190", "v191", "v192", "v193", "v194", "v195", "v196", "v197", "v198", "v199", "v200", "v201", "v202", "v203", "v204", "v205", "v206", "v207", "v208", "v209", "v210", "v211", "v212", "v213", "v214", "v215", "v216", "v217", "v218", "v219", "v220", "v221", "v222", "v223", "v224", "v225", "v226", "v227", "v228", "v229", "v230", "v231", "v232", "v233", "v234", "v235", "v236", "v237", "v238", "v239", "v240", "v241", "v242", "v243", "v244", "v245", "v246", "v247", "v248", "v249", "v250", "v251", "v252", "v253", "v254", "v255", "vcc", "memory", "scc", ACC_FILE);
      }
    asm volatile(
      "v_mov_b32 v160, 0\n\t"
      "v_mov_b32 v161, 0\n\t"
      "v_mov_b32 v162, 0\n\t"
      "v_mov_b32 v163, 0\n\t"
      "v_mov_b32 v164, 0\n\t"
      "v_mov_b32 v165, 0\n\t"
      "v_mov_b32 v166, 0\n\t"
      "v_mov_b32 v167, 0\n\t"
      "v_mov_b32 v168, 0\n\t"
      "v_mov_b32 v169, 0\n\t"
      "v_mov_b32 v170, 0\n\t"
      "v_mov_b32 v171, 0\n\t"
      "v_mov_b32 v172, 0\n\t"
      "v_mov_b32 v173, 0\n\t"
      "v_mov_b32 v174, 0\n\t"
      "v_mov_b32 v175, 0\n\t"
      "v_mov_b32 v176, 0\n\t"
      "v_mov_b32 v177, 0\n\t"
      "v_mov_b32 v178, 0\n\t"
      "v_mov_b32 v179, 0\n\t"
      "v_mov_b32 v180, 0\n\t"
      "v_mov_b32 v181, 0\n\t"
      "v_mov_b32 v182, 0\n\t"
      "v_mov_b32 v183, 0\n\t"
      "v_mov_b32 v184, 0\n\t"
      "v_mov_b32 v185, 0\n\t"
      "v_mov_b32 v186, 0\n\t"
      "v_mov_b32 v187, 0\n\t"
      "v_mov_b32 v188, 0\n\t"
      "v_mov_b32 v189, 0\n\t"
      "v_mov_b32 v190, 0\n\t"
      "v_mov_b32 v191, 0\n\t"
      "v_mov_b32 v192, 0\n\t"
      "v_mov_b32 v193, 0\n\t"
      "v_mov_b32 v194, 0\n\t"
      "v_mov_b32 v195, 0\n\t"
      "v_mov_b32 v196, 0\n\t"
      "v_mov_b32 v197, 0\n\t"
      "v_mov_b32 v198, 0\n\t"
      "v_mov_b32 v199, 0\n\t"
      "v_mov_b32 v200, 0\n\t"
      "v_mov_b32 v201, 0\n\t"
      "v_mov_b32 v202, 0\n\t"
      "v_mov_b32 v203, 0\n\t"
      "v_mov_b32 v204, 0\n\t"
      "v_mov_b32 v205, 0\n\t"
      "v_mov_b32 v206, 0\n\t"
      "v_mov_b32 v207, 0\n\t"
      "v_mov_b32 v208, 0\n\t"
      "v_mov_b32 v209, 0\n\t"
      "v_mov_b32 v210, 0\n\t"
      "v_mov_b32 v211, 0\n\t"
      "v_mov_b32 v212, 0\n\t"
      "v_mov_b32 v213, 0\n\t"
      "v_mov_b32 v214, 0\n\t"
      "v_mov_b32 v215, 0\n\t"
      "v_mov_b32 v216, 0\n\t"
      "v_mov_b32 v217, 0\n\t"
      "v_mov_b32 v218, 0\n\t"
      "v_mov_b32 v219, 0\n\t"
      "v_mov_b32 v220, 0\n\t"
      "v_mov_b32 v221, 0\n\t"
      "v_mov_b32 v222, 0\n\t"
      "v_mov_b32 v223, 0\n\t"
      : "={v[160:191]}"(zo0), "={v[192:223]}"(zo1)
      :
      : "memory", ACC_FILE);
      publish_kv(c, 5, c.c, c.r, L_MISC);
    } else asm volatile("" : "={v[160:191]}"(zo0), "={v[192:223]}"(zo1));
  }
}

extern "C" void kernel_launch(void* const* d_in, const int* in_sizes, int n_in, void* d_out, int out_size, void* d_ws, size_t ws_size,
                              hipStream_t stream) {
  Params p;
  p.x = d_in[0]; p.wqkv = d_in[1]; p.wo = d_in[2]; p.w1 = d_in[4]; p.b1 = d_in[5]; p.w2 = d_in[6]; p.b2 = d_in[7];
  p.out = d_out; p.ws = (unsigned char*)d_ws;
  (void)hipFuncSetAttribute((const void*)fused_kernel, hipFuncAttributeMaxDynamicSharedMemorySize, L_TOTAL);
  hipLaunchKernelGGL(fused_kernel, dim3(256), dim3(256), L_TOTAL, stream, p);
}
```
